```python
import torch

def _softmax(x, axis=-1):
    return torch.softmax(x, dim=axis)
def _gelu(x):
    return torch.nn.functional.gelu(x, approximate="tanh")
def _silu(x):
    return torch.nn.functional.silu(x)
def _sigmoid(x):
    return torch.sigmoid(x)
def _layernorm(x, axis=-1, eps=1e-5):
    m = x.mean(dim=axis, keepdim=True)
    v = x.var(dim=axis, keepdim=True, unbiased=False)
    return (x - m) / torch.sqrt(v + eps)
def _scatter_add(tgt, idx, src, axis=0):
    return tgt.clone().index_add_(axis, idx, src)
def _segment_sum(src, seg, n):
    out = torch.zeros((n,) + src.shape[1:], dtype=src.dtype)
    return out.index_add_(0, seg, src)
def _pad(x, pw):
    flat = tuple(v for pair in reversed(pw) for v in pair)
    return torch.nn.functional.pad(x, flat)

kernel_name = 'recurrent_scan__sliding_local_dilated__dense_activation_squared_relu__prompt'

def setup_inputs():

    _w = (lambda a: (lambda t: t.to(torch.bfloat16) if t.is_floating_point() else t)(torch.as_tensor(a)))
    def _A_setup():
        B, S, D = 1, 2048, 512
        return {
            "x": _w((lambda *s: torch.randn(*s, dtype=torch.bfloat16) * 0.05)(B, S, D)),
            "wqkv": _w((lambda *s: torch.randn(*s, dtype=torch.bfloat16) * 0.05)(D, 3 * D)),
            "wo": _w((lambda *s: torch.randn(*s, dtype=torch.bfloat16) * 0.05)(D, D)),
        }
    _A_in = _A_setup()

    def _B_setup():
        B, S, D, F = 1, 2048, 512, 1792
        return {
            "x": _w((lambda *s: torch.randn(*s, dtype=torch.bfloat16) * 0.05)(B, S, D)),
            "w1": _w((lambda *s: torch.randn(*s, dtype=torch.bfloat16) * 0.05)(D, F)),
            "b1": _w((lambda *s: torch.randn(*s, dtype=torch.bfloat16) * 0.4)(F)),
            "w2": _w((lambda *s: torch.randn(*s, dtype=torch.bfloat16) * 0.05)(F, D)),
            "b2": _w((lambda *s: torch.randn(*s, dtype=torch.bfloat16) * 0.8)(D)),
        }
    _B_in = _B_setup()

    return {
        "A_x": _A_in["x"],
        "A_wqkv": _A_in["wqkv"],
        "A_wo": _A_in["wo"],
        "B_x": _B_in["x"],
        "B_w1": _B_in["w1"],
        "B_b1": _B_in["b1"],
        "B_w2": _B_in["w2"],
        "B_b2": _B_in["b2"],
    }

def reference(A_x, A_wqkv, A_wo, B_x, B_w1, B_b1, B_w2, B_b2):
    def _A_ref(x, wqkv, wo):
        B, S, D = x.shape
        H = 32; Dh = D // H
        W, dil = 8, 2
        qkv = (lambda x, *d: x.permute(*d))((x @ wqkv).reshape(B, S, 3, H, Dh), 2, 0, 3, 1, 4)
        q, k, v = qkv[0], qkv[1], qkv[2]
        scale = Dh ** -0.5
        pos = torch.arange(S)
        off = torch.arange(W) * dil
        idx = pos[:, None] - off[None, :]
        mask = idx >= 0
        idx_c = torch.where(mask, idx, 0).reshape(-1)
        kg = k[:, :, idx_c, :].reshape(B, H, S, W, Dh)
        vg = v[:, :, idx_c, :].reshape(B, H, S, W, Dh)
        logits = torch.einsum("bhsd,bhswd->bhsw", q, kg) * scale
        logits = torch.where(mask[None, None, :, :], logits, -1e9)
        attn = _softmax(logits, axis=-1)
        o = torch.einsum("bhsw,bhswd->bhsd", attn, vg)
        return (lambda x, *d: x.permute(*d))(o, 0, 2, 1, 3).reshape(B, S, D) @ wo

    def _B_ref(x, w1, b1, w2, b2):
        h = torch.einsum("bsd,df->bsf", x, w1) + b1
        zero = torch.zeros_like(h)
        r = torch.where(h > zero, h, zero)
        h = r * r
        out = torch.einsum("bsf,fd->bsd", h, w2) + b2
        return out

    x = A_x
    xn = _layernorm(x, axis=-1)
    s = _A_ref(xn, A_wqkv, A_wo)
    g = _silu(_B_ref(xn, B_w1, B_b1, B_w2, B_b2))
    return x + s * g
```

```cpp
#include <hip/hip_runtime.h>
#include <hip/hip_bf16.h>
#include <stdint.h>

static constexpr int S = 2048, D = 512, F = 1792, H = 32, DH = 16;
typedef unsigned short u16;
__device__ __forceinline__ float bf2f(u16 v) { return __uint_as_float(((unsigned)v) << 16); }
__device__ __forceinline__ u16 f2bf(float f) { unsigned u = __float_as_uint(f); u += 0x7fffu + ((u >> 16) & 1u); return (u16)(u >> 16); }

__global__ void k_ln(const u16* x, float* xn) {
  int r = blockIdx.x; int t = threadIdx.x;
  float v[8]; float s = 0.f;
  for (int j = 0; j < 8; ++j) { v[j] = bf2f(x[r * D + t * 8 + j]); s += v[j]; }
  for (int o = 32; o; o >>= 1) s += __shfl_xor(s, o);
  float m = s / D; float q = 0.f;
  for (int j = 0; j < 8; ++j) { float d = v[j] - m; q += d * d; }
  for (int o = 32; o; o >>= 1) q += __shfl_xor(q, o);
  float mb = bf2f(f2bf(m)); float vb = bf2f(f2bf(q / D)); float sd = bf2f(f2bf(sqrtf(bf2f(f2bf(vb + 1e-5f)))));
  float rs = 1.0f / sd;
  for (int j = 0; j < 8; ++j) xn[r * D + t * 8 + j] = (v[j] - mb) * rs;
}
__global__ void k_gemm(const float* A, const u16* B, float* C, int M, int N, int K) {
  int n = blockIdx.x * 256 + threadIdx.x; int m0 = blockIdx.y * 8;
  if (n >= N) return;
  float acc[8] = {0,0,0,0,0,0,0,0};
  for (int k = 0; k < K; ++k) { float b = bf2f(B[(size_t)k * N + n]);
    #pragma unroll
    for (int i = 0; i < 8; ++i) acc[i] += A[(size_t)(m0 + i) * K + k] * b; }
  #pragma unroll
  for (int i = 0; i < 8; ++i) C[(size_t)(m0 + i) * N + n] = acc[i];
}
__global__ void k_attn(const float* qkv, float* o) {
  int idx = blockIdx.x * 256 + threadIdx.x;
  int s = idx / H, h = idx % H;
  const float* q = qkv + (size_t)s * 3 * D + h * DH;
  float l[8]; float mx = -1e30f;
  #pragma unroll
  for (int w = 0; w < 8; ++w) { int p = s - 2 * w; float a;
    if (p >= 0) { const float* k = qkv + (size_t)p * 3 * D + D + h * DH; a = 0.f; for (int d = 0; d < DH; ++d) a += q[d] * k[d]; a *= 0.25f; } else a = -1e9f;
    l[w] = a; mx = fmaxf(mx, a); }
  float sum = 0.f;
  #pragma unroll
  for (int w = 0; w < 8; ++w) { l[w] = __expf(l[w] - mx); sum += l[w]; }
  for (int d = 0; d < DH; ++d) { float a = 0.f;
    #pragma unroll
    for (int w = 0; w < 8; ++w) { int p = s - 2 * w; if (p < 0) p = 0; a += l[w] * qkv[(size_t)p * 3 * D + 2 * D + h * DH + d]; }
    o[(size_t)s * D + h * DH + d] = a / sum; }
}
__global__ void k_act(float* h, const u16* b1) {
  int i = blockIdx.x * 256 + threadIdx.x; float v = h[i] + bf2f(b1[i % F]); v = v > 0.f ? v : 0.f; h[i] = v * v;
}
__global__ void k_fin(const u16* x, const float* s, const float* y, const u16* b2, u16* out) {
  int i = blockIdx.x * 256 + threadIdx.x; float g = y[i] + bf2f(b2[i % D]); g = g / (1.f + __expf(-g));
  out[i] = f2bf(bf2f(x[i]) + s[i] * g);
}

extern "C" void kernel_launch(void* const* d_in, const int* in_sizes, int n_in,
                              void* d_out, int out_size, void* d_ws, size_t ws_size,
                              hipStream_t stream) {
  const u16* x = (const u16*)d_in[0]; const u16* wqkv = (const u16*)d_in[1]; const u16* wo = (const u16*)d_in[2];
  const u16* w1 = (const u16*)d_in[4]; const u16* b1 = (const u16*)d_in[5]; const u16* w2 = (const u16*)d_in[6]; const u16* b2 = (const u16*)d_in[7];
  float* ws = (float*)d_ws;
  float* xn = ws; float* qkv = xn + S * D; float* o = qkv + S * 3 * D; float* s = o + S * D; float* h = s + S * D; float* y = h + S * F;
  k_ln<<<S, 64, 0, stream>>>(x, xn);
  k_gemm<<<dim3(3 * D / 256, S / 8), 256, 0, stream>>>(xn, wqkv, qkv, S, 3 * D, D);
  k_attn<<<S * H / 256, 256, 0, stream>>>(qkv, o);
  k_gemm<<<dim3(D / 256, S / 8), 256, 0, stream>>>(o, wo, s, S, D, D);
  k_gemm<<<dim3(F / 256, S / 8), 256, 0, stream>>>(xn, w1, h, S, F, D);
  k_act<<<S * F / 256, 256, 0, stream>>>(h, b1);
  k_gemm<<<dim3(D / 256, S / 8), 256, 0, stream>>>(h, w2, y, S, D, F);
  k_fin<<<S * D / 256, 256, 0, stream>>>(x, s, y, b2, (u16*)d_out);
}
```

```cpp
#include <hip/hip_runtime.h>
#include <hip/hip_bf16.h>

__global__ __launch_bounds__(512, 2) void fused_block_kernel(const void* x, const void* wqkv, const void* wo, const void* w1, const void* b1,
                                                             const void* w2, const void* b2, void* out, void* ws) {
  __shared__ __attribute__((aligned(16))) char smem[163840];
  unsigned tid = threadIdx.x;
  asm volatile(
    "KBEGIN:\n"
    "s_load_dwordx16 s[8:23], %1, 0x0\n"
    "s_load_dwordx2 s[24:25], %1, 0x40\n"
    "s_mov_b32 s71, %2\n"
    "v_lshrrev_b32 v1, 6, v0\n"
    "v_and_b32 v255, 63, v0\n"
    "v_readfirstlane_b32 s64, v1\n"
    "s_and_b32 s72, s71, 7\n"
    "s_lshr_b32 s73, s71, 3\n"
    "s_and_b32 s74, s72, 1\n"
    "s_lshl_b32 s74, s74, 3\n"
    "s_and_b32 s65, s73, 7\n"
    "s_add_u32 s65, s65, s74\n"
    "s_lshr_b32 s67, s73, 4\n"
    "s_bfe_u32 s66, s73, 0x10003\n"
    "s_and_b32 s74, s72, 6\n"
    "s_add_u32 s66, s66, s74\n"
    "s_and_b32 s68, s73, 3\n"
    "s_lshl_b32 s74, s72, 2\n"
    "s_add_u32 s68, s68, s74\n"
    "s_lshr_b32 s69, s73, 2\n"
    "s_and_b32 s70, s64, 3\n"
    "s_cmp_ge_u32 s64, 4\n"
    "s_cbranch_scc1 LBL_L\n"
    "s_cmp_lg_u32 s64, 0\n"
    "s_cbranch_scc1 NOCODE\n"
    "s_getpc_b64 s[32:33]\n"
    "PCREF:\n"
    "s_sub_u32 s32, s32, PCREF-KBEGIN\n"
    "s_subb_u32 s33, s33, 0\n"
    "s_and_b32 s33, s33, 0xffff\n"
    "s_mov_b32 s34, KEND-KBEGIN\n"
    "s_mov_b32 s35, 0x20000\n"
    "v_min_u32 v1, 12, v255\n"
    "v_lshlrev_b32 v1, 12, v1\n"
    "s_lshl_b32 s74, s73, 7\n"
    "buffer_load_dword v251, v1, s[32:35], s74 offen\n"
    "NOCODE:\n"
    "s_cmp_eq_u32 s67, 0\n"
    "s_cbranch_scc1 LBL_C_att\n"
    "s_branch LBL_C_ffn\n"
    "LBL_L:\n"
    "s_cmp_lg_u32 s67, 0\n"
    "s_cbranch_scc1 LBL_L_ffn\n"
    "LBL_L_att:\n"
    "v_lshrrev_b32 v134, 3, v255\n"
    "v_and_b32 v135, 7, v255\n"
    "v_lshrrev_b32 v136, 4, v255\n"
    "v_xor_b32 v137, v135, v136\n"
    "v_lshlrev_b32 v96, 11, v134\n"
    "v_lshl_add_u32 v96, v137, 4, v96\n"
    "v_xor_b32 v97, 64, v96\n"
    "v_add_u32 v97, 15360, v97\n"
    "v_add_u32 v98, 30720, v96\n"
    "v_add_u32 v99, 30720, v97\n"
    "v_bfe_u32 v137, v255, 4, 1\n"
    "v_lshlrev_b32 v137, 2, v137\n"
    "v_xor_b32 v137, v137, v135\n"
    "v_mul_u32_u24 v100, 3072, v134\n"
    "v_lshl_add_u32 v100, v137, 4, v100\n"
    "v_xor_b32 v101, 32, v100\n"
    "v_add_u32 v101, 23552, v101\n"
    "s_lshl_b32 s72, s65, 7\n"
    "s_lshr_b32 s73, s70, 1\n"
    "s_lshl_b32 s73, s73, 6\n"
    "s_and_b32 s74, s70, 1\n"
    "s_add_u32 s72, s72, s73\n"
    "s_add_u32 s72, s72, s74\n"
    "s_lshl_b32 s76, s72, 10\n"
    "s_mul_i32 s77, s70, 49152\n"
    "s_lshl_b32 s75, s66, 7\n"
    "s_add_u32 s77, s77, s75\n"
    "s_lshl_b32 s81, s70, 12\n"
    "s_lshl_b32 s82, s70, 11\n"
    "s_lshl_b32 s83, s70, 9\n"
    "s_lshl_b32 s72, s70, 1\n"
    "v_add_u32 v138, s72, v136\n"
    "v_xor_b32 v138, v138, v135\n"
    "v_and_b32 v138, 7, v138\n"
    "v_lshlrev_b32 v106, 11, v134\n"
    "v_lshl_add_u32 v106, v138, 4, v106\n"
    "s_lshl_b32 s72, s65, 7\n"
    "s_max_u32 s72, s72, 16\n"
    "s_sub_u32 s72, s72, 16\n"
    "s_and_b32 s73, s70, 1\n"
    "s_lshl_b32 s73, s73, 3\n"
    "s_add_u32 s72, s72, s73\n"
    "s_lshr_b32 s73, s70, 1\n"
    "s_add_u32 s72, s72, s73\n"
    "s_lshl_b32 s80, s72, 10\n"
    "s_bfe_u32 s72, s71, 0x20001\n"
    "s_add_u32 s90, s72, s65\n"
    "s_add_u32 s72, s90, 0\n"
    "s_and_b32 s72, s72, 3\n"
    "s_lshl_b32 s73, s72, 8\n"
    "s_add_u32 s52, s76, s73\n"
    "s_add_u32 s60, s80, s73\n"
    "s_mul_i32 s73, s72, 393216\n"
    "s_add_u32 s56, s77, s73\n"
    "s_mov_b32 s30, 0x200000\n"
    "s_mov_b32 s31, 0x20000\n"
    "s_mov_b32 s34, 0x180000\n"
    "s_mov_b32 s35, 0x20000\n"
    "s_setprio 2\n"
    "s_waitcnt lgkmcnt(0)\n"
    "s_mov_b32 s28, s8\n"
    "s_and_b32 s29, s9, 0xffff\n"
    "s_mov_b32 s32, s10\n"
    "s_and_b32 s33, s11, 0xffff\n"
    "s_add_u32 m0, s81, 0x0\n"
    "s_add_u32 s84, s52, 0x0\n"
    "buffer_load_dwordx4 v96, s[28:31], s84 offen offset:0 lds\n"
    "buffer_load_dwordx4 v97, s[28:31], s84 offen offset:1024 lds\n"
    "buffer_load_dwordx4 v98, s[28:31], s84 offen offset:2048 lds\n"
    "buffer_load_dwordx4 v99, s[28:31], s84 offen offset:3072 lds\n"
    "s_add_u32 m0, s83, 0x20000\n"
    "s_add_u32 s84, s60, 0x0\n"
    "s_mov_b32 exec_hi, 0\n"
    "buffer_load_dwordx4 v106, s[28:31], s84 offen offset:0 lds\n"
    "s_mov_b32 exec_hi, -1\n"
    "s_add_u32 m0, s82, 0x4000\n"
    "s_add_u32 s84, s56, 0x400\n"
    "buffer_load_dwordx4 v100, s[32:35], s84 offen offset:0 lds\n"
    "buffer_load_dwordx4 v101, s[32:35], s84 offen offset:1024 lds\n"
    "s_add_u32 m0, s81, 0x8000\n"
    "s_add_u32 s84, s52, 0x80\n"
    "buffer_load_dwordx4 v96, s[28:31], s84 offen offset:0 lds\n"
    "buffer_load_dwordx4 v97, s[28:31], s84 offen offset:1024 lds\n"
    "buffer_load_dwordx4 v98, s[28:31], s84 offen offset:2048 lds\n"
    "buffer_load_dwordx4 v99, s[28:31], s84 offen offset:3072 lds\n"
    "s_branch REST_L_att\n"
    "LBL_L_ffn:\n"
    "v_lshrrev_b32 v134, 3, v255\n"
    "v_and_b32 v135, 7, v255\n"
    "v_lshrrev_b32 v136, 4, v255\n"
    "v_xor_b32 v137, v135, v136\n"
    "v_lshlrev_b32 v96, 11, v134\n"
    "v_lshl_add_u32 v96, v137, 4, v96\n"
    "v_xor_b32 v97, 64, v96\n"
    "v_add_u32 v97, 15360, v97\n"
    "v_add_u32 v98, 30720, v96\n"
    "v_add_u32 v99, 30720, v97\n"
    "v_bfe_u32 v137, v255, 4, 1\n"
    "v_lshlrev_b32 v137, 2, v137\n"
    "v_xor_b32 v137, v137, v135\n"
    "v_mul_u32_u24 v100, 3584, v134\n"
    "v_lshl_add_u32 v100, v137, 4, v100\n"
    "v_xor_b32 v101, 32, v100\n"
    "v_add_u32 v101, 27648, v101\n"
    "s_lshl_b32 s72, s65, 7\n"
    "s_lshr_b32 s73, s70, 1\n"
    "s_lshl_b32 s73, s73, 6\n"
    "s_and_b32 s74, s70, 1\n"
    "s_add_u32 s72, s72, s73\n"
    "s_add_u32 s72, s72, s74\n"
    "s_lshl_b32 s76, s72, 10\n"
    "s_mul_i32 s77, s70, 57344\n"
    "s_mul_i32 s72, s66, 448\n"
    "s_and_b32 s73, s66, 1\n"
    "s_lshl_b32 s74, s73, 6\n"
    "s_add_u32 s75, s72, s74\n"
    "s_add_u32 s77, s77, s75\n"
    "s_lshl_b32 s81, s70, 12\n"
    "s_lshl_b32 s82, s70, 11\n"
    "s_lshl_b32 s83, s70, 9\n"
    "s_bfe_u32 s72, s71, 0x20001\n"
    "s_add_u32 s90, s72, s65\n"
    "s_add_u32 s72, s90, 0\n"
    "s_and_b32 s72, s72, 3\n"
    "s_lshl_b32 s73, s72, 8\n"
    "s_add_u32 s52, s76, s73\n"
    "s_mul_i32 s73, s72, 458752\n"
    "s_add_u32 s56, s77, s73\n"
    "s_mov_b32 s30, 0x200000\n"
    "s_mov_b32 s31, 0x20000\n"
    "s_mov_b32 s34, 0x1c0000\n"
    "s_mov_b32 s35, 0x20000\n"
    "s_setprio 2\n"
    "s_waitcnt lgkmcnt(0)\n"
    "s_mov_b32 s28, s8\n"
    "s_and_b32 s29, s9, 0xffff\n"
    "s_mov_b32 s32, s14\n"
    "s_and_b32 s33, s15, 0xffff\n"
    "s_add_u32 m0, s81, 0x0\n"
    "s_add_u32 s84, s52, 0x0\n"
    "buffer_load_dwordx4 v96, s[28:31], s84 offen offset:0 lds\n"
    "buffer_load_dwordx4 v97, s[28:31], s84 offen offset:1024 lds\n"
    "buffer_load_dwordx4 v98, s[28:31], s84 offen offset:2048 lds\n"
    "buffer_load_dwordx4 v99, s[28:31], s84 offen offset:3072 lds\n"
    "s_add_u32 m0, s82, 0x4000\n"
    "s_add_u32 s84, s56, 0x0\n"
    "buffer_load_dwordx4 v100, s[32:35], s84 offen offset:0 lds\n"
    "buffer_load_dwordx4 v101, s[32:35], s84 offen offset:1024 lds\n"
    "s_add_u32 m0, s81, 0x8000\n"
    "s_add_u32 s84, s52, 0x80\n"
    "buffer_load_dwordx4 v96, s[28:31], s84 offen offset:0 lds\n"
    "buffer_load_dwordx4 v97, s[28:31], s84 offen offset:1024 lds\n"
    "buffer_load_dwordx4 v98, s[28:31], s84 offen offset:2048 lds\n"
    "buffer_load_dwordx4 v99, s[28:31], s84 offen offset:3072 lds\n"
    "s_add_u32 s26, s24, 0x1000000\n"
    "s_addc_u32 s27, s25, 0\n"
    "s_mov_b32 s48, s26\n"
    "s_and_b32 s49, s27, 0xffff\n"
    "s_mov_b32 s50, 0x40000\n"
    "s_mov_b32 s51, 0x20000\n"
    "s_lshl_b32 s72, s65, 14\n"
    "s_cmp_eq_u32 s70, 1\n"
    "s_cselect_b32 s73, 16, 0\n"
    "v_lshl_add_u32 v142, v255, 10, s72\n"
    "v_mov_b32 v166, 0x40000000\n"
    "v_cmp_gt_u32 vcc, s73, v255\n"
    "s_nop 1\n"
    "v_cndmask_b32 v166, v166, v142, vcc\n"
    "buffer_load_dwordx2 v[164:165], v166, s[48:51], 0 offen offset:640 sc1\n"
    "s_lshl_b32 s73, s66, 3\n"
    "v_add_u32 v166, s73, v166\n"
    "s_lshl_b32 s72, s68, 3\n"
    "s_add_u32 s72, s72, s69\n"
    "s_lshl_b32 s72, s72, 10\n"
    "s_add_u32 s98, s26, s72\n"
    "s_addc_u32 s99, s27, 0\n"
    "s_load_dwordx2 s[96:97], s[98:99], 0x280\n"
    "s_cmp_eq_u32 s70, 3\n"
    "s_cselect_b32 s72, s72, 0x40000000\n"
    "v_min_u32 v142, 4, v255\n"
    "v_lshl_add_u32 v142, v142, 7, s72\n"
    "buffer_load_dword v163, v142, s[48:51], 0 offen sc1\n"
    "s_mov_b32 s36, s18\n"
    "s_and_b32 s37, s19, 0xffff\n"
    "s_mov_b32 s38, 0x1c0000\n"
    "s_mov_b32 s39, 0x20000\n"
    "s_mov_b32 s40, s12\n"
    "s_and_b32 s41, s13, 0xffff\n"
    "s_mov_b32 s42, 0x80000\n"
    "s_mov_b32 s43, 0x20000\n"
    "s_mov_b32 s8, s16\n"
    "s_and_b32 s9, s17, 0xffff\n"
    "s_mov_b32 s10, 0xe04\n"
    "s_mov_b32 s11, 0x20000\n"
    "s_mul_i32 s72, s66, 448\n"
    "s_bitcmp1_b32 s66, 0\n"
    "s_cselect_b32 s73, 0, 384\n"
    "s_add_u32 s78, s72, s73\n"
    "v_and_b32 v142, 15, v255\n"
    "v_lshlrev_b32 v142, 1, v142\n"
    "s_and_b32 s73, s70, 1\n"
    "s_lshl_b32 s73, s73, 5\n"
    "s_add_u32 s72, s78, s73\n"
    "buffer_load_ushort v131, v142, s[8:11], s72 offen\n"
    "s_lshl_b32 s73, s70, 5\n"
    "s_add_u32 s72, s75, s73\n"
    "buffer_load_ushort v128, v142, s[8:11], s72 offen offset:0\n"
    "buffer_load_ushort v129, v142, s[8:11], s72 offen offset:128\n"
    "buffer_load_ushort v130, v142, s[8:11], s72 offen offset:256\n"
    "s_add_u32 s72, s90, 1\n"
    "s_and_b32 s72, s72, 3\n"
    "s_lshl_b32 s73, s72, 8\n"
    "s_add_u32 s53, s76, s73\n"
    "s_mul_i32 s73, s72, 458752\n"
    "s_add_u32 s57, s77, s73\n"
    "s_add_u32 s72, s90, 2\n"
    "s_and_b32 s72, s72, 3\n"
    "s_lshl_b32 s73, s72, 8\n"
    "s_add_u32 s54, s76, s73\n"
    "s_mul_i32 s73, s72, 458752\n"
    "s_add_u32 s58, s77, s73\n"
    "s_add_u32 s72, s90, 3\n"
    "s_and_b32 s72, s72, 3\n"
    "s_lshl_b32 s73, s72, 8\n"
    "s_add_u32 s55, s76, s73\n"
    "s_mul_i32 s73, s72, 458752\n"
    "s_add_u32 s59, s77, s73\n"
    "v_bfe_u32 v138, v255, 2, 2\n"
    "v_lshl_add_u32 v138, v136, 3, v138\n"
    "v_bfe_u32 v139, v255, 1, 1\n"
    "v_bfe_u32 v140, v255, 4, 1\n"
    "v_lshlrev_b32 v140, 1, v140\n"
    "v_xor_b32 v139, v139, v140\n"
    "v_and_b32 v140, 1, v255\n"
    "v_lshlrev_b32 v140, 3, v140\n"
    "s_and_b32 s72, s70, 1\n"
    "s_lshl_b32 s72, s72, 1\n"
    "v_xor_b32 v141, s72, v139\n"
    "v_lshlrev_b32 v111, 6, v138\n"
    "v_lshl_add_u32 v111, v141, 4, v111\n"
    "v_add_u32 v111, v111, v140\n"
    "v_add_u32 v112, 0x10000, v111\n"
    "v_add_u32 v113, 0x20000, v111\n"
    "s_lshl_b32 s72, s70, 1\n"
    "v_xor_b32 v139, s72, v139\n"
    "v_bfe_u32 v141, v255, 3, 1\n"
    "v_lshlrev_b32 v141, 2, v141\n"
    "v_xor_b32 v139, v139, v141\n"
    "v_lshlrev_b32 v108, 7, v138\n"
    "v_lshl_add_u32 v108, v139, 4, v108\n"
    "v_add_u32 v108, v108, v140\n"
    "v_add_u32 v109, 0x10000, v108\n"
    "v_add_u32 v110, 0x20000, v108\n"
    "v_mov_b32 v124, 0x3f803f80\n"
    "v_mov_b32 v125, 0x3f803f80\n"
    "v_mov_b32 v126, 0x3f803f80\n"
    "v_mov_b32 v127, 0x3f803f80\n"
    "v_and_b32 v138, 15, v255\n"
    "v_and_b32 v138, 15, v255\n"
    "s_lshl_b32 s72, s70, 7\n"
    "s_add_u32 s72, s72, 0x27800\n"
    "v_lshl_add_u32 v132, v138, 3, s72\n"
    "v_lshlrev_b32 v104, 10, v134\n"
    "v_lshl_add_u32 v104, v137, 4, v104\n"
    "v_add_u32 v105, 7168, v104\n"
    "s_lshl_b32 s79, s70, 14\n"
    "s_lshl_b32 s72, s69, 7\n"
    "s_add_u32 s79, s79, s72\n"
    "s_lshl_b32 s73, s68, 6\n"
    "s_and_b32 s74, s70, 1\n"
    "s_lshl_b32 s74, s74, 5\n"
    "s_add_u32 s73, s73, s74\n"
    "s_lshr_b32 s74, s70, 1\n"
    "s_add_u32 s73, s73, s74\n"
    "s_lshl_b32 s73, s73, 10\n"
    "s_add_u32 s91, s73, s72\n"
    "s_bfe_u32 s89, s71, 0x40003\n"
    "s_lshl_b32 s89, s89, 2\n"
    "s_add_u32 s89, s89, s70\n"
    "s_lshl_b32 s89, s89, 10\n"
    "v_min_u32 v143, 7, v255\n"
    "v_lshlrev_b32 v143, 7, v143\n"
    "s_mov_b32 s44, s24\n"
    "s_and_b32 s45, s25, 0xffff\n"
    "s_mov_b32 s46, 0x900000\n"
    "s_mov_b32 s47, 0x20000\n"
    "s_lshl_b32 s72, s68, 3\n"
    "s_add_u32 s72, s72, s69\n"
    "s_lshl_b32 s72, s72, 10\n"
    "s_cmp_eq_u32 s70, 0\n"
    "s_cselect_b32 s73, 1, 0\n"
    "s_cselect_b32 s72, s72, 0x40000000\n"
    "v_and_b32 v156, 7, v255\n"
    "v_lshl_add_u32 v156, v156, 3, s72\n"
    "v_mov_b32 v162, 0x40000000\n"
    "v_cmp_gt_u32 vcc, s73, v255\n"
    "s_nop 1\n"
    "v_cndmask_b32 v162, v162, v156, vcc\n"
    "s_and_b32 s72, s70, 1\n"
    "s_lshl_b32 s72, s72, 7\n"
    "s_add_u32 s72, s72, 0x27e00\n"
    "v_lshl_add_u32 v133, v138, 3, s72\n"
    "v_lshrrev_b32 v139, 2, v255\n"
    "v_mul_u32_u24 v102, 3584, v139\n"
    "v_lshrrev_b32 v139, 5, v255\n"
    "v_lshlrev_b32 v139, 1, v139\n"
    "v_and_b32 v140, 3, v255\n"
    "v_xor_b32 v139, v139, v140\n"
    "v_lshl_add_u32 v102, v139, 4, v102\n"
    "v_add_u32 v103, 56320, v102\n"
    "s_mul_i32 s72, s70, 114688\n"
    "s_add_u32 s72, s72, s78\n"
    "s_sub_u32 s72, s72, s77\n"
    "s_add_u32 s60, s56, s72\n"
    "s_add_u32 s61, s57, s72\n"
    "s_add_u32 s62, s58, s72\n"
    "s_add_u32 s63, s59, s72\n"
    "s_add_u32 m0, s82, 0x6000\n"
    "s_add_u32 s84, s56, 0x38000\n"
    "buffer_load_dwordx4 v100, s[32:35], s84 offen offset:0 lds\n"
    "buffer_load_dwordx4 v101, s[32:35], s84 offen offset:1024 lds\n"
    "s_waitcnt vmcnt(12)\n"
    "s_waitcnt lgkmcnt(0)\n"
    "s_barrier\n"
    "s_add_u32 m0, s81, 0xc000\n"
    "s_add_u32 s84, s53, 0x0\n"
    "buffer_load_dwordx4 v96, s[28:31], s84 offen offset:0 lds\n"
    "buffer_load_dwordx4 v97, s[28:31], s84 offen offset:1024 lds\n"
    "buffer_load_dwordx4 v98, s[28:31], s84 offen offset:2048 lds\n"
    "ds_read_b64_tr_b16 v[64:65], v108 offset:16384\n"
    "ds_read_b64_tr_b16 v[66:67], v108 offset:16896\n"
    "buffer_load_dwordx4 v99, s[28:31], s84 offen offset:3072 lds\n"
    "ds_read_b64_tr_b16 v[68:69], v108 offset:20480\n"
    "ds_read_b64_tr_b16 v[70:71], v108 offset:20992\n"
    "s_add_u32 m0, s82, 0x10000\n"
    "s_add_u32 s84, s57, 0x0\n"
    "buffer_load_dwordx4 v100, s[32:35], s84 offen offset:0 lds\n"
    "s_waitcnt lgkmcnt(2)\n"
    "v_mfma_f32_16x16x32_bf16 v[116:119], v[124:127], v[64:67], 0\n"
    "s_waitcnt lgkmcnt(0)\n"
    "v_mfma_f32_16x16x32_bf16 v[116:119], v[124:127], v[68:71], v[116:119]\n"
    "buffer_load_dwordx4 v101, s[32:35], s84 offen offset:1024 lds\n"
    "s_add_u32 m0, s81, 0x14000\n"
    "s_add_u32 s84, s53, 0x80\n"
    "buffer_load_dwordx4 v96, s[28:31], s84 offen offset:0 lds\n"
    "buffer_load_dwordx4 v97, s[28:31], s84 offen offset:1024 lds\n"
    "buffer_load_dwordx4 v98, s[28:31], s84 offen offset:2048 lds\n"
    "buffer_load_dwordx4 v99, s[28:31], s84 offen offset:3072 lds\n"
    "s_waitcnt vmcnt(10)\n"
    "s_barrier\n"
    "s_add_u32 m0, s82, 0x12000\n"
    "s_add_u32 s84, s57, 0x38000\n"
    "buffer_load_dwordx4 v100, s[32:35], s84 offen offset:0 lds\n"
    "buffer_load_dwordx4 v101, s[32:35], s84 offen offset:1024 lds\n"
    "s_add_u32 m0, s81, 0x18000\n"
    "s_add_u32 s84, s54, 0x0\n"
    "buffer_load_dwordx4 v96, s[28:31], s84 offen offset:0 lds\n"
    "ds_read_b64_tr_b16 v[72:73], v108 offset:24576\n"
    "ds_read_b64_tr_b16 v[74:75], v108 offset:25088\n"
    "buffer_load_dwordx4 v97, s[28:31], s84 offen offset:1024 lds\n"
    "ds_read_b64_tr_b16 v[76:77], v108 offset:28672\n"
    "ds_read_b64_tr_b16 v[78:79], v108 offset:29184\n"
    "buffer_load_dwordx4 v98, s[28:31], s84 offen offset:2048 lds\n"
    "s_waitcnt lgkmcnt(2)\n"
    "v_mfma_f32_16x16x32_bf16 v[116:119], v[124:127], v[72:75], v[116:119]\n"
    "s_waitcnt lgkmcnt(0)\n"
    "v_mfma_f32_16x16x32_bf16 v[116:119], v[124:127], v[76:79], v[116:119]\n"
    "buffer_load_dwordx4 v99, s[28:31], s84 offen offset:3072 lds\n"
    "s_add_u32 m0, s82, 0x1c000\n"
    "s_add_u32 s84, s58, 0x0\n"
    "buffer_load_dwordx4 v100, s[32:35], s84 offen offset:0 lds\n"
    "buffer_load_dwordx4 v101, s[32:35], s84 offen offset:1024 lds\n"
    "s_waitcnt vmcnt(12)\n"
    "s_barrier\n"
    "s_add_u32 m0, s81, 0x20000\n"
    "s_add_u32 s84, s54, 0x80\n"
    "buffer_load_dwordx4 v96, s[28:31], s84 offen offset:0 lds\n"
    "buffer_load_dwordx4 v97, s[28:31], s84 offen offset:1024 lds\n"
    "buffer_load_dwordx4 v98, s[28:31], s84 offen offset:2048 lds\n"
    "ds_read_b64_tr_b16 v[64:65], v109 offset:0\n"
    "ds_read_b64_tr_b16 v[66:67], v109 offset:512\n"
    "buffer_load_dwordx4 v99, s[28:31], s84 offen offset:3072 lds\n"
    "ds_read_b64_tr_b16 v[68:69], v109 offset:4096\n"
    "ds_read_b64_tr_b16 v[70:71], v109 offset:4608\n"
    "s_add_u32 m0, s82, 0x1e000\n"
    "s_add_u32 s84, s58, 0x38000\n"
    "buffer_load_dwordx4 v100, s[32:35], s84 offen offset:0 lds\n"
    "s_waitcnt lgkmcnt(2)\n"
    "v_mfma_f32_16x16x32_bf16 v[116:119], v[124:127], v[64:67], v[116:119]\n"
    "s_waitcnt lgkmcnt(0)\n"
    "v_mfma_f32_16x16x32_bf16 v[116:119], v[124:127], v[68:71], v[116:119]\n"
    "buffer_load_dwordx4 v101, s[32:35], s84 offen offset:1024 lds\n"
    "s_lshl_b32 s92, s65, 2\n"
    "s_add_u32 s92, s92, s70\n"
    "s_mul_i32 s92, s92, 28672\n"
    "v_and_b32 v157, 7, v255\n"
    "v_mul_u32_u24 v157, 3584, v157\n"
    "v_lshrrev_b32 v142, 3, v255\n"
    "v_min_u32 v142, 3, v142\n"
    "v_lshl_add_u32 v157, v142, 7, v157\n"
    "s_mul_i32 s93, s66, 448\n"
    "s_add_u32 s92, s92, s93\n"
    "buffer_load_dword v163, v157, s[32:35], s92 offen\n"
    "s_waitcnt vmcnt(13)\n"
    "s_barrier\n"
    "s_add_u32 m0, s81, 0x0\n"
    "s_add_u32 s84, s55, 0x0\n"
    "buffer_load_dwordx4 v96, s[28:31], s84 offen offset:0 lds\n"
    "buffer_load_dwordx4 v97, s[28:31], s84 offen offset:1024 lds\n"
    "buffer_load_dwordx4 v98, s[28:31], s84 offen offset:2048 lds\n"
    "ds_read_b64_tr_b16 v[72:73], v109 offset:8192\n"
    "ds_read_b64_tr_b16 v[74:75], v109 offset:8704\n"
    "buffer_load_dwordx4 v99, s[28:31], s84 offen offset:3072 lds\n"
    "ds_read_b64_tr_b16 v[76:77], v109 offset:12288\n"
    "ds_read_b64_tr_b16 v[78:79], v109 offset:12800\n"
    "s_add_u32 m0, s82, 0x4000\n"
    "s_add_u32 s84, s59, 0x0\n"
    "buffer_load_dwordx4 v100, s[32:35], s84 offen offset:0 lds\n"
    "s_waitcnt lgkmcnt(2)\n"
    "v_mfma_f32_16x16x32_bf16 v[116:119], v[124:127], v[72:75], v[116:119]\n"
    "s_waitcnt lgkmcnt(0)\n"
    "v_mfma_f32_16x16x32_bf16 v[116:119], v[124:127], v[76:79], v[116:119]\n"
    "buffer_load_dwordx4 v101, s[32:35], s84 offen offset:1024 lds\n"
    "s_waitcnt vmcnt(13)\n"
    "s_barrier\n"
    "s_add_u32 m0, s81, 0x8000\n"
    "s_add_u32 s84, s55, 0x80\n"
    "buffer_load_dwordx4 v96, s[28:31], s84 offen offset:0 lds\n"
    "buffer_load_dwordx4 v97, s[28:31], s84 offen offset:1024 lds\n"
    "buffer_load_dwordx4 v98, s[28:31], s84 offen offset:2048 lds\n"
    "ds_read_b64_tr_b16 v[64:65], v109 offset:49152\n"
    "ds_read_b64_tr_b16 v[66:67], v109 offset:49664\n"
    "buffer_load_dwordx4 v99, s[28:31], s84 offen offset:3072 lds\n"
    "ds_read_b64_tr_b16 v[68:69], v109 offset:53248\n"
    "ds_read_b64_tr_b16 v[70:71], v109 offset:53760\n"
    "s_add_u32 m0, s82, 0x6000\n"
    "s_add_u32 s84, s59, 0x38000\n"
    "buffer_load_dwordx4 v100, s[32:35], s84 offen offset:0 lds\n"
    "s_waitcnt lgkmcnt(2)\n"
    "v_mfma_f32_16x16x32_bf16 v[116:119], v[124:127], v[64:67], v[116:119]\n"
    "s_waitcnt lgkmcnt(0)\n"
    "v_mfma_f32_16x16x32_bf16 v[116:119], v[124:127], v[68:71], v[116:119]\n"
    "buffer_load_dwordx4 v101, s[32:35], s84 offen offset:1024 lds\n"
    "s_waitcnt vmcnt(13)\n"
    "s_barrier\n"
    "s_add_u32 m0, s82, 0xc000\n"
    "s_add_u32 s84, s56, 0x80\n"
    "buffer_load_dwordx4 v100, s[32:35], s84 offen offset:0 lds\n"
    "buffer_load_dwordx4 v101, s[32:35], s84 offen offset:1024 lds\n"
    "s_add_u32 m0, s82, 0xe000\n"
    "s_add_u32 s84, s56, 0x38080\n"
    "buffer_load_dwordx4 v100, s[32:35], s84 offen offset:0 lds\n"
    "ds_read_b64_tr_b16 v[72:73], v109 offset:57344\n"
    "ds_read_b64_tr_b16 v[74:75], v109 offset:57856\n"
    "buffer_load_dwordx4 v101, s[32:35], s84 offen offset:1024 lds\n"
    "ds_read_b64_tr_b16 v[76:77], v109 offset:61440\n"
    "ds_read_b64_tr_b16 v[78:79], v109 offset:61952\n"
    "s_add_u32 m0, s82, 0x10000\n"
    "s_add_u32 s84, s57, 0x80\n"
    "buffer_load_dwordx4 v100, s[32:35], s84 offen offset:0 lds\n"
    "s_waitcnt lgkmcnt(2)\n"
    "v_mfma_f32_16x16x32_bf16 v[116:119], v[124:127], v[72:75], v[116:119]\n"
    "s_waitcnt lgkmcnt(0)\n"
    "v_mfma_f32_16x16x32_bf16 v[116:119], v[124:127], v[76:79], v[116:119]\n"
    "s_mul_i32 s96, s96, 0x9e3779b1\n"
    "s_mul_i32 s97, s97, 0x85ebca6d\n"
    "s_add_u32 s96, s96, 0x7f4a7c15\n"
    "s_add_u32 s97, s97, 0x165667b1\n"
    "buffer_load_dwordx4 v101, s[32:35], s84 offen offset:1024 lds\n"
    "s_mov_b32 s74, 0x9e3779b1\n"
    "s_mov_b32 s75, 0x85ebca6d\n"
    "v_mul_lo_u32 v164, v164, s74\n"
    "v_mul_lo_u32 v165, v165, s75\n"
    "v_add_u32 v164, 0x7f4a7c15, v164\n"
    "v_add_u32 v165, 0x165667b1, v165\n"
    "s_add_u32 m0, s82, 0x12000\n"
    "s_add_u32 s84, s57, 0x38080\n"
    "buffer_load_dwordx4 v100, s[32:35], s84 offen offset:0 lds\n"
    "buffer_load_dwordx4 v101, s[32:35], s84 offen offset:1024 lds\n"
    "s_waitcnt vmcnt(14)\n"
    "s_barrier\n"
    "s_add_u32 m0, s82, 0x14000\n"
    "s_add_u32 s84, s58, 0x80\n"
    "buffer_load_dwordx4 v100, s[32:35], s84 offen offset:0 lds\n"
    "buffer_load_dwordx4 v101, s[32:35], s84 offen offset:1024 lds\n"
    "s_add_u32 m0, s82, 0x16000\n"
    "s_add_u32 s84, s58, 0x38080\n"
    "buffer_load_dwordx4 v100, s[32:35], s84 offen offset:0 lds\n"
    "ds_read_b64_tr_b16 v[64:65], v108 offset:16384\n"
    "ds_read_b64_tr_b16 v[66:67], v108 offset:16896\n"
    "buffer_load_dwordx4 v101, s[32:35], s84 offen offset:1024 lds\n"
    "ds_read_b64_tr_b16 v[68:69], v108 offset:20480\n"
    "ds_read_b64_tr_b16 v[70:71], v108 offset:20992\n"
    "s_add_u32 m0, s82, 0x18000\n"
    "s_add_u32 s84, s59, 0x80\n"
    "buffer_load_dwordx4 v100, s[32:35], s84 offen offset:0 lds\n"
    "s_waitcnt lgkmcnt(2)\n"
    "v_mfma_f32_16x16x32_bf16 v[116:119], v[124:127], v[64:67], v[116:119]\n"
    "s_waitcnt lgkmcnt(0)\n"
    "v_mfma_f32_16x16x32_bf16 v[116:119], v[124:127], v[68:71], v[116:119]\n"
    "buffer_load_dwordx4 v101, s[32:35], s84 offen offset:1024 lds\n"
    "s_add_u32 m0, s82, 0x1a000\n"
    "s_add_u32 s84, s59, 0x38080\n"
    "buffer_load_dwordx4 v100, s[32:35], s84 offen offset:0 lds\n"
    "buffer_load_dwordx4 v101, s[32:35], s84 offen offset:1024 lds\n"
    "s_waitcnt vmcnt(16)\n"
    "s_barrier\n"
    "s_lshl_b32 s92, s71, 2\n"
    "s_add_u32 s92, s92, s70\n"
    "v_min_u32 v157, 13, v255\n"
    "v_lshlrev_b32 v157, 7, v157\n"
    "s_mul_i32 s93, s92, 1792\n"
    "buffer_load_dword v163, v157, s[36:39], s93 offen\n"
    "v_min_u32 v157, 0x180, v157\n"
    "s_lshl_b32 s93, s92, 9\n"
    "buffer_load_dword v163, v157, s[40:43], s93 offen\n"
    "ds_read_b64_tr_b16 v[72:73], v108 offset:24576\n"
    "ds_read_b64_tr_b16 v[74:75], v108 offset:25088\n"
    "ds_read_b64_tr_b16 v[76:77], v108 offset:28672\n"
    "ds_read_b64_tr_b16 v[78:79], v108 offset:29184\n"
    "s_waitcnt lgkmcnt(2)\n"
    "v_mfma_f32_16x16x32_bf16 v[116:119], v[124:127], v[72:75], v[116:119]\n"
    "s_waitcnt lgkmcnt(0)\n"
    "v_mfma_f32_16x16x32_bf16 v[116:119], v[124:127], v[76:79], v[116:119]\n"
    "s_nop 7\n"
    "v_cvt_pk_bf16_f32 v134, v116, v116\n"
    "v_lshlrev_b32 v134, 16, v134\n"
    "v_sub_f32 v134, v116, v134\n"
    "v_cvt_pk_bf16_f32 v148, v116, v134\n"
    "v_and_b32 v135, 0xffff0000, v148\n"
    "v_sub_f32 v134, v134, v135\n"
    "v_cvt_pk_bf16_f32 v149, 0, v134\n"
    "v_or_b32 v149, v149, v128\n"
    "ds_write_b64 v132, v[148:149] offset:0\n"
    "s_waitcnt vmcnt(14)\n"
    "s_waitcnt lgkmcnt(0)\n"
    "s_barrier\n"
    "s_add_u32 m0, s82, 0x1c000\n"
    "s_add_u32 s84, s56, 0x100\n"
    "buffer_load_dwordx4 v100, s[32:35], s84 offen offset:0 lds\n"
    "buffer_load_dwordx4 v101, s[32:35], s84 offen offset:1024 lds\n"
    "s_add_u32 m0, s82, 0x1e000\n"
    "s_add_u32 s84, s56, 0x38100\n"
    "buffer_load_dwordx4 v100, s[32:35], s84 offen offset:0 lds\n"
    "ds_read_b64_tr_b16 v[64:65], v108 offset:49152\n"
    "ds_read_b64_tr_b16 v[66:67], v108 offset:49664\n"
    "buffer_load_dwordx4 v101, s[32:35], s84 offen offset:1024 lds\n"
    "ds_read_b64_tr_b16 v[68:69], v108 offset:53248\n"
    "ds_read_b64_tr_b16 v[70:71], v108 offset:53760\n"
    "s_add_u32 m0, s82, 0x20000\n"
    "s_add_u32 s84, s57, 0x100\n"
    "buffer_load_dwordx4 v100, s[32:35], s84 offen offset:0 lds\n"
    "ds_read_b64_tr_b16 v[72:73], v108 offset:57344\n"
    "ds_read_b64_tr_b16 v[74:75], v108 offset:57856\n"
    "buffer_load_dwordx4 v101, s[32:35], s84 offen offset:1024 lds\n"
    "ds_read_b64_tr_b16 v[76:77], v108 offset:61440\n"
    "ds_read_b64_tr_b16 v[78:79], v108 offset:61952\n"
    "s_add_u32 m0, s82, 0x22000\n"
    "s_add_u32 s84, s57, 0x38100\n"
    "buffer_load_dwordx4 v100, s[32:35], s84 offen offset:0 lds\n"
    "s_waitcnt lgkmcnt(6)\n"
    "v_mfma_f32_16x16x32_bf16 v[116:119], v[124:127], v[64:67], 0\n"
    "s_waitcnt lgkmcnt(4)\n"
    "v_mfma_f32_16x16x32_bf16 v[116:119], v[124:127], v[68:71], v[116:119]\n"
    "s_waitcnt lgkmcnt(2)\n"
    "v_mfma_f32_16x16x32_bf16 v[116:119], v[124:127], v[72:75], v[116:119]\n"
    "s_waitcnt lgkmcnt(0)\n"
    "v_mfma_f32_16x16x32_bf16 v[116:119], v[124:127], v[76:79], v[116:119]\n"
    "buffer_load_dwordx4 v101, s[32:35], s84 offen offset:1024 lds\n"
    "s_add_u32 m0, s82, 0x0\n"
    "s_add_u32 s84, s58, 0x100\n"
    "buffer_load_dwordx4 v100, s[32:35], s84 offen offset:0 lds\n"
    "buffer_load_dwordx4 v101, s[32:35], s84 offen offset:1024 lds\n"
    "s_waitcnt vmcnt(20)\n"
    "s_barrier\n"
    "s_add_u32 s92, s89, 0xf0000\n"
    "buffer_load_dword v163, v143, s[36:39], s92 offen\n"
    "s_add_u32 m0, s82, 0x2000\n"
    "s_add_u32 s84, s58, 0x38100\n"
    "buffer_load_dwordx4 v100, s[32:35], s84 offen offset:0 lds\n"
    "buffer_load_dwordx4 v101, s[32:35], s84 offen offset:1024 lds\n"
    "ds_read_b64_tr_b16 v[64:65], v109 offset:0\n"
    "ds_read_b64_tr_b16 v[66:67], v109 offset:512\n"
    "s_add_u32 m0, s82, 0x4000\n"
    "s_add_u32 s84, s59, 0x100\n"
    "buffer_load_dwordx4 v100, s[32:35], s84 offen offset:0 lds\n"
    "ds_read_b64_tr_b16 v[68:69], v109 offset:4096\n"
    "ds_read_b64_tr_b16 v[70:71], v109 offset:4608\n"
    "buffer_load_dwordx4 v101, s[32:35], s84 offen offset:1024 lds\n"
    "ds_read_b64_tr_b16 v[72:73], v109 offset:8192\n"
    "ds_read_b64_tr_b16 v[74:75], v109 offset:8704\n"
    "s_add_u32 m0, s82, 0x6000\n"
    "s_add_u32 s84, s59, 0x38100\n"
    "buffer_load_dwordx4 v100, s[32:35], s84 offen offset:0 lds\n"
    "ds_read_b64_tr_b16 v[76:77], v109 offset:12288\n"
    "ds_read_b64_tr_b16 v[78:79], v109 offset:12800\n"
    "buffer_load_dwordx4 v101, s[32:35], s84 offen offset:1024 lds\n"
    "s_waitcnt lgkmcnt(6)\n"
    "v_mfma_f32_16x16x32_bf16 v[116:119], v[124:127], v[64:67], v[116:119]\n"
    "s_waitcnt lgkmcnt(4)\n"
    "v_mfma_f32_16x16x32_bf16 v[116:119], v[124:127], v[68:71], v[116:119]\n"
    "s_waitcnt lgkmcnt(2)\n"
    "v_mfma_f32_16x16x32_bf16 v[116:119], v[124:127], v[72:75], v[116:119]\n"
    "s_waitcnt lgkmcnt(0)\n"
    "v_mfma_f32_16x16x32_bf16 v[116:119], v[124:127], v[76:79], v[116:119]\n"
    "s_add_u32 m0, s82, 0x8000\n"
    "s_add_u32 s84, s60, 0x0\n"
    "buffer_load_dwordx4 v102, s[32:35], s84 offen offset:0 lds\n"
    "buffer_load_dwordx4 v103, s[32:35], s84 offen offset:1024 lds\n"
    "s_waitcnt vmcnt(25)\n"
    "s_barrier\n"
    "s_add_u32 s92, s89, 0x160000\n"
    "buffer_load_dword v163, v143, s[36:39], s92 offen\n"
    "s_add_u32 s92, s89, 0x130000\n"
    "buffer_load_dword v163, v143, s[36:39], s92 offen\n"
    "s_add_u32 m0, s82, 0xa000\n"
    "s_add_u32 s84, s61, 0x0\n"
    "buffer_load_dwordx4 v102, s[32:35], s84 offen offset:0 lds\n"
    "ds_read_b64_tr_b16 v[64:65], v109 offset:16384\n"
    "ds_read_b64_tr_b16 v[66:67], v109 offset:16896\n"
    "ds_read_b64_tr_b16 v[68:69], v109 offset:20480\n"
    "buffer_load_dwordx4 v103, s[32:35], s84 offen offset:1024 lds\n"
    "ds_read_b64_tr_b16 v[70:71], v109 offset:20992\n"
    "ds_read_b64_tr_b16 v[72:73], v109 offset:24576\n"
    "s_add_u32 m0, s82, 0xc000\n"
    "s_add_u32 s84, s62, 0x0\n"
    "buffer_load_dwordx4 v102, s[32:35], s84 offen offset:0 lds\n"
    "ds_read_b64_tr_b16 v[74:75], v109 offset:25088\n"
    "ds_read_b64_tr_b16 v[76:77], v109 offset:28672\n"
    "buffer_load_dwordx4 v103, s[32:35], s84 offen offset:1024 lds\n"
    "ds_read_b64_tr_b16 v[78:79], v109 offset:29184\n"
    "s_waitcnt lgkmcnt(6)\n"
    "v_mfma_f32_16x16x32_bf16 v[116:119], v[124:127], v[64:67], v[116:119]\n"
    "s_waitcnt lgkmcnt(4)\n"
    "v_mfma_f32_16x16x32_bf16 v[116:119], v[124:127], v[68:71], v[116:119]\n"
    "s_waitcnt lgkmcnt(2)\n"
    "v_mfma_f32_16x16x32_bf16 v[116:119], v[124:127], v[72:75], v[116:119]\n"
    "s_waitcnt lgkmcnt(0)\n"
    "v_mfma_f32_16x16x32_bf16 v[116:119], v[124:127], v[76:79], v[116:119]\n"
    "s_waitcnt vmcnt(27)\n"
    "s_barrier\n"
    "ds_read_b64_tr_b16 v[64:65], v109 offset:32768\n"
    "ds_read_b64_tr_b16 v[66:67], v109 offset:33280\n"
    "ds_read_b64_tr_b16 v[68:69], v109 offset:36864\n"
    "ds_read_b64_tr_b16 v[70:71], v109 offset:37376\n"
    "ds_read_b64_tr_b16 v[72:73], v109 offset:40960\n"
    "ds_read_b64_tr_b16 v[74:75], v109 offset:41472\n"
    "ds_read_b64_tr_b16 v[76:77], v109 offset:45056\n"
    "ds_read_b64_tr_b16 v[78:79], v109 offset:45568\n"
    "s_waitcnt lgkmcnt(6)\n"
    "v_mfma_f32_16x16x32_bf16 v[116:119], v[124:127], v[64:67], v[116:119]\n"
    "s_waitcnt lgkmcnt(4)\n"
    "v_mfma_f32_16x16x32_bf16 v[116:119], v[124:127], v[68:71], v[116:119]\n"
    "s_waitcnt lgkmcnt(2)\n"
    "v_mfma_f32_16x16x32_bf16 v[116:119], v[124:127], v[72:75], v[116:119]\n"
    "s_waitcnt lgkmcnt(0)\n"
    "v_mfma_f32_16x16x32_bf16 v[116:119], v[124:127], v[76:79], v[116:119]\n"
    "s_nop 7\n"
    "v_cvt_pk_bf16_f32 v134, v116, v116\n"
    "v_lshlrev_b32 v134, 16, v134\n"
    "v_sub_f32 v134, v116, v134\n"
    "v_cvt_pk_bf16_f32 v148, v116, v134\n"
    "v_and_b32 v135, 0xffff0000, v148\n"
    "v_sub_f32 v134, v134, v135\n"
    "v_cvt_pk_bf16_f32 v149, 0, v134\n"
    "v_or_b32 v149, v149, v129\n"
    "ds_write_b64 v132, v[148:149] offset:512\n"
    "s_waitcnt vmcnt(21)\n"
    "s_waitcnt lgkmcnt(0)\n"
    "s_barrier\n"
    "s_add_u32 s92, s89, 0x1a0000\n"
    "buffer_load_dword v163, v143, s[36:39], s92 offen\n"
    "s_add_u32 m0, s82, 0xe000\n"
    "s_add_u32 s84, s63, 0x0\n"
    "buffer_load_dwordx4 v102, s[32:35], s84 offen offset:0 lds\n"
    "buffer_load_dwordx4 v103, s[32:35], s84 offen offset:1024 lds\n"
    "ds_read_b64_tr_b16 v[64:65], v109 offset:49152\n"
    "ds_read_b64_tr_b16 v[66:67], v109 offset:49664\n"
    "s_add_u32 m0, s82, 0x10000\n"
    "s_add_u32 s84, s79, 0x0\n"
    "buffer_load_dwordx4 v104, s[36:39], s84 offen offset:0 lds\n"
    "ds_read_b64_tr_b16 v[68:69], v109 offset:53248\n"
    "ds_read_b64_tr_b16 v[70:71], v109 offset:53760\n"
    "buffer_load_dwordx4 v105, s[36:39], s84 offen offset:1024 lds\n"
    "ds_read_b64_tr_b16 v[72:73], v109 offset:57344\n"
    "ds_read_b64_tr_b16 v[74:75], v109 offset:57856\n"
    "s_add_u32 m0, s82, 0x12000\n"
    "s_add_u32 s84, s79, 0xe0000\n"
    "buffer_load_dwordx4 v104, s[36:39], s84 offen offset:0 lds\n"
    "ds_read_b64_tr_b16 v[76:77], v109 offset:61440\n"
    "ds_read_b64_tr_b16 v[78:79], v109 offset:61952\n"
    "buffer_load_dwordx4 v105, s[36:39], s84 offen offset:1024 lds\n"
    "s_waitcnt lgkmcnt(6)\n"
    "v_mfma_f32_16x16x32_bf16 v[116:119], v[124:127], v[64:67], 0\n"
    "s_waitcnt lgkmcnt(4)\n"
    "v_mfma_f32_16x16x32_bf16 v[116:119], v[124:127], v[68:71], v[116:119]\n"
    "s_waitcnt lgkmcnt(2)\n"
    "v_mfma_f32_16x16x32_bf16 v[116:119], v[124:127], v[72:75], v[116:119]\n"
    "s_waitcnt lgkmcnt(0)\n"
    "v_mfma_f32_16x16x32_bf16 v[116:119], v[124:127], v[76:79], v[116:119]\n"
    "s_add_u32 m0, s82, 0x14000\n"
    "s_add_u32 s84, s79, 0x40000\n"
    "buffer_load_dwordx4 v104, s[36:39], s84 offen offset:0 lds\n"
    "buffer_load_dwordx4 v105, s[36:39], s84 offen offset:1024 lds\n"
    "s_waitcnt vmcnt(26)\n"
    "s_barrier\n"
    "s_add_u32 s92, s89, 0x170000\n"
    "buffer_load_dword v163, v143, s[36:39], s92 offen\n"
    "s_add_u32 s92, s89, 0x140000\n"
    "buffer_load_dword v163, v143, s[36:39], s92 offen\n"
    "s_add_u32 s92, s89, 0x100000\n"
    "buffer_load_dword v163, v143, s[36:39], s92 offen\n"
    "ds_read_b64_tr_b16 v[64:65], v110 offset:0\n"
    "ds_read_b64_tr_b16 v[66:67], v110 offset:512\n"
    "buffer_store_dwordx2 v[164:165], v166, s[48:51], 0 offen offset:0 sc1\n"
    "ds_read_b64_tr_b16 v[68:69], v110 offset:4096\n"
    "ds_read_b64_tr_b16 v[70:71], v110 offset:4608\n"
    "s_add_u32 m0, s82, 0x16000\n"
    "s_add_u32 s84, s79, 0x120000\n"
    "buffer_load_dwordx4 v104, s[36:39], s84 offen offset:0 lds\n"
    "ds_read_b64_tr_b16 v[72:73], v110 offset:8192\n"
    "ds_read_b64_tr_b16 v[74:75], v110 offset:8704\n"
    "buffer_load_dwordx4 v105, s[36:39], s84 offen offset:1024 lds\n"
    "ds_read_b64_tr_b16 v[76:77], v110 offset:12288\n"
    "ds_read_b64_tr_b16 v[78:79], v110 offset:12800\n"
    "s_add_u32 m0, s82, 0x18000\n"
    "s_add_u32 s84, s79, 0x70000\n"
    "buffer_load_dwordx4 v104, s[36:39], s84 offen offset:0 lds\n"
    "s_waitcnt lgkmcnt(6)\n"
    "v_mfma_f32_16x16x32_bf16 v[116:119], v[124:127], v[64:67], v[116:119]\n"
    "s_waitcnt lgkmcnt(4)\n"
    "v_mfma_f32_16x16x32_bf16 v[116:119], v[124:127], v[68:71], v[116:119]\n"
    "s_waitcnt lgkmcnt(2)\n"
    "v_mfma_f32_16x16x32_bf16 v[116:119], v[124:127], v[72:75], v[116:119]\n"
    "s_waitcnt lgkmcnt(0)\n"
    "v_mfma_f32_16x16x32_bf16 v[116:119], v[124:127], v[76:79], v[116:119]\n"
    "buffer_load_dwordx4 v105, s[36:39], s84 offen offset:1024 lds\n"
    "s_add_u32 m0, s82, 0x1a000\n"
    "s_add_u32 s84, s79, 0x150000\n"
    "buffer_load_dwordx4 v104, s[36:39], s84 offen offset:0 lds\n"
    "buffer_load_dwordx4 v105, s[36:39], s84 offen offset:1024 lds\n"
    "s_add_u32 m0, s82, 0x1c000\n"
    "s_add_u32 s84, s79, 0xb0000\n"
    "buffer_load_dwordx4 v104, s[36:39], s84 offen offset:0 lds\n"
    "buffer_load_dwordx4 v105, s[36:39], s84 offen offset:1024 lds\n"
    "s_add_u32 m0, s82, 0x1e000\n"
    "s_add_u32 s84, s79, 0x190000\n"
    "buffer_load_dwordx4 v104, s[36:39], s84 offen offset:0 lds\n"
    "buffer_load_dwordx4 v105, s[36:39], s84 offen offset:1024 lds\n"
    "s_waitcnt vmcnt(35)\n"
    "s_barrier\n"
    "s_add_u32 s92, s89, 0x180000\n"
    "buffer_load_dword v163, v143, s[36:39], s92 offen\n"
    "s_add_u32 s92, s89, 0x110000\n"
    "buffer_load_dword v163, v143, s[36:39], s92 offen\n"
    "s_add_u32 s92, s89, 0x1b0000\n"
    "buffer_load_dword v163, v143, s[36:39], s92 offen\n"
    "ds_read_b64_tr_b16 v[64:65], v108 offset:0\n"
    "ds_read_b64_tr_b16 v[66:67], v108 offset:512\n"
    "s_add_u32 m0, s82, 0x20000\n"
    "s_add_u32 s84, s79, 0x10000\n"
    "buffer_load_dwordx4 v104, s[36:39], s84 offen offset:0 lds\n"
    "ds_read_b64_tr_b16 v[68:69], v108 offset:4096\n"
    "ds_read_b64_tr_b16 v[70:71], v108 offset:4608\n"
    "buffer_load_dwordx4 v105, s[36:39], s84 offen offset:1024 lds\n"
    "ds_read_b64_tr_b16 v[72:73], v108 offset:8192\n"
    "ds_read_b64_tr_b16 v[74:75], v108 offset:8704\n"
    "s_add_u32 m0, s82, 0x22000\n"
    "s_add_u32 s84, s79, 0xf0000\n"
    "buffer_load_dwordx4 v104, s[36:39], s84 offen offset:0 lds\n"
    "ds_read_b64_tr_b16 v[76:77], v108 offset:12288\n"
    "ds_read_b64_tr_b16 v[78:79], v108 offset:12800\n"
    "buffer_load_dwordx4 v105, s[36:39], s84 offen offset:1024 lds\n"
    "s_waitcnt lgkmcnt(6)\n"
    "v_mfma_f32_16x16x32_bf16 v[116:119], v[124:127], v[64:67], v[116:119]\n"
    "s_waitcnt lgkmcnt(4)\n"
    "v_mfma_f32_16x16x32_bf16 v[116:119], v[124:127], v[68:71], v[116:119]\n"
    "s_waitcnt lgkmcnt(2)\n"
    "v_mfma_f32_16x16x32_bf16 v[116:119], v[124:127], v[72:75], v[116:119]\n"
    "s_waitcnt lgkmcnt(0)\n"
    "v_mfma_f32_16x16x32_bf16 v[116:119], v[124:127], v[76:79], v[116:119]\n"
    "s_waitcnt vmcnt(38)\n"
    "s_barrier\n"
    "buffer_load_dwordx2 v[158:159], v156, s[48:51], 0 offen offset:0 sc1\n"
    "ds_read_b64_tr_b16 v[64:65], v108 offset:16384\n"
    "ds_read_b64_tr_b16 v[66:67], v108 offset:16896\n"
    "ds_read_b64_tr_b16 v[68:69], v108 offset:20480\n"
    "ds_read_b64_tr_b16 v[70:71], v108 offset:20992\n"
    "ds_read_b64_tr_b16 v[72:73], v108 offset:24576\n"
    "ds_read_b64_tr_b16 v[74:75], v108 offset:25088\n"
    "ds_read_b64_tr_b16 v[76:77], v108 offset:28672\n"
    "ds_read_b64_tr_b16 v[78:79], v108 offset:29184\n"
    "s_waitcnt lgkmcnt(6)\n"
    "v_mfma_f32_16x16x32_bf16 v[116:119], v[124:127], v[64:67], v[116:119]\n"
    "s_waitcnt lgkmcnt(4)\n"
    "v_mfma_f32_16x16x32_bf16 v[116:119], v[124:127], v[68:71], v[116:119]\n"
    "s_waitcnt lgkmcnt(2)\n"
    "v_mfma_f32_16x16x32_bf16 v[116:119], v[124:127], v[72:75], v[116:119]\n"
    "s_waitcnt lgkmcnt(0)\n"
    "v_mfma_f32_16x16x32_bf16 v[116:119], v[124:127], v[76:79], v[116:119]\n"
    "s_nop 7\n"
    "v_cvt_pk_bf16_f32 v134, v116, v116\n"
    "v_lshlrev_b32 v134, 16, v134\n"
    "v_sub_f32 v134, v116, v134\n"
    "v_cvt_pk_bf16_f32 v148, v116, v134\n"
    "v_and_b32 v135, 0xffff0000, v148\n"
    "v_sub_f32 v134, v134, v135\n"
    "v_cvt_pk_bf16_f32 v149, 0, v134\n"
    "v_or_b32 v149, v149, v130\n"
    "ds_write_b64 v132, v[148:149] offset:1024\n"
    "s_waitcnt vmcnt(33)\n"
    "s_waitcnt lgkmcnt(0)\n"
    "s_barrier\n"
    "s_setprio 0\n"
    "s_add_u32 s92, s89, 0x50000\n"
    "buffer_load_dword v163, v143, s[40:43], s92 offen\n"
    "s_setprio 2\n"
    "s_setprio 0\n"
    "s_add_u32 s92, s89, 0x40000\n"
    "buffer_load_dword v163, v143, s[40:43], s92 offen\n"
    "s_setprio 2\n"
    "s_setprio 0\n"
    "s_add_u32 m0, s82, 0x0\n"
    "s_add_u32 s84, s79, 0x50000\n"
    "buffer_load_dwordx4 v104, s[36:39], s84 offen offset:0 lds\n"
    "s_setprio 2\n"
    "ds_read_b64_tr_b16 v[64:65], v111 offset:32768\n"
    "ds_read_b64_tr_b16 v[66:67], v111 offset:33024\n"
    "ds_read_b64_tr_b16 v[68:69], v111 offset:34816\n"
    "s_setprio 0\n"
    "buffer_load_dwordx4 v105, s[36:39], s84 offen offset:1024 lds\n"
    "s_setprio 2\n"
    "ds_read_b64_tr_b16 v[70:71], v111 offset:35072\n"
    "ds_read_b64_tr_b16 v[72:73], v111 offset:36864\n"
    "s_setprio 0\n"
    "s_add_u32 m0, s82, 0x2000\n"
    "s_add_u32 s84, s79, 0x130000\n"
    "buffer_load_dwordx4 v104, s[36:39], s84 offen offset:0 lds\n"
    "s_setprio 2\n"
    "ds_read_b64_tr_b16 v[74:75], v111 offset:37120\n"
    "ds_read_b64_tr_b16 v[76:77], v111 offset:38912\n"
    "s_setprio 0\n"
    "buffer_load_dwordx4 v105, s[36:39], s84 offen offset:1024 lds\n"
    "s_setprio 2\n"
    "ds_read_b64_tr_b16 v[78:79], v111 offset:39168\n"
    "ds_read_b64_tr_b16 v[80:81], v111 offset:40960\n"
    "s_setprio 0\n"
    "s_add_u32 m0, s82, 0x4000\n"
    "s_add_u32 s84, s79, 0x80000\n"
    "buffer_load_dwordx4 v104, s[36:39], s84 offen offset:0 lds\n"
    "s_setprio 2\n"
    "ds_read_b64_tr_b16 v[82:83], v111 offset:41216\n"
    "ds_read_b64_tr_b16 v[84:85], v111 offset:43008\n"
    "s_setprio 0\n"
    "buffer_load_dwordx4 v105, s[36:39], s84 offen offset:1024 lds\n"
    "s_setprio 2\n"
    "ds_read_b64_tr_b16 v[86:87], v111 offset:43264\n"
    "ds_read_b64_tr_b16 v[88:89], v111 offset:45056\n"
    "s_setprio 0\n"
    "s_add_u32 m0, s82, 0x6000\n"
    "s_add_u32 s84, s79, 0x160000\n"
    "buffer_load_dwordx4 v104, s[36:39], s84 offen offset:0 lds\n"
    "s_setprio 2\n"
    "ds_read_b64_tr_b16 v[90:91], v111 offset:45312\n"
    "ds_read_b64_tr_b16 v[92:93], v111 offset:47104\n"
    "s_setprio 0\n"
    "buffer_load_dwordx4 v105, s[36:39], s84 offen offset:1024 lds\n"
    "s_setprio 2\n"
    "ds_read_b64_tr_b16 v[94:95], v111 offset:47360\n"
    "s_waitcnt lgkmcnt(14)\n"
    "v_mfma_f32_16x16x32_bf16 v[116:119], v[124:127], v[64:67], 0\n"
    "s_waitcnt lgkmcnt(12)\n"
    "v_mfma_f32_16x16x32_bf16 v[116:119], v[124:127], v[68:71], v[116:119]\n"
    "s_waitcnt lgkmcnt(10)\n"
    "v_mfma_f32_16x16x32_bf16 v[116:119], v[124:127], v[72:75], v[116:119]\n"
    "s_waitcnt lgkmcnt(8)\n"
    "v_mfma_f32_16x16x32_bf16 v[116:119], v[124:127], v[76:79], v[116:119]\n"
    "s_waitcnt lgkmcnt(6)\n"
    "v_mfma_f32_16x16x32_bf16 v[116:119], v[124:127], v[80:83], v[116:119]\n"
    "s_waitcnt lgkmcnt(4)\n"
    "v_mfma_f32_16x16x32_bf16 v[116:119], v[124:127], v[84:87], v[116:119]\n"
    "s_waitcnt lgkmcnt(2)\n"
    "v_mfma_f32_16x16x32_bf16 v[116:119], v[124:127], v[88:91], v[116:119]\n"
    "s_waitcnt lgkmcnt(0)\n"
    "v_mfma_f32_16x16x32_bf16 v[116:119], v[124:127], v[92:95], v[116:119]\n"
    "s_waitcnt vmcnt(10)\n"
    "v_cmp_ne_u64 vcc, s[96:97], v[158:159]\n"
    "s_cmp_lg_u32 s70, 0\n"
    "s_cbranch_scc1 POK_ffn_1\n"
    "s_mov_b32 s90, 0\n"
    "s_nop 1\n"
    "s_cbranch_vccz POK_ffn_1\n"
    "s_branch PS1_ffn_1\n"
    "PS0_ffn_1:\n"
    "s_xor_b64 s[8:9], s[8:9], s[96:97]\n"
    "s_xor_b64 s[10:11], s[10:11], s[96:97]\n"
    "s_xor_b64 s[12:13], s[12:13], s[96:97]\n"
    "s_xor_b64 s[14:15], s[14:15], s[96:97]\n"
    "s_xor_b64 s[16:17], s[16:17], s[96:97]\n"
    "s_xor_b64 s[18:19], s[18:19], s[96:97]\n"
    "s_xor_b64 s[20:21], s[20:21], s[96:97]\n"
    "s_xor_b64 s[22:23], s[22:23], s[96:97]\n"
    "s_or_b64 s[8:9], s[8:9], s[10:11]\n"
    "s_or_b64 s[8:9], s[8:9], s[12:13]\n"
    "s_or_b64 s[8:9], s[8:9], s[14:15]\n"
    "s_or_b64 s[8:9], s[8:9], s[16:17]\n"
    "s_or_b64 s[8:9], s[8:9], s[18:19]\n"
    "s_or_b64 s[8:9], s[8:9], s[20:21]\n"
    "s_or_b64 s[8:9], s[8:9], s[22:23]\n"
    "s_cbranch_scc0 POK_ffn_1\n"
    "PS1_ffn_1:\n"
    "s_load_dwordx16 s[8:23], s[98:99], 0x0 glc\n"
    "s_add_u32 s90, s90, 1\n"
    "s_cmp_lt_u32 s90, 64\n"
    "s_waitcnt lgkmcnt(0)\n"
    "s_cbranch_scc1 PS0_ffn_1\n"
    "PSP_ffn_1:\n"
    "buffer_load_dwordx2 v[158:159], v156, s[48:51], 0 offen offset:0 sc1\n"
    "s_waitcnt vmcnt(0)\n"
    "v_cmp_ne_u64 vcc, s[96:97], v[158:159]\n"
    "s_nop 4\n"
    "s_cbranch_vccz POK_ffn_1\n"
    "s_add_u32 s90, s90, 1\n"
    "s_cmp_lt_u32 s90, 0x40000\n"
    "s_cbranch_scc1 PSP_ffn_1\n"
    "POK_ffn_1:\n"
    "s_barrier\n"
    "s_setprio 0\n"
    "buffer_store_dwordx2 v[164:165], v166, s[48:51], 0 offen offset:128 sc1\n"
    "s_setprio 2\n"
    "ds_read_b64_tr_b16 v[64:65], v111 offset:49152\n"
    "ds_read_b64_tr_b16 v[66:67], v111 offset:49408\n"
    "ds_read_b64_tr_b16 v[68:69], v111 offset:51200\n"
    "ds_read_b64_tr_b16 v[70:71], v111 offset:51456\n"
    "ds_read_b64_tr_b16 v[72:73], v111 offset:53248\n"
    "ds_read_b64_tr_b16 v[74:75], v111 offset:53504\n"
    "ds_read_b64_tr_b16 v[76:77], v111 offset:55296\n"
    "ds_read_b64_tr_b16 v[78:79], v111 offset:55552\n"
    "ds_read_b64_tr_b16 v[80:81], v111 offset:57344\n"
    "ds_read_b64_tr_b16 v[82:83], v111 offset:57600\n"
    "ds_read_b64_tr_b16 v[84:85], v111 offset:59392\n"
    "ds_read_b64_tr_b16 v[86:87], v111 offset:59648\n"
    "ds_read_b64_tr_b16 v[88:89], v111 offset:61440\n"
    "ds_read_b64_tr_b16 v[90:91], v111 offset:61696\n"
    "ds_read_b64_tr_b16 v[92:93], v111 offset:63488\n"
    "ds_read_b64_tr_b16 v[94:95], v111 offset:63744\n"
    "s_waitcnt lgkmcnt(14)\n"
    "v_mfma_f32_16x16x32_bf16 v[116:119], v[124:127], v[64:67], v[116:119]\n"
    "s_waitcnt lgkmcnt(12)\n"
    "v_mfma_f32_16x16x32_bf16 v[116:119], v[124:127], v[68:71], v[116:119]\n"
    "s_waitcnt lgkmcnt(10)\n"
    "v_mfma_f32_16x16x32_bf16 v[116:119], v[124:127], v[72:75], v[116:119]\n"
    "s_waitcnt lgkmcnt(8)\n"
    "v_mfma_f32_16x16x32_bf16 v[116:119], v[124:127], v[76:79], v[116:119]\n"
    "s_waitcnt lgkmcnt(6)\n"
    "v_mfma_f32_16x16x32_bf16 v[116:119], v[124:127], v[80:83], v[116:119]\n"
    "s_waitcnt lgkmcnt(4)\n"
    "v_mfma_f32_16x16x32_bf16 v[116:119], v[124:127], v[84:87], v[116:119]\n"
    "s_waitcnt lgkmcnt(2)\n"
    "v_mfma_f32_16x16x32_bf16 v[116:119], v[124:127], v[88:91], v[116:119]\n"
    "s_waitcnt lgkmcnt(0)\n"
    "v_mfma_f32_16x16x32_bf16 v[116:119], v[124:127], v[92:95], v[116:119]\n"
    "s_nop 7\n"
    "v_cvt_pk_bf16_f32 v134, v116, v116\n"
    "v_lshlrev_b32 v134, 16, v134\n"
    "v_sub_f32 v134, v116, v134\n"
    "v_cvt_pk_bf16_f32 v148, v116, v134\n"
    "v_and_b32 v135, 0xffff0000, v148\n"
    "v_sub_f32 v134, v134, v135\n"
    "v_cvt_pk_bf16_f32 v149, 0, v134\n"
    "v_or_b32 v149, v149, v131\n"
    "ds_write_b64 v133, v[148:149]\n"
    "s_waitcnt lgkmcnt(0)\n"
    "s_barrier\n"
    "s_add_u32 s92, s89, 0x70000\n"
    "buffer_load_dword v163, v143, s[40:43], s92 offen\n"
    "s_add_u32 s92, s89, 0x60000\n"
    "buffer_load_dword v163, v143, s[40:43], s92 offen\n"
    "s_add_u32 m0, s82, 0x8000\n"
    "s_add_u32 s84, s79, 0xc0000\n"
    "buffer_load_dwordx4 v104, s[36:39], s84 offen offset:0 lds\n"
    "buffer_load_dwordx4 v105, s[36:39], s84 offen offset:1024 lds\n"
    "s_add_u32 m0, s82, 0xa000\n"
    "s_add_u32 s84, s79, 0x1a0000\n"
    "buffer_load_dwordx4 v104, s[36:39], s84 offen offset:0 lds\n"
    "buffer_load_dwordx4 v105, s[36:39], s84 offen offset:1024 lds\n"
    "s_barrier\n"
    "s_cmp_lg_u32 s70, 0\n"
    "s_cbranch_scc1 PI_ffn_2\n"
    "s_load_dwordx16 s[8:23], s[98:99], 0x80 glc\n"
    "PI_ffn_2:\n"
    "s_add_u32 m0, s82, 0xc000\n"
    "s_add_u32 s84, s79, 0x20000\n"
    "buffer_load_dwordx4 v104, s[36:39], s84 offen offset:0 lds\n"
    "buffer_load_dwordx4 v105, s[36:39], s84 offen offset:1024 lds\n"
    "s_waitcnt lgkmcnt(0)\n"
    "s_cmp_lg_u32 s70, 0\n"
    "s_cbranch_scc1 POK_ffn_3\n"
    "s_mov_b32 s90, 0\n"
    "PS0_ffn_3:\n"
    "s_xor_b64 s[8:9], s[8:9], s[96:97]\n"
    "s_xor_b64 s[10:11], s[10:11], s[96:97]\n"
    "s_xor_b64 s[12:13], s[12:13], s[96:97]\n"
    "s_xor_b64 s[14:15], s[14:15], s[96:97]\n"
    "s_xor_b64 s[16:17], s[16:17], s[96:97]\n"
    "s_xor_b64 s[18:19], s[18:19], s[96:97]\n"
    "s_xor_b64 s[20:21], s[20:21], s[96:97]\n"
    "s_xor_b64 s[22:23], s[22:23], s[96:97]\n"
    "s_or_b64 s[8:9], s[8:9], s[10:11]\n"
    "s_or_b64 s[8:9], s[8:9], s[12:13]\n"
    "s_or_b64 s[8:9], s[8:9], s[14:15]\n"
    "s_or_b64 s[8:9], s[8:9], s[16:17]\n"
    "s_or_b64 s[8:9], s[8:9], s[18:19]\n"
    "s_or_b64 s[8:9], s[8:9], s[20:21]\n"
    "s_or_b64 s[8:9], s[8:9], s[22:23]\n"
    "s_cbranch_scc0 POK_ffn_3\n"
    "PS1_ffn_3:\n"
    "s_load_dwordx16 s[8:23], s[98:99], 0x80 glc\n"
    "s_add_u32 s90, s90, 1\n"
    "s_cmp_lt_u32 s90, 64\n"
    "s_waitcnt lgkmcnt(0)\n"
    "s_cbranch_scc1 PS0_ffn_3\n"
    "PSP_ffn_3:\n"
    "buffer_load_dwordx2 v[158:159], v156, s[48:51], 0 offen offset:128 sc1\n"
    "s_waitcnt vmcnt(0)\n"
    "v_cmp_ne_u64 vcc, s[96:97], v[158:159]\n"
    "s_nop 4\n"
    "s_cbranch_vccz POK_ffn_3\n"
    "s_add_u32 s90, s90, 1\n"
    "s_cmp_lt_u32 s90, 0x40000\n"
    "s_cbranch_scc1 PSP_ffn_3\n"
    "POK_ffn_3:\n"
    "s_barrier\n"
    "buffer_store_dwordx2 v[164:165], v166, s[48:51], 0 offen offset:256 sc1\n"
    "s_add_u32 m0, s82, 0xe000\n"
    "s_add_u32 s84, s79, 0x100000\n"
    "buffer_load_dwordx4 v104, s[36:39], s84 offen offset:0 lds\n"
    "buffer_load_dwordx4 v105, s[36:39], s84 offen offset:1024 lds\n"
    "s_add_u32 m0, s82, 0x10000\n"
    "s_add_u32 s84, s79, 0x60000\n"
    "buffer_load_dwordx4 v104, s[36:39], s84 offen offset:0 lds\n"
    "buffer_load_dwordx4 v105, s[36:39], s84 offen offset:1024 lds\n"
    "s_barrier\n"
    "s_add_u32 m0, s82, 0x12000\n"
    "s_add_u32 s84, s79, 0x140000\n"
    "buffer_load_dwordx4 v104, s[36:39], s84 offen offset:0 lds\n"
    "buffer_load_dwordx4 v105, s[36:39], s84 offen offset:1024 lds\n"
    "s_barrier\n"
    "buffer_store_dwordx2 v[164:165], v166, s[48:51], 0 offen offset:384 sc1\n"
    "s_add_u32 m0, s82, 0x14000\n"
    "s_add_u32 s84, s79, 0x90000\n"
    "buffer_load_dwordx4 v104, s[36:39], s84 offen offset:0 lds\n"
    "buffer_load_dwordx4 v105, s[36:39], s84 offen offset:1024 lds\n"
    "s_add_u32 m0, s82, 0x16000\n"
    "s_add_u32 s84, s79, 0x170000\n"
    "buffer_load_dwordx4 v104, s[36:39], s84 offen offset:0 lds\n"
    "buffer_load_dwordx4 v105, s[36:39], s84 offen offset:1024 lds\n"
    "s_waitcnt vmcnt(25)\n"
    "s_barrier\n"
    "s_cmp_lg_u32 s70, 0\n"
    "s_cbranch_scc1 PI_ffn_4\n"
    "s_load_dwordx16 s[8:23], s[98:99], 0x100 glc\n"
    "PI_ffn_4:\n"
    "s_add_u32 m0, s82, 0x18000\n"
    "s_add_u32 s84, s79, 0xd0000\n"
    "buffer_load_dwordx4 v104, s[36:39], s84 offen offset:0 lds\n"
    "buffer_load_dwordx4 v105, s[36:39], s84 offen offset:1024 lds\n"
    "s_add_u32 m0, s82, 0x24000\n"
    "s_add_u32 s84, s91, 0x0\n"
    "buffer_load_dwordx4 v96, s[28:31], s84 offen offset:0 lds\n"
    "buffer_load_dwordx4 v97, s[28:31], s84 offen offset:1024 lds\n"
    "s_waitcnt lgkmcnt(0)\n"
    "s_cmp_lg_u32 s70, 0\n"
    "s_cbranch_scc1 POK_ffn_5\n"
    "s_mov_b32 s90, 0\n"
    "PS0_ffn_5:\n"
    "s_xor_b64 s[8:9], s[8:9], s[96:97]\n"
    "s_xor_b64 s[10:11], s[10:11], s[96:97]\n"
    "s_xor_b64 s[12:13], s[12:13], s[96:97]\n"
    "s_xor_b64 s[14:15], s[14:15], s[96:97]\n"
    "s_xor_b64 s[16:17], s[16:17], s[96:97]\n"
    "s_xor_b64 s[18:19], s[18:19], s[96:97]\n"
    "s_xor_b64 s[20:21], s[20:21], s[96:97]\n"
    "s_xor_b64 s[22:23], s[22:23], s[96:97]\n"
    "s_or_b64 s[8:9], s[8:9], s[10:11]\n"
    "s_or_b64 s[8:9], s[8:9], s[12:13]\n"
    "s_or_b64 s[8:9], s[8:9], s[14:15]\n"
    "s_or_b64 s[8:9], s[8:9], s[16:17]\n"
    "s_or_b64 s[8:9], s[8:9], s[18:19]\n"
    "s_or_b64 s[8:9], s[8:9], s[20:21]\n"
    "s_or_b64 s[8:9], s[8:9], s[22:23]\n"
    "s_cbranch_scc0 POK_ffn_5\n"
    "PS1_ffn_5:\n"
    "s_load_dwordx16 s[8:23], s[98:99], 0x100 glc\n"
    "s_add_u32 s90, s90, 1\n"
    "s_cmp_lt_u32 s90, 64\n"
    "s_waitcnt lgkmcnt(0)\n"
    "s_cbranch_scc1 PS0_ffn_5\n"
    "PSP_ffn_5:\n"
    "buffer_load_dwordx2 v[158:159], v156, s[48:51], 0 offen offset:256 sc1\n"
    "s_waitcnt vmcnt(0)\n"
    "v_cmp_ne_u64 vcc, s[96:97], v[158:159]\n"
    "s_nop 4\n"
    "s_cbranch_vccz POK_ffn_5\n"
    "s_add_u32 s90, s90, 1\n"
    "s_cmp_lt_u32 s90, 0x40000\n"
    "s_cbranch_scc1 PSP_ffn_5\n"
    "POK_ffn_5:\n"
    "s_waitcnt vmcnt(25)\n"
    "s_barrier\n"
    "s_add_u32 m0, s82, 0x1a000\n"
    "s_add_u32 s84, s79, 0x1b0000\n"
    "buffer_load_dwordx4 v104, s[36:39], s84 offen offset:0 lds\n"
    "buffer_load_dwordx4 v105, s[36:39], s84 offen offset:1024 lds\n"
    "s_add_u32 m0, s82, 0x1c000\n"
    "s_add_u32 s84, s79, 0x30000\n"
    "buffer_load_dwordx4 v104, s[36:39], s84 offen offset:0 lds\n"
    "buffer_load_dwordx4 v105, s[36:39], s84 offen offset:1024 lds\n"
    "s_waitcnt vmcnt(22)\n"
    "s_barrier\n"
    "s_add_u32 m0, s82, 0x1e000\n"
    "s_add_u32 s84, s79, 0x110000\n"
    "buffer_load_dwordx4 v104, s[36:39], s84 offen offset:0 lds\n"
    "buffer_load_dwordx4 v105, s[36:39], s84 offen offset:1024 lds\n"
    "s_waitcnt vmcnt(19)\n"
    "s_barrier\n"
    "s_add_u32 m0, s82, 0x20000\n"
    "s_add_u32 s84, s79, 0xa0000\n"
    "buffer_load_dwordx4 v104, s[36:39], s84 offen offset:0 lds\n"
    "buffer_load_dwordx4 v105, s[36:39], s84 offen offset:1024 lds\n"
    "s_waitcnt vmcnt(8)\n"
    "s_barrier\n"
    "s_cmp_lg_u32 s70, 0\n"
    "s_cbranch_scc1 PI_ffn_6\n"
    "s_load_dwordx16 s[8:23], s[98:99], 0x180 glc\n"
    "PI_ffn_6:\n"
    "s_add_u32 m0, s82, 0x22000\n"
    "s_add_u32 s84, s79, 0x180000\n"
    "buffer_load_dwordx4 v104, s[36:39], s84 offen offset:0 lds\n"
    "buffer_load_dwordx4 v105, s[36:39], s84 offen offset:1024 lds\n"
    "s_add_u32 m0, s82, 0x0\n"
    "s_add_u32 s84, s79, 0x0\n"
    "buffer_load_dwordx4 v104, s[40:43], s84 offen offset:0 lds\n"
    "buffer_load_dwordx4 v105, s[40:43], s84 offen offset:1024 lds\n"
    "s_waitcnt lgkmcnt(0)\n"
    "s_cmp_lg_u32 s70, 0\n"
    "s_cbranch_scc1 POK_ffn_7\n"
    "s_mov_b32 s90, 0\n"
    "PS0_ffn_7:\n"
    "s_xor_b64 s[8:9], s[8:9], s[96:97]\n"
    "s_xor_b64 s[10:11], s[10:11], s[96:97]\n"
    "s_xor_b64 s[12:13], s[12:13], s[96:97]\n"
    "s_xor_b64 s[14:15], s[14:15], s[96:97]\n"
    "s_xor_b64 s[16:17], s[16:17], s[96:97]\n"
    "s_xor_b64 s[18:19], s[18:19], s[96:97]\n"
    "s_xor_b64 s[20:21], s[20:21], s[96:97]\n"
    "s_xor_b64 s[22:23], s[22:23], s[96:97]\n"
    "s_or_b64 s[8:9], s[8:9], s[10:11]\n"
    "s_or_b64 s[8:9], s[8:9], s[12:13]\n"
    "s_or_b64 s[8:9], s[8:9], s[14:15]\n"
    "s_or_b64 s[8:9], s[8:9], s[16:17]\n"
    "s_or_b64 s[8:9], s[8:9], s[18:19]\n"
    "s_or_b64 s[8:9], s[8:9], s[20:21]\n"
    "s_or_b64 s[8:9], s[8:9], s[22:23]\n"
    "s_cbranch_scc0 POK_ffn_7\n"
    "PS1_ffn_7:\n"
    "s_load_dwordx16 s[8:23], s[98:99], 0x180 glc\n"
    "s_add_u32 s90, s90, 1\n"
    "s_cmp_lt_u32 s90, 64\n"
    "s_waitcnt lgkmcnt(0)\n"
    "s_cbranch_scc1 PS0_ffn_7\n"
    "PSP_ffn_7:\n"
    "buffer_load_dwordx2 v[158:159], v156, s[48:51], 0 offen offset:384 sc1\n"
    "s_waitcnt vmcnt(0)\n"
    "v_cmp_ne_u64 vcc, s[96:97], v[158:159]\n"
    "s_nop 4\n"
    "s_cbranch_vccz POK_ffn_7\n"
    "s_add_u32 s90, s90, 1\n"
    "s_cmp_lt_u32 s90, 0x40000\n"
    "s_cbranch_scc1 PSP_ffn_7\n"
    "POK_ffn_7:\n"
    "s_barrier\n"
    "s_add_u32 m0, s82, 0x2000\n"
    "s_add_u32 s84, s79, 0x40000\n"
    "buffer_load_dwordx4 v104, s[40:43], s84 offen offset:0 lds\n"
    "buffer_load_dwordx4 v105, s[40:43], s84 offen offset:1024 lds\n"
    "s_waitcnt vmcnt(12)\n"
    "s_barrier\n"
    "s_cmp_lg_u32 s70, 0\n"
    "s_cbranch_scc1 PI_ffn_8\n"
    "s_load_dwordx16 s[8:23], s[98:99], 0x200 glc\n"
    "PI_ffn_8:\n"
    "s_add_u32 m0, s82, 0x4000\n"
    "s_add_u32 s84, s79, 0x10000\n"
    "buffer_load_dwordx4 v104, s[40:43], s84 offen offset:0 lds\n"
    "buffer_load_dwordx4 v105, s[40:43], s84 offen offset:1024 lds\n"
    "s_waitcnt lgkmcnt(0)\n"
    "s_cmp_lg_u32 s70, 0\n"
    "s_cbranch_scc1 POK_ffn_9\n"
    "s_mov_b32 s90, 0\n"
    "PS0_ffn_9:\n"
    "s_xor_b64 s[8:9], s[8:9], s[96:97]\n"
    "s_xor_b64 s[10:11], s[10:11], s[96:97]\n"
    "s_xor_b64 s[12:13], s[12:13], s[96:97]\n"
    "s_xor_b64 s[14:15], s[14:15], s[96:97]\n"
    "s_xor_b64 s[16:17], s[16:17], s[96:97]\n"
    "s_xor_b64 s[18:19], s[18:19], s[96:97]\n"
    "s_xor_b64 s[20:21], s[20:21], s[96:97]\n"
    "s_xor_b64 s[22:23], s[22:23], s[96:97]\n"
    "s_or_b64 s[8:9], s[8:9], s[10:11]\n"
    "s_or_b64 s[8:9], s[8:9], s[12:13]\n"
    "s_or_b64 s[8:9], s[8:9], s[14:15]\n"
    "s_or_b64 s[8:9], s[8:9], s[16:17]\n"
    "s_or_b64 s[8:9], s[8:9], s[18:19]\n"
    "s_or_b64 s[8:9], s[8:9], s[20:21]\n"
    "s_or_b64 s[8:9], s[8:9], s[22:23]\n"
    "s_cbranch_scc0 POK_ffn_9\n"
    "PS1_ffn_9:\n"
    "s_load_dwordx16 s[8:23], s[98:99], 0x200 glc\n"
    "s_add_u32 s90, s90, 1\n"
    "s_cmp_lt_u32 s90, 64\n"
    "s_waitcnt lgkmcnt(0)\n"
    "s_cbranch_scc1 PS0_ffn_9\n"
    "PSP_ffn_9:\n"
    "buffer_load_dwordx2 v[158:159], v156, s[48:51], 0 offen offset:512 sc1\n"
    "s_waitcnt vmcnt(0)\n"
    "v_cmp_ne_u64 vcc, s[96:97], v[158:159]\n"
    "s_nop 4\n"
    "s_cbranch_vccz POK_ffn_9\n"
    "s_add_u32 s90, s90, 1\n"
    "s_cmp_lt_u32 s90, 0x40000\n"
    "s_cbranch_scc1 PSP_ffn_9\n"
    "POK_ffn_9:\n"
    "v_mov_b32 v160, s96\n"
    "v_mov_b32 v161, s97\n"
    "s_waitcnt vmcnt(10)\n"
    "s_barrier\n"
    "s_add_u32 m0, s82, 0x6000\n"
    "s_add_u32 s84, s79, 0x50000\n"
    "buffer_load_dwordx4 v104, s[40:43], s84 offen offset:0 lds\n"
    "buffer_load_dwordx4 v105, s[40:43], s84 offen offset:1024 lds\n"
    "s_add_u32 m0, s82, 0x8000\n"
    "s_add_u32 s84, s79, 0x20000\n"
    "buffer_load_dwordx4 v104, s[40:43], s84 offen offset:0 lds\n"
    "buffer_load_dwordx4 v105, s[40:43], s84 offen offset:1024 lds\n"
    "s_waitcnt vmcnt(10)\n"
    "s_barrier\n"
    "s_add_u32 m0, s82, 0xa000\n"
    "s_add_u32 s84, s79, 0x60000\n"
    "buffer_load_dwordx4 v104, s[40:43], s84 offen offset:0 lds\n"
    "buffer_load_dwordx4 v105, s[40:43], s84 offen offset:1024 lds\n"
    "s_waitcnt vmcnt(8)\n"
    "s_barrier\n"
    "s_waitcnt vmcnt(4)\n"
    "s_barrier\n"
    "s_add_u32 m0, s82, 0xc000\n"
    "s_add_u32 s84, s79, 0x30000\n"
    "buffer_load_dwordx4 v104, s[40:43], s84 offen offset:0 lds\n"
    "buffer_load_dwordx4 v105, s[40:43], s84 offen offset:1024 lds\n"
    "s_add_u32 m0, s82, 0xe000\n"
    "s_add_u32 s84, s79, 0x70000\n"
    "buffer_load_dwordx4 v104, s[40:43], s84 offen offset:0 lds\n"
    "buffer_load_dwordx4 v105, s[40:43], s84 offen offset:1024 lds\n"
    "s_waitcnt vmcnt(4)\n"
    "s_barrier\n"
    "s_waitcnt vmcnt(0)\n"
    "s_barrier\n"
    "buffer_store_dwordx2 v[160:161], v162, s[48:51], 0 offen offset:640\n"
    "s_barrier\n"
    "s_endpgm\n"
    "REST_L_att:\n"
    "s_add_u32 s26, s24, 0x1000000\n"
    "s_addc_u32 s27, s25, 0\n"
    "s_mov_b32 s48, s26\n"
    "s_and_b32 s49, s27, 0xffff\n"
    "s_mov_b32 s50, 0x40000\n"
    "s_mov_b32 s51, 0x20000\n"
    "s_lshl_b32 s72, s65, 14\n"
    "s_cmp_eq_u32 s70, 1\n"
    "s_cselect_b32 s73, 16, 0\n"
    "v_lshl_add_u32 v142, v255, 10, s72\n"
    "v_mov_b32 v166, 0x40000000\n"
    "v_cmp_gt_u32 vcc, s73, v255\n"
    "s_nop 1\n"
    "v_cndmask_b32 v166, v166, v142, vcc\n"
    "buffer_load_dwordx2 v[164:165], v166, s[48:51], 0 offen offset:640 sc1\n"
    "s_lshl_b32 s73, s66, 3\n"
    "v_add_u32 v166, s73, v166\n"
    "s_lshl_b32 s72, s68, 3\n"
    "s_add_u32 s72, s72, s69\n"
    "s_lshl_b32 s72, s72, 10\n"
    "s_add_u32 s98, s26, s72\n"
    "s_addc_u32 s99, s27, 0\n"
    "s_load_dwordx2 s[96:97], s[98:99], 0x280\n"
    "s_cmp_eq_u32 s70, 3\n"
    "s_cselect_b32 s72, s72, 0x40000000\n"
    "v_min_u32 v142, 4, v255\n"
    "v_lshl_add_u32 v142, v142, 7, s72\n"
    "buffer_load_dword v163, v142, s[48:51], 0 offen sc1\n"
    "s_mov_b32 s36, s18\n"
    "s_and_b32 s37, s19, 0xffff\n"
    "s_mov_b32 s38, 0x1c0000\n"
    "s_mov_b32 s39, 0x20000\n"
    "s_mov_b32 s40, s12\n"
    "s_and_b32 s41, s13, 0xffff\n"
    "s_mov_b32 s42, 0x80000\n"
    "s_mov_b32 s43, 0x20000\n"
    "s_add_u32 s72, s90, 1\n"
    "s_and_b32 s72, s72, 3\n"
    "s_lshl_b32 s73, s72, 8\n"
    "s_add_u32 s53, s76, s73\n"
    "s_add_u32 s61, s80, s73\n"
    "s_mul_i32 s73, s72, 393216\n"
    "s_add_u32 s57, s77, s73\n"
    "s_add_u32 s72, s90, 2\n"
    "s_and_b32 s72, s72, 3\n"
    "s_lshl_b32 s73, s72, 8\n"
    "s_add_u32 s54, s76, s73\n"
    "s_add_u32 s62, s80, s73\n"
    "s_mul_i32 s73, s72, 393216\n"
    "s_add_u32 s58, s77, s73\n"
    "s_add_u32 s72, s90, 3\n"
    "s_and_b32 s72, s72, 3\n"
    "s_lshl_b32 s73, s72, 8\n"
    "s_add_u32 s55, s76, s73\n"
    "s_add_u32 s63, s80, s73\n"
    "s_mul_i32 s73, s72, 393216\n"
    "s_add_u32 s59, s77, s73\n"
    "v_bfe_u32 v138, v255, 2, 2\n"
    "v_lshl_add_u32 v138, v136, 3, v138\n"
    "v_bfe_u32 v139, v255, 1, 1\n"
    "v_bfe_u32 v140, v255, 4, 1\n"
    "v_lshlrev_b32 v140, 1, v140\n"
    "v_xor_b32 v139, v139, v140\n"
    "v_and_b32 v140, 1, v255\n"
    "v_lshlrev_b32 v140, 3, v140\n"
    "s_lshl_b32 s72, s70, 1\n"
    "v_xor_b32 v139, s72, v139\n"
    "v_bfe_u32 v141, v255, 3, 1\n"
    "v_lshlrev_b32 v141, 2, v141\n"
    "v_xor_b32 v139, v139, v141\n"
    "v_lshlrev_b32 v108, 7, v138\n"
    "v_lshl_add_u32 v108, v139, 4, v108\n"
    "v_add_u32 v108, v108, v140\n"
    "v_add_u32 v109, 0x10000, v108\n"
    "v_add_u32 v110, 0x20000, v108\n"
    "v_mov_b32 v124, 0x3f803f80\n"
    "v_mov_b32 v125, 0x3f803f80\n"
    "v_mov_b32 v126, 0x3f803f80\n"
    "v_mov_b32 v127, 0x3f803f80\n"
    "v_and_b32 v138, 15, v255\n"
    "v_bfe_u32 v139, v255, 1, 3\n"
    "v_xor_b32 v139, v139, v136\n"
    "v_lshlrev_b32 v114, 7, v138\n"
    "v_lshl_add_u32 v114, v139, 4, v114\n"
    "v_add_u32 v114, 0x20000, v114\n"
    "v_xor_b32 v115, 64, v114\n"
    "v_mov_b32 v0, 0x3f803f80\n"
    "v_mov_b32 v1, 0x3f803f80\n"
    "v_mov_b32 v2, 0x3f803f80\n"
    "v_mov_b32 v3, 0x3f803f80\n"
    "v_mov_b32 v4, 0x3f803f80\n"
    "v_mov_b32 v5, 0x3f803f80\n"
    "v_mov_b32 v6, 0x3f803f80\n"
    "v_mov_b32 v7, 0x3f803f80\n"
    "v_and_b32 v138, 15, v255\n"
    "s_lshl_b32 s72, s70, 7\n"
    "s_add_u32 s72, s72, 0x27800\n"
    "v_lshl_add_u32 v132, v138, 3, s72\n"
    "v_lshlrev_b32 v104, 10, v134\n"
    "v_lshl_add_u32 v104, v137, 4, v104\n"
    "v_add_u32 v105, 7168, v104\n"
    "s_lshl_b32 s79, s70, 14\n"
    "s_lshl_b32 s72, s69, 7\n"
    "s_add_u32 s79, s79, s72\n"
    "s_lshl_b32 s73, s68, 6\n"
    "s_and_b32 s74, s70, 1\n"
    "s_lshl_b32 s74, s74, 5\n"
    "s_add_u32 s73, s73, s74\n"
    "s_lshr_b32 s74, s70, 1\n"
    "s_add_u32 s73, s73, s74\n"
    "s_lshl_b32 s73, s73, 10\n"
    "s_add_u32 s91, s73, s72\n"
    "s_bfe_u32 s89, s71, 0x40003\n"
    "s_lshl_b32 s89, s89, 2\n"
    "s_add_u32 s89, s89, s70\n"
    "s_lshl_b32 s89, s89, 10\n"
    "v_min_u32 v143, 7, v255\n"
    "v_lshlrev_b32 v143, 7, v143\n"
    "s_mov_b32 s44, s24\n"
    "s_and_b32 s45, s25, 0xffff\n"
    "s_mov_b32 s46, 0x900000\n"
    "s_mov_b32 s47, 0x20000\n"
    "s_lshl_b32 s72, s68, 3\n"
    "s_add_u32 s72, s72, s69\n"
    "s_lshl_b32 s72, s72, 10\n"
    "s_cmp_eq_u32 s70, 0\n"
    "s_cselect_b32 s73, 1, 0\n"
    "s_cselect_b32 s72, s72, 0x40000000\n"
    "v_and_b32 v156, 7, v255\n"
    "v_lshl_add_u32 v156, v156, 3, s72\n"
    "v_mov_b32 v162, 0x40000000\n"
    "v_cmp_gt_u32 vcc, s73, v255\n"
    "s_nop 1\n"
    "v_cndmask_b32 v162, v162, v156, vcc\n"
    "v_mov_b32 v148, 0\n"
    "v_mov_b32 v149, 0\n"
    "v_mov_b32 v150, 0\n"
    "v_mov_b32 v151, 0\n"
    "v_mov_b32 v152, 0\n"
    "v_mov_b32 v153, 0\n"
    "v_mov_b32 v154, 0\n"
    "v_mov_b32 v155, 0\n"
    "v_mov_b32 v8, 0x3f803f80\n"
    "v_mov_b32 v9, 0x3f803f80\n"
    "v_mov_b32 v10, 0x3f803f80\n"
    "v_mov_b32 v11, 0x3f803f80\n"
    "v_mov_b32 v12, 0x3f803f80\n"
    "v_mov_b32 v13, 0x3f803f80\n"
    "v_mov_b32 v14, 0x3f803f80\n"
    "v_mov_b32 v15, 0x3f803f80\n"
    "v_mov_b32 v16, 0x3f803f80\n"
    "v_mov_b32 v17, 0x3f803f80\n"
    "v_mov_b32 v18, 0x3f803f80\n"
    "v_mov_b32 v19, 0x3f803f80\n"
    "v_mov_b32 v20, 0x3f803f80\n"
    "v_mov_b32 v21, 0x3f803f80\n"
    "v_mov_b32 v22, 0x3f803f80\n"
    "v_mov_b32 v23, 0x3f803f80\n"
    "v_mov_b32 v24, 0x3f803f80\n"
    "v_mov_b32 v25, 0x3f803f80\n"
    "v_mov_b32 v26, 0x3f803f80\n"
    "v_mov_b32 v27, 0x3f803f80\n"
    "v_mov_b32 v28, 0x3f803f80\n"
    "v_mov_b32 v29, 0x3f803f80\n"
    "v_mov_b32 v30, 0x3f803f80\n"
    "v_mov_b32 v31, 0x3f803f80\n"
    "v_mov_b32 v32, 0x3f803f80\n"
    "v_mov_b32 v33, 0x3f803f80\n"
    "v_mov_b32 v34, 0x3f803f80\n"
    "v_mov_b32 v35, 0x3f803f80\n"
    "v_mov_b32 v36, 0x3f803f80\n"
    "v_mov_b32 v37, 0x3f803f80\n"
    "v_mov_b32 v38, 0x3f803f80\n"
    "v_mov_b32 v39, 0x3f803f80\n"
    "v_mov_b32 v40, 0x3f803f80\n"
    "v_mov_b32 v41, 0x3f803f80\n"
    "v_mov_b32 v42, 0x3f803f80\n"
    "v_mov_b32 v43, 0x3f803f80\n"
    "v_mov_b32 v44, 0x3f803f80\n"
    "v_mov_b32 v45, 0x3f803f80\n"
    "v_mov_b32 v46, 0x3f803f80\n"
    "v_mov_b32 v47, 0x3f803f80\n"
    "v_mov_b32 v48, 0x3f803f80\n"
    "v_mov_b32 v49, 0x3f803f80\n"
    "v_mov_b32 v50, 0x3f803f80\n"
    "v_mov_b32 v51, 0x3f803f80\n"
    "v_mov_b32 v52, 0x3f803f80\n"
    "v_mov_b32 v53, 0x3f803f80\n"
    "v_mov_b32 v54, 0x3f803f80\n"
    "v_mov_b32 v55, 0x3f803f80\n"
    "v_mov_b32 v56, 0x3f803f80\n"
    "v_mov_b32 v57, 0x3f803f80\n"
    "v_mov_b32 v58, 0x3f803f80\n"
    "v_mov_b32 v59, 0x3f803f80\n"
    "v_mov_b32 v60, 0x3f803f80\n"
    "v_mov_b32 v61, 0x3f803f80\n"
    "v_mov_b32 v62, 0x3f803f80\n"
    "v_mov_b32 v63, 0x3f803f80\n"
    "s_add_u32 m0, s83, 0x20800\n"
    "s_add_u32 s84, s60, 0x80\n"
    "s_mov_b32 exec_hi, 0\n"
    "buffer_load_dwordx4 v106, s[28:31], s84 offen offset:0 lds\n"
    "s_mov_b32 exec_hi, -1\n"
    "s_add_u32 m0, s82, 0x6000\n"
    "s_add_u32 s84, s56, 0x30400\n"
    "buffer_load_dwordx4 v100, s[32:35], s84 offen offset:0 lds\n"
    "buffer_load_dwordx4 v101, s[32:35], s84 offen offset:1024 lds\n"
    "s_waitcnt vmcnt(9)\n"
    "s_waitcnt lgkmcnt(0)\n"
    "s_barrier\n"
    "s_add_u32 m0, s81, 0xc000\n"
    "s_add_u32 s84, s53, 0x0\n"
    "buffer_load_dwordx4 v96, s[28:31], s84 offen offset:0 lds\n"
    "buffer_load_dwordx4 v97, s[28:31], s84 offen offset:1024 lds\n"
    "buffer_load_dwordx4 v98, s[28:31], s84 offen offset:2048 lds\n"
    "s_mov_b32 exec_lo, 0xfffefffe\n"
    "s_mov_b32 exec_hi, 0xfffefffe\n"
    "ds_read_b128 v[0:3], v114 offset:0\n"
    "ds_read_b128 v[4:7], v115 offset:0\n"
    "s_mov_b64 exec, -1\n"
    "ds_read_b64_tr_b16 v[64:65], v108 offset:16384\n"
    "ds_read_b64_tr_b16 v[66:67], v108 offset:16896\n"
    "buffer_load_dwordx4 v99, s[28:31], s84 offen offset:3072 lds\n"
    "ds_read_b64_tr_b16 v[68:69], v108 offset:20480\n"
    "ds_read_b64_tr_b16 v[70:71], v108 offset:20992\n"
    "s_add_u32 m0, s83, 0x21000\n"
    "s_add_u32 s84, s61, 0x0\n"
    "s_mov_b32 exec_hi, 0\n"
    "buffer_load_dwordx4 v106, s[28:31], s84 offen offset:0 lds\n"
    "s_mov_b32 exec_hi, -1\n"
    "s_waitcnt lgkmcnt(5)\n"
    "v_mfma_f32_16x16x32_bf16 v[168:171], v[0:3], v[0:3], 0\n"
    "s_waitcnt lgkmcnt(4)\n"
    "v_mfma_f32_16x16x32_bf16 v[168:171], v[4:7], v[4:7], v[168:171]\n"
    "s_waitcnt lgkmcnt(2)\n"
    "v_mfma_f32_16x16x32_bf16 v[116:119], v[124:127], v[64:67], 0\n"
    "v_mfma_f32_16x16x32_bf16 v[120:123], v[64:67], v[0:3], 0\n"
    "s_waitcnt lgkmcnt(0)\n"
    "v_mfma_f32_16x16x32_bf16 v[116:119], v[124:127], v[68:71], v[116:119]\n"
    "v_mfma_f32_16x16x32_bf16 v[120:123], v[68:71], v[4:7], v[120:123]\n"
    "s_add_u32 m0, s82, 0x10000\n"
    "s_add_u32 s84, s57, 0x400\n"
    "buffer_load_dwordx4 v100, s[32:35], s84 offen offset:0 lds\n"
    "buffer_load_dwordx4 v101, s[32:35], s84 offen offset:1024 lds\n"
    "s_add_u32 m0, s81, 0x14000\n"
    "s_add_u32 s84, s53, 0x80\n"
    "buffer_load_dwordx4 v96, s[28:31], s84 offen offset:0 lds\n"
    "buffer_load_dwordx4 v97, s[28:31], s84 offen offset:1024 lds\n"
    "buffer_load_dwordx4 v98, s[28:31], s84 offen offset:2048 lds\n"
    "buffer_load_dwordx4 v99, s[28:31], s84 offen offset:3072 lds\n"
    "s_add_u32 m0, s83, 0x21800\n"
    "s_add_u32 s84, s61, 0x80\n"
    "s_mov_b32 exec_hi, 0\n"
    "buffer_load_dwordx4 v106, s[28:31], s84 offen offset:0 lds\n"
    "s_mov_b32 exec_hi, -1\n"
    "s_waitcnt vmcnt(12)\n"
    "s_barrier\n"
    "s_add_u32 m0, s82, 0x12000\n"
    "s_add_u32 s84, s57, 0x30400\n"
    "buffer_load_dwordx4 v100, s[32:35], s84 offen offset:0 lds\n"
    "buffer_load_dwordx4 v101, s[32:35], s84 offen offset:1024 lds\n"
    "s_add_u32 m0, s81, 0x18000\n"
    "s_add_u32 s84, s54, 0x0\n"
    "buffer_load_dwordx4 v96, s[28:31], s84 offen offset:0 lds\n"
    "s_mov_b32 exec_lo, 0xfffefffe\n"
    "s_mov_b32 exec_hi, 0xfffefffe\n"
    "ds_read_b128 v[8:11], v114 offset:2048\n"
    "ds_read_b128 v[12:15], v115 offset:2048\n"
    "s_mov_b64 exec, -1\n"
    "ds_read_b64_tr_b16 v[72:73], v108 offset:24576\n"
    "ds_read_b64_tr_b16 v[74:75], v108 offset:25088\n"
    "buffer_load_dwordx4 v97, s[28:31], s84 offen offset:1024 lds\n"
    "ds_read_b64_tr_b16 v[76:77], v108 offset:28672\n"
    "ds_read_b64_tr_b16 v[78:79], v108 offset:29184\n"
    "buffer_load_dwordx4 v98, s[28:31], s84 offen offset:2048 lds\n"
    "s_waitcnt lgkmcnt(5)\n"
    "v_mfma_f32_16x16x32_bf16 v[168:171], v[8:11], v[8:11], v[168:171]\n"
    "s_waitcnt lgkmcnt(4)\n"
    "v_mfma_f32_16x16x32_bf16 v[168:171], v[12:15], v[12:15], v[168:171]\n"
    "s_waitcnt lgkmcnt(2)\n"
    "v_mfma_f32_16x16x32_bf16 v[116:119], v[124:127], v[72:75], v[116:119]\n"
    "v_mfma_f32_16x16x32_bf16 v[120:123], v[72:75], v[8:11], v[120:123]\n"
    "s_waitcnt lgkmcnt(0)\n"
    "v_mfma_f32_16x16x32_bf16 v[116:119], v[124:127], v[76:79], v[116:119]\n"
    "v_mfma_f32_16x16x32_bf16 v[120:123], v[76:79], v[12:15], v[120:123]\n"
    "buffer_load_dwordx4 v99, s[28:31], s84 offen offset:3072 lds\n"
    "s_add_u32 m0, s83, 0x22000\n"
    "s_add_u32 s84, s62, 0x0\n"
    "s_mov_b32 exec_hi, 0\n"
    "buffer_load_dwordx4 v106, s[28:31], s84 offen offset:0 lds\n"
    "s_mov_b32 exec_hi, -1\n"
    "s_waitcnt vmcnt(12)\n"
    "s_barrier\n"
    "s_add_u32 m0, s82, 0x1c000\n"
    "s_add_u32 s84, s58, 0x400\n"
    "buffer_load_dwordx4 v100, s[32:35], s84 offen offset:0 lds\n"
    "buffer_load_dwordx4 v101, s[32:35], s84 offen offset:1024 lds\n"
    "s_add_u32 m0, s81, 0x0\n"
    "s_add_u32 s84, s54, 0x80\n"
    "buffer_load_dwordx4 v96, s[28:31], s84 offen offset:0 lds\n"
    "s_mov_b32 exec_lo, 0xfffefffe\n"
    "s_mov_b32 exec_hi, 0xfffefffe\n"
    "ds_read_b128 v[16:19], v114 offset:4096\n"
    "ds_read_b128 v[20:23], v115 offset:4096\n"
    "s_mov_b64 exec, -1\n"
    "ds_read_b64_tr_b16 v[64:65], v109 offset:0\n"
    "ds_read_b64_tr_b16 v[66:67], v109 offset:512\n"
    "buffer_load_dwordx4 v97, s[28:31], s84 offen offset:1024 lds\n"
    "ds_read_b64_tr_b16 v[68:69], v109 offset:4096\n"
    "ds_read_b64_tr_b16 v[70:71], v109 offset:4608\n"
    "buffer_load_dwordx4 v98, s[28:31], s84 offen offset:2048 lds\n"
    "s_waitcnt lgkmcnt(5)\n"
    "v_mfma_f32_16x16x32_bf16 v[168:171], v[16:19], v[16:19], v[168:171]\n"
    "s_waitcnt lgkmcnt(4)\n"
    "v_mfma_f32_16x16x32_bf16 v[168:171], v[20:23], v[20:23], v[168:171]\n"
    "s_waitcnt lgkmcnt(2)\n"
    "v_mfma_f32_16x16x32_bf16 v[116:119], v[124:127], v[64:67], v[116:119]\n"
    "v_mfma_f32_16x16x32_bf16 v[120:123], v[64:67], v[16:19], v[120:123]\n"
    "s_waitcnt lgkmcnt(0)\n"
    "v_mfma_f32_16x16x32_bf16 v[116:119], v[124:127], v[68:71], v[116:119]\n"
    "v_mfma_f32_16x16x32_bf16 v[120:123], v[68:71], v[20:23], v[120:123]\n"
    "buffer_load_dwordx4 v99, s[28:31], s84 offen offset:3072 lds\n"
    "s_add_u32 m0, s83, 0x22800\n"
    "s_add_u32 s84, s62, 0x80\n"
    "s_mov_b32 exec_hi, 0\n"
    "buffer_load_dwordx4 v106, s[28:31], s84 offen offset:0 lds\n"
    "s_mov_b32 exec_hi, -1\n"
    "s_lshl_b32 s92, s65, 2\n"
    "s_add_u32 s92, s92, s70\n"
    "s_mul_i32 s92, s92, 24576\n"
    "v_and_b32 v157, 7, v255\n"
    "v_mul_u32_u24 v157, 3072, v157\n"
    "v_lshrrev_b32 v142, 3, v255\n"
    "v_min_u32 v142, 1, v142\n"
    "v_lshl_add_u32 v157, v142, 11, v157\n"
    "s_lshl_b32 s93, s66, 7\n"
    "s_add_u32 s92, s92, s93\n"
    "buffer_load_dword v163, v157, s[32:35], s92 offen\n"
    "s_waitcnt vmcnt(13)\n"
    "s_barrier\n"
    "s_add_u32 m0, s82, 0x1e000\n"
    "s_add_u32 s84, s58, 0x30400\n"
    "buffer_load_dwordx4 v100, s[32:35], s84 offen offset:0 lds\n"
    "buffer_load_dwordx4 v101, s[32:35], s84 offen offset:1024 lds\n"
    "s_add_u32 m0, s81, 0x4000\n"
    "s_add_u32 s84, s55, 0x0\n"
    "buffer_load_dwordx4 v96, s[28:31], s84 offen offset:0 lds\n"
    "s_mov_b32 exec_lo, 0xfffefffe\n"
    "s_mov_b32 exec_hi, 0xfffefffe\n"
    "ds_read_b128 v[24:27], v114 offset:6144\n"
    "ds_read_b128 v[28:31], v115 offset:6144\n"
    "s_mov_b64 exec, -1\n"
    "ds_read_b64_tr_b16 v[72:73], v109 offset:8192\n"
    "ds_read_b64_tr_b16 v[74:75], v109 offset:8704\n"
    "buffer_load_dwordx4 v97, s[28:31], s84 offen offset:1024 lds\n"
    "ds_read_b64_tr_b16 v[76:77], v109 offset:12288\n"
    "ds_read_b64_tr_b16 v[78:79], v109 offset:12800\n"
    "buffer_load_dwordx4 v98, s[28:31], s84 offen offset:2048 lds\n"
    "s_waitcnt lgkmcnt(5)\n"
    "v_mfma_f32_16x16x32_bf16 v[168:171], v[24:27], v[24:27], v[168:171]\n"
    "s_waitcnt lgkmcnt(4)\n"
    "v_mfma_f32_16x16x32_bf16 v[168:171], v[28:31], v[28:31], v[168:171]\n"
    "s_waitcnt lgkmcnt(2)\n"
    "v_mfma_f32_16x16x32_bf16 v[116:119], v[124:127], v[72:75], v[116:119]\n"
    "v_mfma_f32_16x16x32_bf16 v[120:123], v[72:75], v[24:27], v[120:123]\n"
    "s_waitcnt lgkmcnt(0)\n"
    "v_mfma_f32_16x16x32_bf16 v[116:119], v[124:127], v[76:79], v[116:119]\n"
    "v_mfma_f32_16x16x32_bf16 v[120:123], v[76:79], v[28:31], v[120:123]\n"
    "buffer_load_dwordx4 v99, s[28:31], s84 offen offset:3072 lds\n"
    "s_add_u32 m0, s83, 0x23000\n"
    "s_add_u32 s84, s63, 0x0\n"
    "s_mov_b32 exec_hi, 0\n"
    "buffer_load_dwordx4 v106, s[28:31], s84 offen offset:0 lds\n"
    "s_mov_b32 exec_hi, -1\n"
    "s_waitcnt vmcnt(13)\n"
    "s_barrier\n"
    "s_add_u32 m0, s82, 0x8000\n"
    "s_add_u32 s84, s59, 0x400\n"
    "buffer_load_dwordx4 v100, s[32:35], s84 offen offset:0 lds\n"
    "buffer_load_dwordx4 v101, s[32:35], s84 offen offset:1024 lds\n"
    "s_add_u32 m0, s81, 0xc000\n"
    "s_add_u32 s84, s55, 0x80\n"
    "buffer_load_dwordx4 v96, s[28:31], s84 offen offset:0 lds\n"
    "s_mov_b32 exec_lo, 0xfffefffe\n"
    "s_mov_b32 exec_hi, 0xfffefffe\n"
    "ds_read_b128 v[32:35], v114 offset:8192\n"
    "ds_read_b128 v[36:39], v115 offset:8192\n"
    "s_mov_b64 exec, -1\n"
    "ds_read_b64_tr_b16 v[64:65], v109 offset:49152\n"
    "ds_read_b64_tr_b16 v[66:67], v109 offset:49664\n"
    "buffer_load_dwordx4 v97, s[28:31], s84 offen offset:1024 lds\n"
    "ds_read_b64_tr_b16 v[68:69], v109 offset:53248\n"
    "ds_read_b64_tr_b16 v[70:71], v109 offset:53760\n"
    "buffer_load_dwordx4 v98, s[28:31], s84 offen offset:2048 lds\n"
    "s_waitcnt lgkmcnt(5)\n"
    "v_mfma_f32_16x16x32_bf16 v[168:171], v[32:35], v[32:35], v[168:171]\n"
    "s_waitcnt lgkmcnt(4)\n"
    "v_mfma_f32_16x16x32_bf16 v[168:171], v[36:39], v[36:39], v[168:171]\n"
    "s_waitcnt lgkmcnt(2)\n"
    "v_mfma_f32_16x16x32_bf16 v[116:119], v[124:127], v[64:67], v[116:119]\n"
    "v_mfma_f32_16x16x32_bf16 v[120:123], v[64:67], v[32:35], v[120:123]\n"
    "s_waitcnt lgkmcnt(0)\n"
    "v_mfma_f32_16x16x32_bf16 v[116:119], v[124:127], v[68:71], v[116:119]\n"
    "v_mfma_f32_16x16x32_bf16 v[120:123], v[68:71], v[36:39], v[120:123]\n"
    "buffer_load_dwordx4 v99, s[28:31], s84 offen offset:3072 lds\n"
    "s_add_u32 m0, s83, 0x23800\n"
    "s_add_u32 s84, s63, 0x80\n"
    "s_mov_b32 exec_hi, 0\n"
    "buffer_load_dwordx4 v106, s[28:31], s84 offen offset:0 lds\n"
    "s_mov_b32 exec_hi, -1\n"
    "s_waitcnt vmcnt(12)\n"
    "s_barrier\n"
    "s_add_u32 m0, s82, 0xa000\n"
    "s_add_u32 s84, s59, 0x30400\n"
    "buffer_load_dwordx4 v100, s[32:35], s84 offen offset:0 lds\n"
    "buffer_load_dwordx4 v101, s[32:35], s84 offen offset:1024 lds\n"
    "s_add_u32 m0, s82, 0x10000\n"
    "s_add_u32 s84, s56, 0x0\n"
    "buffer_load_dwordx4 v100, s[32:35], s84 offen offset:0 lds\n"
    "s_mov_b32 exec_lo, 0xfffefffe\n"
    "s_mov_b32 exec_hi, 0xfffefffe\n"
    "ds_read_b128 v[40:43], v114 offset:10240\n"
    "ds_read_b128 v[44:47], v115 offset:10240\n"
    "s_mov_b64 exec, -1\n"
    "ds_read_b64_tr_b16 v[72:73], v109 offset:57344\n"
    "ds_read_b64_tr_b16 v[74:75], v109 offset:57856\n"
    "buffer_load_dwordx4 v101, s[32:35], s84 offen offset:1024 lds\n"
    "ds_read_b64_tr_b16 v[76:77], v109 offset:61440\n"
    "ds_read_b64_tr_b16 v[78:79], v109 offset:61952\n"
    "s_add_u32 m0, s82, 0x12000\n"
    "s_add_u32 s84, s56, 0x30000\n"
    "buffer_load_dwordx4 v100, s[32:35], s84 offen offset:0 lds\n"
    "s_waitcnt lgkmcnt(5)\n"
    "v_mfma_f32_16x16x32_bf16 v[168:171], v[40:43], v[40:43], v[168:171]\n"
    "s_waitcnt lgkmcnt(4)\n"
    "v_mfma_f32_16x16x32_bf16 v[168:171], v[44:47], v[44:47], v[168:171]\n"
    "s_waitcnt lgkmcnt(2)\n"
    "v_mfma_f32_16x16x32_bf16 v[116:119], v[124:127], v[72:75], v[116:119]\n"
    "v_mfma_f32_16x16x32_bf16 v[120:123], v[72:75], v[40:43], v[120:123]\n"
    "s_waitcnt lgkmcnt(0)\n"
    "v_mfma_f32_16x16x32_bf16 v[116:119], v[124:127], v[76:79], v[116:119]\n"
    "v_mfma_f32_16x16x32_bf16 v[120:123], v[76:79], v[44:47], v[120:123]\n"
    "buffer_load_dwordx4 v101, s[32:35], s84 offen offset:1024 lds\n"
    "s_mul_i32 s96, s96, 0x9e3779b1\n"
    "s_mul_i32 s97, s97, 0x85ebca6d\n"
    "s_add_u32 s96, s96, 0x7f4a7c15\n"
    "s_add_u32 s97, s97, 0x165667b1\n"
    "s_mov_b32 s74, 0x9e3779b1\n"
    "s_mov_b32 s75, 0x85ebca6d\n"
    "s_add_u32 m0, s82, 0x14000\n"
    "s_add_u32 s84, s57, 0x0\n"
    "buffer_load_dwordx4 v100, s[32:35], s84 offen offset:0 lds\n"
    "v_mul_lo_u32 v164, v164, s74\n"
    "v_mul_lo_u32 v165, v165, s75\n"
    "v_add_u32 v164, 0x7f4a7c15, v164\n"
    "v_add_u32 v165, 0x165667b1, v165\n"
    "buffer_load_dwordx4 v101, s[32:35], s84 offen offset:1024 lds\n"
    "s_waitcnt vmcnt(13)\n"
    "s_barrier\n"
    "s_add_u32 m0, s82, 0x16000\n"
    "s_add_u32 s84, s57, 0x30000\n"
    "buffer_load_dwordx4 v100, s[32:35], s84 offen offset:0 lds\n"
    "buffer_load_dwordx4 v101, s[32:35], s84 offen offset:1024 lds\n"
    "s_add_u32 m0, s82, 0x18000\n"
    "s_add_u32 s84, s58, 0x0\n"
    "buffer_load_dwordx4 v100, s[32:35], s84 offen offset:0 lds\n"
    "s_mov_b32 exec_lo, 0xfffefffe\n"
    "s_mov_b32 exec_hi, 0xfffefffe\n"
    "ds_read_b128 v[48:51], v114 offset:12288\n"
    "ds_read_b128 v[52:55], v115 offset:12288\n"
    "s_mov_b64 exec, -1\n"
    "ds_read_b64_tr_b16 v[64:65], v108 offset:32768\n"
    "ds_read_b64_tr_b16 v[66:67], v108 offset:33280\n"
    "buffer_load_dwordx4 v101, s[32:35], s84 offen offset:1024 lds\n"
    "ds_read_b64_tr_b16 v[68:69], v108 offset:36864\n"
    "ds_read_b64_tr_b16 v[70:71], v108 offset:37376\n"
    "s_add_u32 m0, s82, 0x1a000\n"
    "s_add_u32 s84, s58, 0x30000\n"
    "buffer_load_dwordx4 v100, s[32:35], s84 offen offset:0 lds\n"
    "s_waitcnt lgkmcnt(5)\n"
    "v_mfma_f32_16x16x32_bf16 v[168:171], v[48:51], v[48:51], v[168:171]\n"
    "s_waitcnt lgkmcnt(4)\n"
    "v_mfma_f32_16x16x32_bf16 v[168:171], v[52:55], v[52:55], v[168:171]\n"
    "s_waitcnt lgkmcnt(2)\n"
    "v_mfma_f32_16x16x32_bf16 v[116:119], v[124:127], v[64:67], v[116:119]\n"
    "v_mfma_f32_16x16x32_bf16 v[120:123], v[64:67], v[48:51], v[120:123]\n"
    "s_waitcnt lgkmcnt(0)\n"
    "v_mfma_f32_16x16x32_bf16 v[116:119], v[124:127], v[68:71], v[116:119]\n"
    "v_mfma_f32_16x16x32_bf16 v[120:123], v[68:71], v[52:55], v[120:123]\n"
    "buffer_load_dwordx4 v101, s[32:35], s84 offen offset:1024 lds\n"
    "s_add_u32 m0, s82, 0x0\n"
    "s_add_u32 s84, s59, 0x0\n"
    "buffer_load_dwordx4 v100, s[32:35], s84 offen offset:0 lds\n"
    "buffer_load_dwordx4 v101, s[32:35], s84 offen offset:1024 lds\n"
    "s_waitcnt vmcnt(14)\n"
    "s_barrier\n"
    "s_lshl_b32 s92, s71, 2\n"
    "s_add_u32 s92, s92, s70\n"
    "v_min_u32 v157, 13, v255\n"
    "v_lshlrev_b32 v157, 7, v157\n"
    "s_mul_i32 s93, s92, 1792\n"
    "buffer_load_dword v163, v157, s[36:39], s93 offen\n"
    "v_min_u32 v157, 0x180, v157\n"
    "s_lshl_b32 s93, s92, 9\n"
    "buffer_load_dword v163, v157, s[40:43], s93 offen\n"
    "s_mov_b32 exec_lo, 0xfffefffe\n"
    "s_mov_b32 exec_hi, 0xfffefffe\n"
    "ds_read_b128 v[56:59], v114 offset:14336\n"
    "ds_read_b128 v[60:63], v115 offset:14336\n"
    "s_mov_b64 exec, -1\n"
    "ds_read_b64_tr_b16 v[72:73], v108 offset:40960\n"
    "ds_read_b64_tr_b16 v[74:75], v108 offset:41472\n"
    "ds_read_b64_tr_b16 v[76:77], v108 offset:45056\n"
    "ds_read_b64_tr_b16 v[78:79], v108 offset:45568\n"
    "s_waitcnt lgkmcnt(5)\n"
    "v_mfma_f32_16x16x32_bf16 v[168:171], v[56:59], v[56:59], v[168:171]\n"
    "s_waitcnt lgkmcnt(4)\n"
    "v_mfma_f32_16x16x32_bf16 v[168:171], v[60:63], v[60:63], v[168:171]\n"
    "s_waitcnt lgkmcnt(2)\n"
    "v_mfma_f32_16x16x32_bf16 v[116:119], v[124:127], v[72:75], v[116:119]\n"
    "v_mfma_f32_16x16x32_bf16 v[120:123], v[72:75], v[56:59], v[120:123]\n"
    "s_waitcnt lgkmcnt(0)\n"
    "v_mfma_f32_16x16x32_bf16 v[116:119], v[124:127], v[76:79], v[116:119]\n"
    "v_mfma_f32_16x16x32_bf16 v[120:123], v[76:79], v[60:63], v[120:123]\n"
    "s_nop 6\n"
    "v_cvt_pk_bf16_f32 v134, v116, v116\n"
    "v_lshlrev_b32 v134, 16, v134\n"
    "v_sub_f32 v134, v116, v134\n"
    "v_cvt_pk_bf16_f32 v148, v116, v134\n"
    "v_and_b32 v135, 0xffff0000, v148\n"
    "v_sub_f32 v134, v134, v135\n"
    "v_cvt_pk_bf16_f32 v149, 0, v134\n"
    "ds_write_b64 v132, v[148:149] offset:0\n"
    "s_waitcnt vmcnt(12)\n"
    "s_waitcnt lgkmcnt(0)\n"
    "s_barrier\n"
    "s_add_u32 m0, s82, 0x2000\n"
    "s_add_u32 s84, s59, 0x30000\n"
    "buffer_load_dwordx4 v100, s[32:35], s84 offen offset:0 lds\n"
    "buffer_load_dwordx4 v101, s[32:35], s84 offen offset:1024 lds\n"
    "s_add_u32 m0, s82, 0x1c000\n"
    "s_add_u32 s84, s56, 0x800\n"
    "buffer_load_dwordx4 v100, s[32:35], s84 offen offset:0 lds\n"
    "ds_read_b64_tr_b16 v[64:65], v109 offset:0\n"
    "ds_read_b64_tr_b16 v[66:67], v109 offset:512\n"
    "ds_read_b64_tr_b16 v[68:69], v109 offset:4096\n"
    "buffer_load_dwordx4 v101, s[32:35], s84 offen offset:1024 lds\n"
    "ds_read_b64_tr_b16 v[70:71], v109 offset:4608\n"
    "ds_read_b64_tr_b16 v[72:73], v109 offset:8192\n"
    "ds_read_b64_tr_b16 v[74:75], v109 offset:8704\n"
    "s_add_u32 m0, s82, 0x1e000\n"
    "s_add_u32 s84, s56, 0x30800\n"
    "buffer_load_dwordx4 v100, s[32:35], s84 offen offset:0 lds\n"
    "ds_read_b64_tr_b16 v[76:77], v109 offset:12288\n"
    "ds_read_b64_tr_b16 v[78:79], v109 offset:12800\n"
    "s_waitcnt lgkmcnt(6)\n"
    "v_mfma_f32_16x16x32_bf16 v[116:119], v[124:127], v[64:67], 0\n"
    "buffer_load_dwordx4 v101, s[32:35], s84 offen offset:1024 lds\n"
    "s_waitcnt lgkmcnt(4)\n"
    "v_mfma_f32_16x16x32_bf16 v[116:119], v[124:127], v[68:71], v[116:119]\n"
    "s_waitcnt lgkmcnt(2)\n"
    "v_mfma_f32_16x16x32_bf16 v[116:119], v[124:127], v[72:75], v[116:119]\n"
    "s_waitcnt lgkmcnt(0)\n"
    "v_mfma_f32_16x16x32_bf16 v[116:119], v[124:127], v[76:79], v[116:119]\n"
    "v_cmp_gt_u32 vcc, 16, v255\n"
    "v_and_b32 v134, 15, v255\n"
    "v_lshrrev_b32 v135, 4, v255\n"
    "v_cndmask_b32 v144, 0, v168, vcc\n"
    "v_lshlrev_b32 v135, 2, v135\n"
    "v_sub_u32 v134, v134, v135\n"
    "s_add_u32 m0, s82, 0x4000\n"
    "s_add_u32 s84, s57, 0x800\n"
    "buffer_load_dwordx4 v100, s[32:35], s84 offen offset:0 lds\n"
    "v_mov_b32 v145, 0\n"
    "v_cmp_eq_u32 vcc, 0, v134\n"
    "s_nop 1\n"
    "v_cndmask_b32 v145, v145, v168, vcc\n"
    "v_cmp_eq_u32 vcc, 1, v134\n"
    "s_nop 1\n"
    "v_cndmask_b32 v145, v145, v169, vcc\n"
    "v_cmp_eq_u32 vcc, 2, v134\n"
    "s_nop 1\n"
    "v_cndmask_b32 v145, v145, v170, vcc\n"
    "v_cmp_eq_u32 vcc, 3, v134\n"
    "s_nop 1\n"
    "v_cndmask_b32 v145, v145, v171, vcc\n"
    "buffer_load_dwordx4 v101, s[32:35], s84 offen offset:1024 lds\n"
    "v_mov_b32 v134, v144\n"
    "v_mov_b32 v135, v145\n"
    "s_nop 0\n"
    "v_permlane16_swap_b32 v144, v134\n"
    "v_permlane16_swap_b32 v145, v135\n"
    "v_add_f32 v144, v144, v134\n"
    "v_add_f32 v145, v145, v135\n"
    "v_mov_b32 v134, v144\n"
    "v_mov_b32 v135, v145\n"
    "s_nop 0\n"
    "v_permlane32_swap_b32 v144, v134\n"
    "s_add_u32 m0, s82, 0x6000\n"
    "s_add_u32 s84, s57, 0x30800\n"
    "buffer_load_dwordx4 v100, s[32:35], s84 offen offset:0 lds\n"
    "v_permlane32_swap_b32 v145, v135\n"
    "v_add_f32 v144, v144, v134\n"
    "v_add_f32 v145, v145, v135\n"
    "v_and_b32 v176, 7, v255\n"
    "v_lshrrev_b32 v177, 4, v255\n"
    "v_bfe_u32 v178, v255, 3, 1\n"
    "v_and_b32 v179, 15, v255\n"
    "v_mov_b32 v180, 0xf149f2ca\n"
    "v_lshlrev_b32 v181, 2, v177\n"
    "buffer_load_dwordx4 v101, s[32:35], s84 offen offset:1024 lds\n"
    "v_sub_u32 v182, v181, v176\n"
    "s_lshr_b32 s72, s70, 1\n"
    "s_add_u32 s72, s72, s65\n"
    "s_cmp_eq_u32 s72, 0\n"
    "s_cselect_b32 s73, 8, 0\n"
    "v_add_u32 v183, -1, v182\n"
    "v_cmp_gt_u32 vcc, 8, v183\n"
    "s_nop 1\n"
    "v_cndmask_b32 v244, v180, 0, vcc\n"
    "v_add_u32 v183, 0, v182\n"
    "v_cmp_gt_u32 vcc, 8, v183\n"
    "s_nop 1\n"
    "v_cndmask_b32 v245, v180, 0, vcc\n"
    "v_add_u32 v183, 1, v182\n"
    "v_cmp_gt_u32 vcc, 8, v183\n"
    "s_nop 1\n"
    "v_cndmask_b32 v246, v180, 0, vcc\n"
    "v_add_u32 v183, 2, v182\n"
    "v_cmp_gt_u32 vcc, 8, v183\n"
    "s_nop 1\n"
    "v_cndmask_b32 v247, v180, 0, vcc\n"
    "v_cmp_gt_u32 vcc, s73, v181\n"
    "s_nop 1\n"
    "v_cndmask_b32 v240, v244, v180, vcc\n"
    "v_cndmask_b32 v241, v245, v180, vcc\n"
    "v_cndmask_b32 v242, v246, v180, vcc\n"
    "v_cndmask_b32 v243, v247, v180, vcc\n"
    "s_waitcnt vmcnt(18)\n"
    "s_barrier\n"
    "s_add_u32 s92, s89, 0x10000\n"
    "buffer_load_dword v163, v143, s[36:39], s92 offen\n"
    "s_add_u32 m0, s82, 0x8000\n"
    "s_add_u32 s84, s58, 0x800\n"
    "buffer_load_dwordx4 v100, s[32:35], s84 offen offset:0 lds\n"
    "buffer_load_dwordx4 v101, s[32:35], s84 offen offset:1024 lds\n"
    "ds_read_b64_tr_b16 v[64:65], v109 offset:16384\n"
    "ds_read_b64_tr_b16 v[66:67], v109 offset:16896\n"
    "ds_read_b64_tr_b16 v[68:69], v109 offset:20480\n"
    "s_add_u32 m0, s82, 0xa000\n"
    "s_add_u32 s84, s58, 0x30800\n"
    "buffer_load_dwordx4 v100, s[32:35], s84 offen offset:0 lds\n"
    "ds_read_b64_tr_b16 v[70:71], v109 offset:20992\n"
    "ds_read_b64_tr_b16 v[72:73], v109 offset:24576\n"
    "ds_read_b64_tr_b16 v[74:75], v109 offset:25088\n"
    "buffer_load_dwordx4 v101, s[32:35], s84 offen offset:1024 lds\n"
    "ds_read_b64_tr_b16 v[76:77], v109 offset:28672\n"
    "ds_read_b64_tr_b16 v[78:79], v109 offset:29184\n"
    "s_waitcnt lgkmcnt(6)\n"
    "v_mfma_f32_16x16x32_bf16 v[116:119], v[124:127], v[64:67], v[116:119]\n"
    "s_add_u32 m0, s82, 0xc000\n"
    "s_add_u32 s84, s59, 0x800\n"
    "buffer_load_dwordx4 v100, s[32:35], s84 offen offset:0 lds\n"
    "s_waitcnt lgkmcnt(4)\n"
    "v_mfma_f32_16x16x32_bf16 v[116:119], v[124:127], v[68:71], v[116:119]\n"
    "s_waitcnt lgkmcnt(2)\n"
    "v_mfma_f32_16x16x32_bf16 v[116:119], v[124:127], v[72:75], v[116:119]\n"
    "s_waitcnt lgkmcnt(0)\n"
    "v_mfma_f32_16x16x32_bf16 v[116:119], v[124:127], v[76:79], v[116:119]\n"
    "v_cmp_gt_u32 vcc, 16, v255\n"
    "s_nop 1\n"
    "v_cndmask_b32 v138, 0, -1, vcc\n"
    "v_mul_f32 v144, 0x3b000000, v144\n"
    "v_mul_f32 v134, v144, v144\n"
    "buffer_load_dwordx4 v101, s[32:35], s84 offen offset:1024 lds\n"
    "v_mul_f32 v145, 0x3b000000, v145\n"
    "v_sub_f32 v145, v145, v134\n"
    "v_max_f32 v145, 0, v145\n"
    "v_cvt_pk_bf16_f32 v134, v145, v145\n"
    "v_lshlrev_b32 v134, 16, v134\n"
    "v_add_f32 v134, 0x3727c5ac, v134\n"
    "v_cvt_pk_bf16_f32 v134, v134, v134\n"
    "s_add_u32 m0, s82, 0xe000\n"
    "s_add_u32 s84, s59, 0x30800\n"
    "buffer_load_dwordx4 v100, s[32:35], s84 offen offset:0 lds\n"
    "v_lshlrev_b32 v134, 16, v134\n"
    "v_sqrt_f32 v134, v134\n"
    "v_cvt_pk_bf16_f32 v152, -v144, -v144\n"
    "v_cvt_pk_bf16_f32 v153, v134, -v144\n"
    "v_lshlrev_b32 v134, 16, v153\n"
    "v_rcp_f32 v146, v134\n"
    "v_and_b32 v152, v152, v138\n"
    "buffer_load_dwordx4 v101, s[32:35], s84 offen offset:1024 lds\n"
    "v_and_b32 v153, v153, v138\n"
    "s_nop 1\n"
    "v_mfma_f32_16x16x32_bf16 v[120:123], v[148:151], v[152:155], v[120:123]\n"
    "s_nop 7\n"
    "v_mul_f32 v120, v146, v120\n"
    "v_mul_f32 v121, v146, v121\n"
    "v_mul_f32 v122, v146, v122\n"
    "v_mul_f32 v123, v146, v123\n"
    "v_cvt_pk_bf16_f32 v102, v120, v121\n"
    "s_add_u32 m0, s82, 0x10000\n"
    "s_add_u32 s84, s79, 0x0\n"
    "buffer_load_dwordx4 v104, s[36:39], s84 offen offset:0 lds\n"
    "v_cvt_pk_bf16_f32 v103, v122, v123\n"
    "v_add_u32 v248, -1, v178\n"
    "v_not_b32 v249, v248\n"
    "s_and_b32 s72, s70, 1\n"
    "s_mul_i32 s72, s72, 10368\n"
    "s_lshr_b32 s73, s70, 1\n"
    "s_mul_i32 s73, s73, 4608\n"
    "buffer_load_dwordx4 v105, s[36:39], s84 offen offset:1024 lds\n"
    "s_add_u32 s72, s72, s73\n"
    "s_add_u32 s72, s72, 0x22700\n"
    "v_mul_u32_u24 v250, 144, v179\n"
    "v_lshl_add_u32 v250, v177, 4, v250\n"
    "v_add_u32 v250, s72, v250\n"
    "v_lshrrev_b32 v183, 2, v255\n"
    "v_mul_u32_u24 v253, 144, v183\n"
    "v_and_b32 v183, 3, v255\n"
    "v_lshl_add_u32 v253, v183, 3, v253\n"
    "v_add_u32 v253, s72, v253\n"
    "s_add_u32 s72, s72, 1152\n"
    "v_mul_u32_u24 v251, 144, v176\n"
    "v_lshl_add_u32 v251, v177, 4, v251\n"
    "v_add_u32 v251, s72, v251\n"
    "v_lshrrev_b32 v183, 1, v177\n"
    "v_cmp_eq_u32 vcc, v183, v178\n"
    "v_mov_b32 v183, 0x27f00\n"
    "s_nop 0\n"
    "v_cndmask_b32 v251, v183, v251, vcc\n"
    "v_cndmask_b32 v252, 0, -1, vcc\n"
    "s_waitcnt vmcnt(25)\n"
    "s_barrier\n"
    "s_add_u32 s92, s89, 0x80000\n"
    "buffer_load_dword v163, v143, s[36:39], s92 offen\n"
    "s_add_u32 s92, s89, 0x50000\n"
    "buffer_load_dword v163, v143, s[36:39], s92 offen\n"
    "s_add_u32 m0, s82, 0x12000\n"
    "s_add_u32 s84, s79, 0xe0000\n"
    "buffer_load_dwordx4 v104, s[36:39], s84 offen offset:0 lds\n"
    "ds_read_b64_tr_b16 v[64:65], v109 offset:32768\n"
    "ds_read_b64_tr_b16 v[66:67], v109 offset:33280\n"
    "buffer_load_dwordx4 v105, s[36:39], s84 offen offset:1024 lds\n"
    "ds_read_b64_tr_b16 v[68:69], v109 offset:36864\n"
    "ds_read_b64_tr_b16 v[70:71], v109 offset:37376\n"
    "s_add_u32 m0, s82, 0x14000\n"
    "s_add_u32 s84, s79, 0x40000\n"
    "buffer_load_dwordx4 v104, s[36:39], s84 offen offset:0 lds\n"
    "ds_read_b64_tr_b16 v[72:73], v109 offset:40960\n"
    "ds_read_b64_tr_b16 v[74:75], v109 offset:41472\n"
    "buffer_load_dwordx4 v105, s[36:39], s84 offen offset:1024 lds\n"
    "ds_read_b64_tr_b16 v[76:77], v109 offset:45056\n"
    "ds_read_b64_tr_b16 v[78:79], v109 offset:45568\n"
    "s_add_u32 m0, s82, 0x16000\n"
    "s_add_u32 s84, s79, 0x120000\n"
    "buffer_load_dwordx4 v104, s[36:39], s84 offen offset:0 lds\n"
    "s_waitcnt lgkmcnt(6)\n"
    "v_mfma_f32_16x16x32_bf16 v[116:119], v[124:127], v[64:67], v[116:119]\n"
    "s_waitcnt lgkmcnt(4)\n"
    "v_mfma_f32_16x16x32_bf16 v[116:119], v[124:127], v[68:71], v[116:119]\n"
    "s_waitcnt lgkmcnt(2)\n"
    "v_mfma_f32_16x16x32_bf16 v[116:119], v[124:127], v[72:75], v[116:119]\n"
    "s_waitcnt lgkmcnt(0)\n"
    "v_mfma_f32_16x16x32_bf16 v[116:119], v[124:127], v[76:79], v[116:119]\n"
    "v_mul_u32_u24 v147, 10368, v178\n"
    "v_mul_u32_u24 v183, 144, v176\n"
    "buffer_load_dwordx4 v105, s[36:39], s84 offen offset:1024 lds\n"
    "v_add_u32 v147, v147, v183\n"
    "v_lshl_add_u32 v147, v177, 3, v147\n"
    "s_lshl_b32 s72, s70, 5\n"
    "s_add_u32 s72, s72, 0x22700\n"
    "v_add_u32 v147, s72, v147\n"
    "v_lshl_add_u32 v183, v178, 2, v177\n"
    "v_lshlrev_b32 v254, 9, v183\n"
    "v_lshl_add_u32 v254, v176, 4, v254\n"
    "s_lshl_b32 s72, s65, 2\n"
    "s_add_u32 s72, s72, s70\n"
    "s_mul_i32 s72, s72, 147456\n"
    "s_lshr_b32 s73, s66, 2\n"
    "s_mul_i32 s73, s73, 73728\n"
    "s_add_u32 s72, s72, s73\n"
    "s_and_b32 s73, s66, 3\n"
    "s_lshl_b32 s73, s73, 12\n"
    "s_add_u32 s72, s72, s73\n"
    "s_add_u32 s85, s72, 57344\n"
    "s_waitcnt vmcnt(27)\n"
    "s_barrier\n"
    "s_add_u32 s92, s89, 0x20000\n"
    "buffer_load_dword v163, v143, s[36:39], s92 offen\n"
    "s_add_u32 s92, s89, 0xc0000\n"
    "buffer_load_dword v163, v143, s[36:39], s92 offen\n"
    "ds_read_b64_tr_b16 v[64:65], v108 offset:0\n"
    "ds_read_b64_tr_b16 v[66:67], v108 offset:512\n"
    "ds_read_b64_tr_b16 v[68:69], v108 offset:4096\n"
    "ds_read_b64_tr_b16 v[70:71], v108 offset:4608\n"
    "ds_read_b64_tr_b16 v[72:73], v108 offset:8192\n"
    "ds_read_b64_tr_b16 v[74:75], v108 offset:8704\n"
    "ds_read_b64_tr_b16 v[76:77], v108 offset:12288\n"
    "ds_read_b64_tr_b16 v[78:79], v108 offset:12800\n"
    "s_waitcnt lgkmcnt(6)\n"
    "v_mfma_f32_16x16x32_bf16 v[116:119], v[124:127], v[64:67], v[116:119]\n"
    "s_waitcnt lgkmcnt(4)\n"
    "v_mfma_f32_16x16x32_bf16 v[116:119], v[124:127], v[68:71], v[116:119]\n"
    "s_waitcnt lgkmcnt(2)\n"
    "v_mfma_f32_16x16x32_bf16 v[116:119], v[124:127], v[72:75], v[116:119]\n"
    "s_waitcnt lgkmcnt(0)\n"
    "v_mfma_f32_16x16x32_bf16 v[116:119], v[124:127], v[76:79], v[116:119]\n"
    "s_nop 7\n"
    "v_cvt_pk_bf16_f32 v134, v116, v116\n"
    "v_lshlrev_b32 v134, 16, v134\n"
    "v_sub_f32 v134, v116, v134\n"
    "v_cvt_pk_bf16_f32 v148, v116, v134\n"
    "v_and_b32 v135, 0xffff0000, v148\n"
    "v_sub_f32 v134, v134, v135\n"
    "v_cvt_pk_bf16_f32 v149, 0, v134\n"
    "v_mov_b32 v134, 0x27f00\n"
    "v_mov_b32 v136, 0\n"
    "v_mov_b32 v137, 0\n"
    "v_mov_b32 v138, 0\n"
    "v_mov_b32 v139, 0\n"
    "ds_write_b64 v132, v[148:149] offset:512\n"
    "ds_write_b64 v147, v[102:103]\n"
    "ds_write_b128 v134, v[136:139]\n"
    "s_waitcnt vmcnt(25)\n"
    "s_waitcnt lgkmcnt(0)\n"
    "s_barrier\n"
    "s_add_u32 s92, s89, 0x60000\n"
    "buffer_load_dword v163, v143, s[36:39], s92 offen\n"
    "s_add_u32 m0, s82, 0x18000\n"
    "s_add_u32 s84, s79, 0x70000\n"
    "buffer_load_dwordx4 v104, s[36:39], s84 offen offset:0 lds\n"
    "buffer_load_dwordx4 v105, s[36:39], s84 offen offset:1024 lds\n"
    "ds_read_b64_tr_b16 v[64:65], v109 offset:49152\n"
    "ds_read_b64_tr_b16 v[66:67], v109 offset:49664\n"
    "ds_read_b64_tr_b16 v[68:69], v109 offset:53248\n"
    "ds_read_b64_tr_b16 v[70:71], v109 offset:53760\n"
    "s_add_u32 m0, s82, 0x1a000\n"
    "s_add_u32 s84, s79, 0x150000\n"
    "buffer_load_dwordx4 v104, s[36:39], s84 offen offset:0 lds\n"
    "ds_read_b64_tr_b16 v[72:73], v109 offset:57344\n"
    "ds_read_b64_tr_b16 v[74:75], v109 offset:57856\n"
    "ds_read_b64_tr_b16 v[76:77], v109 offset:61440\n"
    "buffer_load_dwordx4 v105, s[36:39], s84 offen offset:1024 lds\n"
    "ds_read_b64_tr_b16 v[78:79], v109 offset:61952\n"
    "s_waitcnt lgkmcnt(6)\n"
    "v_mfma_f32_16x16x32_bf16 v[116:119], v[124:127], v[64:67], 0\n"
    "v_mfma_f32_16x16x32_bf16 v[120:123], v[64:67], v[0:3], 0\n"
    "s_waitcnt lgkmcnt(4)\n"
    "v_mfma_f32_16x16x32_bf16 v[116:119], v[124:127], v[68:71], v[116:119]\n"
    "s_add_u32 m0, s82, 0x0\n"
    "s_add_u32 s84, s79, 0xb0000\n"
    "buffer_load_dwordx4 v104, s[36:39], s84 offen offset:0 lds\n"
    "v_mfma_f32_16x16x32_bf16 v[120:123], v[68:71], v[4:7], v[120:123]\n"
    "s_waitcnt lgkmcnt(2)\n"
    "v_mfma_f32_16x16x32_bf16 v[116:119], v[124:127], v[72:75], v[116:119]\n"
    "v_mfma_f32_16x16x32_bf16 v[120:123], v[72:75], v[8:11], v[120:123]\n"
    "s_waitcnt lgkmcnt(0)\n"
    "v_mfma_f32_16x16x32_bf16 v[116:119], v[124:127], v[76:79], v[116:119]\n"
    "v_mfma_f32_16x16x32_bf16 v[120:123], v[76:79], v[12:15], v[120:123]\n"
    "ds_read_b128 v[176:179], v250 offset:0\n"
    "buffer_load_dwordx4 v105, s[36:39], s84 offen offset:1024 lds\n"
    "ds_read_b128 v[180:183], v250 offset:64\n"
    "ds_read_b128 v[184:187], v250 offset:1152\n"
    "ds_read_b128 v[188:191], v250 offset:1216\n"
    "ds_read_b128 v[192:195], v250 offset:2304\n"
    "s_add_u32 m0, s82, 0x2000\n"
    "s_add_u32 s84, s79, 0x190000\n"
    "buffer_load_dwordx4 v104, s[36:39], s84 offen offset:0 lds\n"
    "ds_read_b128 v[196:199], v250 offset:2368\n"
    "ds_read_b128 v[200:203], v250 offset:3456\n"
    "buffer_load_dwordx4 v105, s[36:39], s84 offen offset:1024 lds\n"
    "ds_read_b128 v[204:207], v250 offset:3520\n"
    "s_waitcnt vmcnt(30)\n"
    "s_waitcnt lgkmcnt(0)\n"
    "s_barrier\n"
    "s_add_u32 s92, s89, 0x30000\n"
    "buffer_load_dword v163, v143, s[36:39], s92 offen\n"
    "s_add_u32 s92, s89, 0xd0000\n"
    "buffer_load_dword v163, v143, s[36:39], s92 offen\n"
    "s_add_u32 s92, s89, 0x90000\n"
    "buffer_load_dword v163, v143, s[36:39], s92 offen\n"
    "ds_read_b64_tr_b16 v[64:65], v108 offset:16384\n"
    "ds_read_b64_tr_b16 v[66:67], v108 offset:16896\n"
    "ds_read_b64_tr_b16 v[68:69], v108 offset:20480\n"
    "ds_read_b64_tr_b16 v[70:71], v108 offset:20992\n"
    "ds_read_b64_tr_b16 v[72:73], v108 offset:24576\n"
    "ds_read_b64_tr_b16 v[74:75], v108 offset:25088\n"
    "ds_read_b64_tr_b16 v[76:77], v108 offset:28672\n"
    "ds_read_b64_tr_b16 v[78:79], v108 offset:29184\n"
    "s_waitcnt lgkmcnt(6)\n"
    "v_mfma_f32_16x16x32_bf16 v[116:119], v[124:127], v[64:67], v[116:119]\n"
    "v_mfma_f32_16x16x32_bf16 v[120:123], v[64:67], v[16:19], v[120:123]\n"
    "s_waitcnt lgkmcnt(4)\n"
    "v_mfma_f32_16x16x32_bf16 v[116:119], v[124:127], v[68:71], v[116:119]\n"
    "v_mfma_f32_16x16x32_bf16 v[120:123], v[68:71], v[20:23], v[120:123]\n"
    "s_waitcnt lgkmcnt(2)\n"
    "v_mfma_f32_16x16x32_bf16 v[116:119], v[124:127], v[72:75], v[116:119]\n"
    "v_mfma_f32_16x16x32_bf16 v[120:123], v[72:75], v[24:27], v[120:123]\n"
    "s_waitcnt lgkmcnt(0)\n"
    "v_mfma_f32_16x16x32_bf16 v[116:119], v[124:127], v[76:79], v[116:119]\n"
    "v_mfma_f32_16x16x32_bf16 v[120:123], v[76:79], v[28:31], v[120:123]\n"
    "s_waitcnt vmcnt(28)\n"
    "s_barrier\n"
    "buffer_load_dwordx2 v[158:159], v156, s[48:51], 0 offen offset:0 sc1\n"
    "s_add_u32 s92, s89, 0xa0000\n"
    "buffer_load_dword v163, v143, s[36:39], s92 offen\n"
    "s_add_u32 m0, s82, 0x1c000\n"
    "s_add_u32 s84, s79, 0x10000\n"
    "buffer_load_dwordx4 v104, s[36:39], s84 offen offset:0 lds\n"
    "buffer_load_dwordx4 v105, s[36:39], s84 offen offset:1024 lds\n"
    "ds_read_b64_tr_b16 v[64:65], v108 offset:32768\n"
    "ds_read_b64_tr_b16 v[66:67], v108 offset:33280\n"
    "ds_read_b64_tr_b16 v[68:69], v108 offset:36864\n"
    "ds_read_b64_tr_b16 v[70:71], v108 offset:37376\n"
    "s_add_u32 m0, s82, 0x1e000\n"
    "s_add_u32 s84, s79, 0xf0000\n"
    "buffer_load_dwordx4 v104, s[36:39], s84 offen offset:0 lds\n"
    "ds_read_b64_tr_b16 v[72:73], v108 offset:40960\n"
    "ds_read_b64_tr_b16 v[74:75], v108 offset:41472\n"
    "ds_read_b64_tr_b16 v[76:77], v108 offset:45056\n"
    "buffer_load_dwordx4 v105, s[36:39], s84 offen offset:1024 lds\n"
    "ds_read_b64_tr_b16 v[78:79], v108 offset:45568\n"
    "s_waitcnt lgkmcnt(6)\n"
    "v_mfma_f32_16x16x32_bf16 v[116:119], v[124:127], v[64:67], v[116:119]\n"
    "v_mfma_f32_16x16x32_bf16 v[120:123], v[64:67], v[32:35], v[120:123]\n"
    "s_waitcnt lgkmcnt(4)\n"
    "v_mfma_f32_16x16x32_bf16 v[116:119], v[124:127], v[68:71], v[116:119]\n"
    "s_add_u32 m0, s82, 0x4000\n"
    "s_add_u32 s84, s79, 0x50000\n"
    "buffer_load_dwordx4 v104, s[36:39], s84 offen offset:0 lds\n"
    "v_mfma_f32_16x16x32_bf16 v[120:123], v[68:71], v[36:39], v[120:123]\n"
    "s_waitcnt lgkmcnt(2)\n"
    "v_mfma_f32_16x16x32_bf16 v[116:119], v[124:127], v[72:75], v[116:119]\n"
    "v_mfma_f32_16x16x32_bf16 v[120:123], v[72:75], v[40:43], v[120:123]\n"
    "s_waitcnt lgkmcnt(0)\n"
    "v_mfma_f32_16x16x32_bf16 v[116:119], v[124:127], v[76:79], v[116:119]\n"
    "v_mfma_f32_16x16x32_bf16 v[120:123], v[76:79], v[44:47], v[120:123]\n"
    "v_and_b32 v134, 0, v252\n"
    "buffer_load_dwordx4 v105, s[36:39], s84 offen offset:1024 lds\n"
    "v_add_u32 v134, v134, v251\n"
    "v_and_b32 v135, 64, v252\n"
    "v_add_u32 v135, v135, v251\n"
    "v_and_b32 v136, 1152, v252\n"
    "v_add_u32 v136, v136, v251\n"
    "v_and_b32 v137, 1216, v252\n"
    "s_add_u32 m0, s82, 0x6000\n"
    "s_add_u32 s84, s79, 0x130000\n"
    "buffer_load_dwordx4 v104, s[36:39], s84 offen offset:0 lds\n"
    "v_add_u32 v137, v137, v251\n"
    "v_and_b32 v138, 2304, v252\n"
    "v_add_u32 v138, v138, v251\n"
    "v_and_b32 v139, 2368, v252\n"
    "v_add_u32 v139, v139, v251\n"
    "v_and_b32 v140, 3456, v252\n"
    "buffer_load_dwordx4 v105, s[36:39], s84 offen offset:1024 lds\n"
    "v_add_u32 v140, v140, v251\n"
    "v_and_b32 v141, 3520, v252\n"
    "v_add_u32 v141, v141, v251\n"
    "ds_read_b128 v[208:211], v134\n"
    "ds_read_b128 v[212:215], v135\n"
    "ds_read_b128 v[216:219], v136\n"
    "ds_read_b128 v[220:223], v137\n"
    "ds_read_b128 v[224:227], v138\n"
    "ds_read_b128 v[228:231], v139\n"
    "ds_read_b128 v[232:235], v140\n"
    "ds_read_b128 v[236:239], v141\n"
    "s_waitcnt vmcnt(9)\n"
    "v_cmp_ne_u64 vcc, s[96:97], v[158:159]\n"
    "s_waitcnt lgkmcnt(0)\n"
    "s_cmp_lg_u32 s70, 0\n"
    "s_cbranch_scc1 POK_att_1\n"
    "s_mov_b32 s90, 0\n"
    "s_nop 1\n"
    "s_cbranch_vccz POK_att_1\n"
    "s_branch PS1_att_1\n"
    "PS0_att_1:\n"
    "s_xor_b64 s[8:9], s[8:9], s[96:97]\n"
    "s_xor_b64 s[10:11], s[10:11], s[96:97]\n"
    "s_xor_b64 s[12:13], s[12:13], s[96:97]\n"
    "s_xor_b64 s[14:15], s[14:15], s[96:97]\n"
    "s_xor_b64 s[16:17], s[16:17], s[96:97]\n"
    "s_xor_b64 s[18:19], s[18:19], s[96:97]\n"
    "s_xor_b64 s[20:21], s[20:21], s[96:97]\n"
    "s_xor_b64 s[22:23], s[22:23], s[96:97]\n"
    "s_or_b64 s[8:9], s[8:9], s[10:11]\n"
    "s_or_b64 s[8:9], s[8:9], s[12:13]\n"
    "s_or_b64 s[8:9], s[8:9], s[14:15]\n"
    "s_or_b64 s[8:9], s[8:9], s[16:17]\n"
    "s_or_b64 s[8:9], s[8:9], s[18:19]\n"
    "s_or_b64 s[8:9], s[8:9], s[20:21]\n"
    "s_or_b64 s[8:9], s[8:9], s[22:23]\n"
    "s_cbranch_scc0 POK_att_1\n"
    "PS1_att_1:\n"
    "s_load_dwordx16 s[8:23], s[98:99], 0x0 glc\n"
    "s_add_u32 s90, s90, 1\n"
    "s_cmp_lt_u32 s90, 64\n"
    "s_waitcnt lgkmcnt(0)\n"
    "s_cbranch_scc1 PS0_att_1\n"
    "PSP_att_1:\n"
    "buffer_load_dwordx2 v[158:159], v156, s[48:51], 0 offen offset:0 sc1\n"
    "s_waitcnt vmcnt(0)\n"
    "v_cmp_ne_u64 vcc, s[96:97], v[158:159]\n"
    "s_nop 4\n"
    "s_cbranch_vccz POK_att_1\n"
    "s_add_u32 s90, s90, 1\n"
    "s_cmp_lt_u32 s90, 0x40000\n"
    "s_cbranch_scc1 PSP_att_1\n"
    "POK_att_1:\n"
    "s_barrier\n"
    "s_setprio 0\n"
    "s_add_u32 s92, s89, 0x10000\n"
    "buffer_load_dword v163, v143, s[40:43], s92 offen\n"
    "s_setprio 2\n"
    "s_setprio 0\n"
    "s_add_u32 s92, s89, 0x0\n"
    "buffer_load_dword v163, v143, s[40:43], s92 offen\n"
    "s_setprio 2\n"
    "ds_read_b64_tr_b16 v[64:65], v108 offset:49152\n"
    "ds_read_b64_tr_b16 v[66:67], v108 offset:49664\n"
    "ds_read_b64_tr_b16 v[68:69], v108 offset:53248\n"
    "ds_read_b64_tr_b16 v[70:71], v108 offset:53760\n"
    "ds_read_b64_tr_b16 v[72:73], v108 offset:57344\n"
    "ds_read_b64_tr_b16 v[74:75], v108 offset:57856\n"
    "ds_read_b64_tr_b16 v[76:77], v108 offset:61440\n"
    "ds_read_b64_tr_b16 v[78:79], v108 offset:61952\n"
    "s_waitcnt lgkmcnt(6)\n"
    "v_mfma_f32_16x16x32_bf16 v[116:119], v[124:127], v[64:67], v[116:119]\n"
    "v_mfma_f32_16x16x32_bf16 v[120:123], v[64:67], v[48:51], v[120:123]\n"
    "s_waitcnt lgkmcnt(4)\n"
    "v_mfma_f32_16x16x32_bf16 v[116:119], v[124:127], v[68:71], v[116:119]\n"
    "v_mfma_f32_16x16x32_bf16 v[120:123], v[68:71], v[52:55], v[120:123]\n"
    "s_waitcnt lgkmcnt(2)\n"
    "v_mfma_f32_16x16x32_bf16 v[116:119], v[124:127], v[72:75], v[116:119]\n"
    "v_mfma_f32_16x16x32_bf16 v[120:123], v[72:75], v[56:59], v[120:123]\n"
    "s_waitcnt lgkmcnt(0)\n"
    "v_mfma_f32_16x16x32_bf16 v[116:119], v[124:127], v[76:79], v[116:119]\n"
    "v_mfma_f32_16x16x32_bf16 v[120:123], v[76:79], v[60:63], v[120:123]\n"
    "s_nop 6\n"
    "v_cvt_pk_bf16_f32 v134, v116, v116\n"
    "v_lshlrev_b32 v134, 16, v134\n"
    "v_sub_f32 v134, v116, v134\n"
    "v_cvt_pk_bf16_f32 v148, v116, v134\n"
    "v_and_b32 v135, 0xffff0000, v148\n"
    "v_sub_f32 v134, v134, v135\n"
    "v_cvt_pk_bf16_f32 v149, 0, v134\n"
    "v_mfma_f32_16x16x32_bf16 v[176:179], v[176:179], v[208:211], v[240:243]\n"
    "v_mfma_f32_16x16x32_bf16 v[180:183], v[180:183], v[212:215], v[240:243]\n"
    "v_mfma_f32_16x16x32_bf16 v[184:187], v[184:187], v[216:219], v[244:247]\n"
    "v_mfma_f32_16x16x32_bf16 v[188:191], v[188:191], v[220:223], v[244:247]\n"
    "v_mfma_f32_16x16x32_bf16 v[192:195], v[192:195], v[224:227], v[244:247]\n"
    "v_mfma_f32_16x16x32_bf16 v[196:199], v[196:199], v[228:231], v[244:247]\n"
    "v_mfma_f32_16x16x32_bf16 v[200:203], v[200:203], v[232:235], v[244:247]\n"
    "v_mfma_f32_16x16x32_bf16 v[204:207], v[204:207], v[236:239], v[244:247]\n"
    "s_nop 0\n"
    "v_max3_f32 v168, v176, v177, v178\n"
    "v_max3_f32 v170, v180, v181, v182\n"
    "v_max3_f32 v172, v184, v185, v186\n"
    "v_max3_f32 v174, v188, v189, v190\n"
    "v_max_f32 v168, v168, v179\n"
    "v_max_f32 v170, v170, v183\n"
    "v_max_f32 v172, v172, v187\n"
    "v_max_f32 v174, v174, v191\n"
    "v_mov_b32 v169, v168\n"
    "v_mov_b32 v171, v170\n"
    "v_mov_b32 v173, v172\n"
    "v_mov_b32 v175, v174\n"
    "v_permlane16_swap_b32 v168, v169\n"
    "v_permlane16_swap_b32 v170, v171\n"
    "v_permlane16_swap_b32 v172, v173\n"
    "v_permlane16_swap_b32 v174, v175\n"
    "v_max_f32 v168, v168, v169\n"
    "v_max_f32 v170, v170, v171\n"
    "v_max_f32 v172, v172, v173\n"
    "v_max_f32 v174, v174, v175\n"
    "v_mov_b32 v169, v168\n"
    "v_mov_b32 v171, v170\n"
    "v_mov_b32 v173, v172\n"
    "v_mov_b32 v175, v174\n"
    "v_permlane32_swap_b32 v168, v169\n"
    "v_permlane32_swap_b32 v170, v171\n"
    "v_permlane32_swap_b32 v172, v173\n"
    "v_permlane32_swap_b32 v174, v175\n"
    "v_max_f32 v168, v168, v169\n"
    "v_max_f32 v170, v170, v171\n"
    "v_max_f32 v172, v172, v173\n"
    "v_max_f32 v174, v174, v175\n"
    "v_sub_f32 v176, v176, v168\n"
    "v_sub_f32 v177, v177, v168\n"
    "v_sub_f32 v178, v178, v168\n"
    "v_sub_f32 v179, v179, v168\n"
    "v_exp_f32 v176, v176\n"
    "v_exp_f32 v177, v177\n"
    "v_exp_f32 v178, v178\n"
    "v_exp_f32 v179, v179\n"
    "v_sub_f32 v180, v180, v170\n"
    "v_sub_f32 v181, v181, v170\n"
    "ds_write_b64 v132, v[148:149] offset:1024\n"
    "s_waitcnt lgkmcnt(0)\n"
    "s_barrier\n"
    "s_add_u32 s92, s89, 0x20000\n"
    "buffer_load_dword v163, v143, s[40:43], s92 offen\n"
    "s_add_u32 m0, s82, 0x8000\n"
    "s_add_u32 s84, s79, 0x80000\n"
    "buffer_load_dwordx4 v104, s[36:39], s84 offen offset:0 lds\n"
    "buffer_load_dwordx4 v105, s[36:39], s84 offen offset:1024 lds\n"
    "v_mfma_f32_16x16x32_bf16 v[120:123], v[148:151], v[152:155], v[120:123]\n"
    "s_nop 7\n"
    "v_mul_f32 v120, v146, v120\n"
    "v_mul_f32 v121, v146, v121\n"
    "v_mul_f32 v122, v146, v122\n"
    "v_mul_f32 v123, v146, v123\n"
    "v_cvt_pk_bf16_f32 v102, v120, v121\n"
    "v_cvt_pk_bf16_f32 v103, v122, v123\n"
    "v_sub_f32 v182, v182, v170\n"
    "v_sub_f32 v183, v183, v170\n"
    "v_exp_f32 v180, v180\n"
    "v_exp_f32 v181, v181\n"
    "v_exp_f32 v182, v182\n"
    "v_exp_f32 v183, v183\n"
    "v_sub_f32 v184, v184, v172\n"
    "v_sub_f32 v185, v185, v172\n"
    "v_sub_f32 v186, v186, v172\n"
    "v_sub_f32 v187, v187, v172\n"
    "v_exp_f32 v184, v184\n"
    "v_exp_f32 v185, v185\n"
    "s_add_u32 m0, s82, 0xa000\n"
    "s_add_u32 s84, s79, 0x160000\n"
    "buffer_load_dwordx4 v104, s[36:39], s84 offen offset:0 lds\n"
    "v_exp_f32 v186, v186\n"
    "v_exp_f32 v187, v187\n"
    "v_sub_f32 v188, v188, v174\n"
    "v_sub_f32 v189, v189, v174\n"
    "v_sub_f32 v190, v190, v174\n"
    "v_sub_f32 v191, v191, v174\n"
    "v_exp_f32 v188, v188\n"
    "v_exp_f32 v189, v189\n"
    "v_exp_f32 v190, v190\n"
    "v_exp_f32 v191, v191\n"
    "v_cvt_pk_bf16_f32 v176, v176, v177\n"
    "v_cvt_pk_bf16_f32 v177, v178, v179\n"
    "v_and_b32 v208, v176, v248\n"
    "v_and_b32 v209, v177, v248\n"
    "v_and_b32 v210, v176, v249\n"
    "v_and_b32 v211, v177, v249\n"
    "v_cvt_pk_bf16_f32 v180, v180, v181\n"
    "v_cvt_pk_bf16_f32 v181, v182, v183\n"
    "v_and_b32 v212, v180, v248\n"
    "buffer_load_dwordx4 v105, s[36:39], s84 offen offset:1024 lds\n"
    "v_and_b32 v213, v181, v248\n"
    "v_and_b32 v214, v180, v249\n"
    "v_and_b32 v215, v181, v249\n"
    "v_cvt_pk_bf16_f32 v184, v184, v185\n"
    "v_cvt_pk_bf16_f32 v185, v186, v187\n"
    "v_and_b32 v216, v184, v248\n"
    "v_and_b32 v217, v185, v248\n"
    "v_and_b32 v218, v184, v249\n"
    "v_and_b32 v219, v185, v249\n"
    "v_cvt_pk_bf16_f32 v188, v188, v189\n"
    "v_cvt_pk_bf16_f32 v189, v190, v191\n"
    "v_and_b32 v220, v188, v248\n"
    "v_and_b32 v221, v189, v248\n"
    "v_and_b32 v222, v188, v249\n"
    "v_and_b32 v223, v189, v249\n"
    "v_mfma_f32_16x16x32_bf16 v[176:179], v[124:127], v[208:211], 0\n"
    "v_mfma_f32_16x16x32_bf16 v[180:183], v[124:127], v[212:215], 0\n"
    "v_mfma_f32_16x16x32_bf16 v[184:187], v[124:127], v[216:219], 0\n"
    "v_mfma_f32_16x16x32_bf16 v[188:191], v[124:127], v[220:223], 0\n"
    "s_barrier\n"
    "buffer_load_dwordx2 v[158:159], v156, s[48:51], 0 offen offset:128 sc1\n"
    "s_add_u32 s92, s89, 0x30000\n"
    "buffer_load_dword v163, v143, s[40:43], s92 offen\n"
    "s_nop 0\n"
    "v_rcp_f32 v176, v176\n"
    "v_rcp_f32 v180, v180\n"
    "v_rcp_f32 v184, v184\n"
    "v_rcp_f32 v188, v188\n"
    "v_max3_f32 v168, v192, v193, v194\n"
    "v_max3_f32 v170, v196, v197, v198\n"
    "v_max3_f32 v172, v200, v201, v202\n"
    "v_max3_f32 v174, v204, v205, v206\n"
    "v_max_f32 v168, v168, v195\n"
    "v_max_f32 v170, v170, v199\n"
    "v_max_f32 v172, v172, v203\n"
    "v_max_f32 v174, v174, v207\n"
    "v_mov_b32 v169, v168\n"
    "v_mov_b32 v171, v170\n"
    "v_mov_b32 v173, v172\n"
    "v_mov_b32 v175, v174\n"
    "v_permlane16_swap_b32 v168, v169\n"
    "v_permlane16_swap_b32 v170, v171\n"
    "v_permlane16_swap_b32 v172, v173\n"
    "v_permlane16_swap_b32 v174, v175\n"
    "v_max_f32 v168, v168, v169\n"
    "v_max_f32 v170, v170, v171\n"
    "v_max_f32 v172, v172, v173\n"
    "v_max_f32 v174, v174, v175\n"
    "v_mov_b32 v169, v168\n"
    "v_mov_b32 v171, v170\n"
    "v_mov_b32 v173, v172\n"
    "v_mov_b32 v175, v174\n"
    "v_permlane32_swap_b32 v168, v169\n"
    "v_permlane32_swap_b32 v170, v171\n"
    "v_permlane32_swap_b32 v172, v173\n"
    "v_permlane32_swap_b32 v174, v175\n"
    "v_max_f32 v168, v168, v169\n"
    "v_max_f32 v170, v170, v171\n"
    "v_max_f32 v172, v172, v173\n"
    "v_max_f32 v174, v174, v175\n"
    "v_sub_f32 v192, v192, v168\n"
    "v_sub_f32 v193, v193, v168\n"
    "v_sub_f32 v194, v194, v168\n"
    "v_sub_f32 v195, v195, v168\n"
    "v_exp_f32 v192, v192\n"
    "v_exp_f32 v193, v193\n"
    "v_exp_f32 v194, v194\n"
    "v_exp_f32 v195, v195\n"
    "v_sub_f32 v196, v196, v170\n"
    "v_sub_f32 v197, v197, v170\n"
    "v_sub_f32 v198, v198, v170\n"
    "v_sub_f32 v199, v199, v170\n"
    "v_exp_f32 v196, v196\n"
    "v_exp_f32 v197, v197\n"
    "ds_write_b64 v147, v[102:103]\n"
    "s_waitcnt vmcnt(1)\n"
    "v_cmp_ne_u64 vcc, s[96:97], v[158:159]\n"
    "s_waitcnt lgkmcnt(0)\n"
    "s_cmp_lg_u32 s70, 0\n"
    "s_cbranch_scc1 POK_att_2\n"
    "s_mov_b32 s90, 0\n"
    "s_nop 1\n"
    "s_cbranch_vccz POK_att_2\n"
    "s_branch PS1_att_2\n"
    "PS0_att_2:\n"
    "s_xor_b64 s[8:9], s[8:9], s[96:97]\n"
    "s_xor_b64 s[10:11], s[10:11], s[96:97]\n"
    "s_xor_b64 s[12:13], s[12:13], s[96:97]\n"
    "s_xor_b64 s[14:15], s[14:15], s[96:97]\n"
    "s_xor_b64 s[16:17], s[16:17], s[96:97]\n"
    "s_xor_b64 s[18:19], s[18:19], s[96:97]\n"
    "s_xor_b64 s[20:21], s[20:21], s[96:97]\n"
    "s_xor_b64 s[22:23], s[22:23], s[96:97]\n"
    "s_or_b64 s[8:9], s[8:9], s[10:11]\n"
    "s_or_b64 s[8:9], s[8:9], s[12:13]\n"
    "s_or_b64 s[8:9], s[8:9], s[14:15]\n"
    "s_or_b64 s[8:9], s[8:9], s[16:17]\n"
    "s_or_b64 s[8:9], s[8:9], s[18:19]\n"
    "s_or_b64 s[8:9], s[8:9], s[20:21]\n"
    "s_or_b64 s[8:9], s[8:9], s[22:23]\n"
    "s_cbranch_scc0 POK_att_2\n"
    "PS1_att_2:\n"
    "s_load_dwordx16 s[8:23], s[98:99], 0x80 glc\n"
    "s_add_u32 s90, s90, 1\n"
    "s_cmp_lt_u32 s90, 64\n"
    "s_waitcnt lgkmcnt(0)\n"
    "s_cbranch_scc1 PS0_att_2\n"
    "PSP_att_2:\n"
    "buffer_load_dwordx2 v[158:159], v156, s[48:51], 0 offen offset:128 sc1\n"
    "s_waitcnt vmcnt(0)\n"
    "v_cmp_ne_u64 vcc, s[96:97], v[158:159]\n"
    "s_nop 4\n"
    "s_cbranch_vccz POK_att_2\n"
    "s_add_u32 s90, s90, 1\n"
    "s_cmp_lt_u32 s90, 0x40000\n"
    "s_cbranch_scc1 PSP_att_2\n"
    "POK_att_2:\n"
    "s_barrier\n"
    "s_add_u32 m0, s82, 0xc000\n"
    "s_add_u32 s84, s79, 0xc0000\n"
    "buffer_load_dwordx4 v104, s[36:39], s84 offen offset:0 lds\n"
    "buffer_load_dwordx4 v105, s[36:39], s84 offen offset:1024 lds\n"
    "s_add_u32 m0, s82, 0xe000\n"
    "s_add_u32 s84, s79, 0x1a0000\n"
    "buffer_load_dwordx4 v104, s[36:39], s84 offen offset:0 lds\n"
    "ds_read_b64_tr_b16 v[0:1], v253 offset:0\n"
    "ds_read_b64_tr_b16 v[2:3], v253 offset:32\n"
    "ds_read_b64_tr_b16 v[4:5], v253 offset:64\n"
    "ds_read_b64_tr_b16 v[6:7], v253 offset:96\n"
    "ds_read_b64_tr_b16 v[8:9], v253 offset:1152\n"
    "ds_read_b64_tr_b16 v[10:11], v253 offset:1184\n"
    "ds_read_b64_tr_b16 v[12:13], v253 offset:1216\n"
    "ds_read_b64_tr_b16 v[14:15], v253 offset:1248\n"
    "ds_read_b64_tr_b16 v[16:17], v253 offset:2304\n"
    "ds_read_b64_tr_b16 v[18:19], v253 offset:2336\n"
    "ds_read_b64_tr_b16 v[20:21], v253 offset:2368\n"
    "ds_read_b64_tr_b16 v[22:23], v253 offset:2400\n"
    "ds_read_b64_tr_b16 v[24:25], v253 offset:3456\n"
    "ds_read_b64_tr_b16 v[26:27], v253 offset:3488\n"
    "ds_read_b64_tr_b16 v[28:29], v253 offset:3520\n"
    "buffer_load_dwordx4 v105, s[36:39], s84 offen offset:1024 lds\n"
    "ds_read_b64_tr_b16 v[30:31], v253 offset:3552\n"
    "v_exp_f32 v198, v198\n"
    "v_exp_f32 v199, v199\n"
    "v_sub_f32 v200, v200, v172\n"
    "v_sub_f32 v201, v201, v172\n"
    "v_sub_f32 v202, v202, v172\n"
    "v_sub_f32 v203, v203, v172\n"
    "v_exp_f32 v200, v200\n"
    "v_exp_f32 v201, v201\n"
    "v_exp_f32 v202, v202\n"
    "v_exp_f32 v203, v203\n"
    "v_sub_f32 v204, v204, v174\n"
    "v_sub_f32 v205, v205, v174\n"
    "v_sub_f32 v206, v206, v174\n"
    "v_sub_f32 v207, v207, v174\n"
    "v_exp_f32 v204, v204\n"
    "v_exp_f32 v205, v205\n"
    "v_exp_f32 v206, v206\n"
    "v_exp_f32 v207, v207\n"
    "v_cvt_pk_bf16_f32 v192, v192, v193\n"
    "v_cvt_pk_bf16_f32 v193, v194, v195\n"
    "v_and_b32 v224, v192, v248\n"
    "v_and_b32 v225, v193, v248\n"
    "v_and_b32 v226, v192, v249\n"
    "v_and_b32 v227, v193, v249\n"
    "v_cvt_pk_bf16_f32 v196, v196, v197\n"
    "v_cvt_pk_bf16_f32 v197, v198, v199\n"
    "v_and_b32 v228, v196, v248\n"
    "v_and_b32 v229, v197, v248\n"
    "v_and_b32 v230, v196, v249\n"
    "v_and_b32 v231, v197, v249\n"
    "v_cvt_pk_bf16_f32 v200, v200, v201\n"
    "v_cvt_pk_bf16_f32 v201, v202, v203\n"
    "v_and_b32 v232, v200, v248\n"
    "v_and_b32 v233, v201, v248\n"
    "v_and_b32 v234, v200, v249\n"
    "v_and_b32 v235, v201, v249\n"
    "v_cvt_pk_bf16_f32 v204, v204, v205\n"
    "v_cvt_pk_bf16_f32 v205, v206, v207\n"
    "v_and_b32 v236, v204, v248\n"
    "v_and_b32 v237, v205, v248\n"
    "v_and_b32 v238, v204, v249\n"
    "v_and_b32 v239, v205, v249\n"
    "v_mfma_f32_16x16x32_bf16 v[192:195], v[124:127], v[224:227], 0\n"
    "v_mfma_f32_16x16x32_bf16 v[196:199], v[124:127], v[228:231], 0\n"
    "v_mfma_f32_16x16x32_bf16 v[200:203], v[124:127], v[232:235], 0\n"
    "v_mfma_f32_16x16x32_bf16 v[204:207], v[124:127], v[236:239], 0\n"
    "s_nop 4\n"
    "v_rcp_f32 v192, v192\n"
    "v_rcp_f32 v196, v196\n"
    "v_rcp_f32 v200, v200\n"
    "v_rcp_f32 v204, v204\n"
    "s_waitcnt lgkmcnt(0)\n"
    "s_barrier\n"
    "s_add_u32 m0, s82, 0x10000\n"
    "s_add_u32 s84, s79, 0x20000\n"
    "buffer_load_dwordx4 v104, s[36:39], s84 offen offset:0 lds\n"
    "buffer_load_dwordx4 v105, s[36:39], s84 offen offset:1024 lds\n"
    "s_add_u32 m0, s82, 0x12000\n"
    "s_add_u32 s84, s79, 0x100000\n"
    "buffer_load_dwordx4 v104, s[36:39], s84 offen offset:0 lds\n"
    "v_mfma_f32_16x16x32_bf16 v[32:35], v[0:3], v[208:211], 0\n"
    "v_mfma_f32_16x16x32_bf16 v[36:39], v[4:7], v[212:215], 0\n"
    "v_mfma_f32_16x16x32_bf16 v[40:43], v[8:11], v[216:219], 0\n"
    "v_mfma_f32_16x16x32_bf16 v[44:47], v[12:15], v[220:223], 0\n"
    "v_mfma_f32_16x16x32_bf16 v[48:51], v[16:19], v[224:227], 0\n"
    "v_mfma_f32_16x16x32_bf16 v[52:55], v[20:23], v[228:231], 0\n"
    "v_mfma_f32_16x16x32_bf16 v[56:59], v[24:27], v[232:235], 0\n"
    "v_mfma_f32_16x16x32_bf16 v[60:63], v[28:31], v[236:239], 0\n"
    "s_nop 0\n"
    "v_mul_f32 v32, v32, v176\n"
    "v_mul_f32 v33, v33, v176\n"
    "v_mul_f32 v34, v34, v176\n"
    "v_mul_f32 v35, v35, v176\n"
    "v_mul_f32 v36, v36, v180\n"
    "v_mul_f32 v37, v37, v180\n"
    "v_mul_f32 v38, v38, v180\n"
    "buffer_load_dwordx4 v105, s[36:39], s84 offen offset:1024 lds\n"
    "v_mul_f32 v39, v39, v180\n"
    "v_mul_f32 v40, v40, v184\n"
    "v_mul_f32 v41, v41, v184\n"
    "v_mul_f32 v42, v42, v184\n"
    "v_mul_f32 v43, v43, v184\n"
    "v_mul_f32 v44, v44, v188\n"
    "v_mul_f32 v45, v45, v188\n"
    "v_mul_f32 v46, v46, v188\n"
    "v_mul_f32 v47, v47, v188\n"
    "v_mul_f32 v48, v48, v192\n"
    "v_mul_f32 v49, v49, v192\n"
    "v_mul_f32 v50, v50, v192\n"
    "v_mul_f32 v51, v51, v192\n"
    "v_mul_f32 v52, v52, v196\n"
    "s_barrier\n"
    "s_add_u32 m0, s82, 0x14000\n"
    "s_add_u32 s84, s79, 0x60000\n"
    "buffer_load_dwordx4 v104, s[36:39], s84 offen offset:0 lds\n"
    "buffer_load_dwordx4 v105, s[36:39], s84 offen offset:1024 lds\n"
    "v_mul_f32 v53, v53, v196\n"
    "v_mul_f32 v54, v54, v196\n"
    "v_mul_f32 v55, v55, v196\n"
    "v_mul_f32 v56, v56, v200\n"
    "v_mul_f32 v57, v57, v200\n"
    "v_mul_f32 v58, v58, v200\n"
    "v_mul_f32 v59, v59, v200\n"
    "v_mul_f32 v60, v60, v204\n"
    "v_mul_f32 v61, v61, v204\n"
    "v_mul_f32 v62, v62, v204\n"
    "v_mul_f32 v63, v63, v204\n"
    "v_cvt_pk_bf16_f32 v32, v32, v33\n"
    "v_cvt_pk_bf16_f32 v33, v34, v35\n"
    "v_cvt_pk_bf16_f32 v34, v36, v37\n"
    "v_cvt_pk_bf16_f32 v35, v38, v39\n"
    "v_cvt_pk_bf16_f32 v40, v40, v41\n"
    "v_cvt_pk_bf16_f32 v41, v42, v43\n"
    "v_cvt_pk_bf16_f32 v42, v44, v45\n"
    "v_cvt_pk_bf16_f32 v43, v46, v47\n"
    "v_cvt_pk_bf16_f32 v48, v48, v49\n"
    "v_cvt_pk_bf16_f32 v49, v50, v51\n"
    "v_cvt_pk_bf16_f32 v50, v52, v53\n"
    "v_cvt_pk_bf16_f32 v51, v54, v55\n"
    "v_cvt_pk_bf16_f32 v56, v56, v57\n"
    "v_cvt_pk_bf16_f32 v57, v58, v59\n"
    "v_cvt_pk_bf16_f32 v58, v60, v61\n"
    "v_cvt_pk_bf16_f32 v59, v62, v63\n"
    "buffer_store_dwordx4 v[32:35], v254, s[44:47], s85 offen offset:0 sc1\n"
    "buffer_store_dwordx4 v[40:43], v254, s[44:47], s85 offen offset:128 sc1\n"
    "buffer_store_dwordx4 v[48:51], v254, s[44:47], s85 offen offset:256 sc1\n"
    "buffer_store_dwordx4 v[56:59], v254, s[44:47], s85 offen offset:384 sc1\n"
    "s_barrier\n"
    "s_cmp_lg_u32 s70, 0\n"
    "s_cbranch_scc1 PI_att_3\n"
    "s_load_dwordx16 s[8:23], s[98:99], 0x100 glc\n"
    "PI_att_3:\n"
    "s_add_u32 m0, s82, 0x16000\n"
    "s_add_u32 s84, s79, 0x140000\n"
    "buffer_load_dwordx4 v104, s[36:39], s84 offen offset:0 lds\n"
    "buffer_load_dwordx4 v105, s[36:39], s84 offen offset:1024 lds\n"
    "s_add_u32 m0, s82, 0x18000\n"
    "s_add_u32 s84, s79, 0x90000\n"
    "buffer_load_dwordx4 v104, s[36:39], s84 offen offset:0 lds\n"
    "buffer_load_dwordx4 v105, s[36:39], s84 offen offset:1024 lds\n"
    "s_add_u32 m0, s82, 0x24000\n"
    "s_add_u32 s84, s91, 0x0\n"
    "buffer_load_dwordx4 v96, s[28:31], s84 offen offset:0 lds\n"
    "buffer_load_dwordx4 v97, s[28:31], s84 offen offset:1024 lds\n"
    "s_waitcnt lgkmcnt(0)\n"
    "s_cmp_lg_u32 s70, 0\n"
    "s_cbranch_scc1 POK_att_4\n"
    "s_mov_b32 s90, 0\n"
    "PS0_att_4:\n"
    "s_xor_b64 s[8:9], s[8:9], s[96:97]\n"
    "s_xor_b64 s[10:11], s[10:11], s[96:97]\n"
    "s_xor_b64 s[12:13], s[12:13], s[96:97]\n"
    "s_xor_b64 s[14:15], s[14:15], s[96:97]\n"
    "s_xor_b64 s[16:17], s[16:17], s[96:97]\n"
    "s_xor_b64 s[18:19], s[18:19], s[96:97]\n"
    "s_xor_b64 s[20:21], s[20:21], s[96:97]\n"
    "s_xor_b64 s[22:23], s[22:23], s[96:97]\n"
    "s_or_b64 s[8:9], s[8:9], s[10:11]\n"
    "s_or_b64 s[8:9], s[8:9], s[12:13]\n"
    "s_or_b64 s[8:9], s[8:9], s[14:15]\n"
    "s_or_b64 s[8:9], s[8:9], s[16:17]\n"
    "s_or_b64 s[8:9], s[8:9], s[18:19]\n"
    "s_or_b64 s[8:9], s[8:9], s[20:21]\n"
    "s_or_b64 s[8:9], s[8:9], s[22:23]\n"
    "s_cbranch_scc0 POK_att_4\n"
    "PS1_att_4:\n"
    "s_load_dwordx16 s[8:23], s[98:99], 0x100 glc\n"
    "s_add_u32 s90, s90, 1\n"
    "s_cmp_lt_u32 s90, 64\n"
    "s_waitcnt lgkmcnt(0)\n"
    "s_cbranch_scc1 PS0_att_4\n"
    "PSP_att_4:\n"
    "buffer_load_dwordx2 v[158:159], v156, s[48:51], 0 offen offset:256 sc1\n"
    "s_waitcnt vmcnt(0)\n"
    "v_cmp_ne_u64 vcc, s[96:97], v[158:159]\n"
    "s_nop 4\n"
    "s_cbranch_vccz POK_att_4\n"
    "s_add_u32 s90, s90, 1\n"
    "s_cmp_lt_u32 s90, 0x40000\n"
    "s_cbranch_scc1 PSP_att_4\n"
    "POK_att_4:\n"
    "s_waitcnt vmcnt(6)\n"
    "s_barrier\n"
    "buffer_store_dwordx2 v[164:165], v166, s[48:51], 0 offen offset:512 sc1\n"
    "s_add_u32 m0, s82, 0x1a000\n"
    "s_add_u32 s84, s79, 0x170000\n"
    "buffer_load_dwordx4 v104, s[36:39], s84 offen offset:0 lds\n"
    "buffer_load_dwordx4 v105, s[36:39], s84 offen offset:1024 lds\n"
    "s_barrier\n"
    "s_add_u32 m0, s82, 0x20000\n"
    "s_add_u32 s84, s79, 0xd0000\n"
    "buffer_load_dwordx4 v104, s[36:39], s84 offen offset:0 lds\n"
    "buffer_load_dwordx4 v105, s[36:39], s84 offen offset:1024 lds\n"
    "s_add_u32 m0, s82, 0x22000\n"
    "s_add_u32 s84, s79, 0x1b0000\n"
    "buffer_load_dwordx4 v104, s[36:39], s84 offen offset:0 lds\n"
    "buffer_load_dwordx4 v105, s[36:39], s84 offen offset:1024 lds\n"
    "s_barrier\n"
    "s_add_u32 m0, s82, 0x0\n"
    "s_add_u32 s84, s79, 0x30000\n"
    "buffer_load_dwordx4 v104, s[36:39], s84 offen offset:0 lds\n"
    "buffer_load_dwordx4 v105, s[36:39], s84 offen offset:1024 lds\n"
    "s_waitcnt vmcnt(9)\n"
    "s_barrier\n"
    "s_cmp_lg_u32 s70, 0\n"
    "s_cbranch_scc1 PI_att_5\n"
    "s_load_dwordx16 s[8:23], s[98:99], 0x180 glc\n"
    "PI_att_5:\n"
    "s_add_u32 m0, s82, 0x2000\n"
    "s_add_u32 s84, s79, 0x110000\n"
    "buffer_load_dwordx4 v104, s[36:39], s84 offen offset:0 lds\n"
    "buffer_load_dwordx4 v105, s[36:39], s84 offen offset:1024 lds\n"
    "s_add_u32 m0, s82, 0x1c000\n"
    "s_add_u32 s84, s79, 0xa0000\n"
    "buffer_load_dwordx4 v104, s[36:39], s84 offen offset:0 lds\n"
    "buffer_load_dwordx4 v105, s[36:39], s84 offen offset:1024 lds\n"
    "s_waitcnt lgkmcnt(0)\n"
    "s_cmp_lg_u32 s70, 0\n"
    "s_cbranch_scc1 POK_att_6\n"
    "s_mov_b32 s90, 0\n"
    "PS0_att_6:\n"
    "s_xor_b64 s[8:9], s[8:9], s[96:97]\n"
    "s_xor_b64 s[10:11], s[10:11], s[96:97]\n"
    "s_xor_b64 s[12:13], s[12:13], s[96:97]\n"
    "s_xor_b64 s[14:15], s[14:15], s[96:97]\n"
    "s_xor_b64 s[16:17], s[16:17], s[96:97]\n"
    "s_xor_b64 s[18:19], s[18:19], s[96:97]\n"
    "s_xor_b64 s[20:21], s[20:21], s[96:97]\n"
    "s_xor_b64 s[22:23], s[22:23], s[96:97]\n"
    "s_or_b64 s[8:9], s[8:9], s[10:11]\n"
    "s_or_b64 s[8:9], s[8:9], s[12:13]\n"
    "s_or_b64 s[8:9], s[8:9], s[14:15]\n"
    "s_or_b64 s[8:9], s[8:9], s[16:17]\n"
    "s_or_b64 s[8:9], s[8:9], s[18:19]\n"
    "s_or_b64 s[8:9], s[8:9], s[20:21]\n"
    "s_or_b64 s[8:9], s[8:9], s[22:23]\n"
    "s_cbranch_scc0 POK_att_6\n"
    "PS1_att_6:\n"
    "s_load_dwordx16 s[8:23], s[98:99], 0x180 glc\n"
    "s_add_u32 s90, s90, 1\n"
    "s_cmp_lt_u32 s90, 64\n"
    "s_waitcnt lgkmcnt(0)\n"
    "s_cbranch_scc1 PS0_att_6\n"
    "PSP_att_6:\n"
    "buffer_load_dwordx2 v[158:159], v156, s[48:51], 0 offen offset:384 sc1\n"
    "s_waitcnt vmcnt(0)\n"
    "v_cmp_ne_u64 vcc, s[96:97], v[158:159]\n"
    "s_nop 4\n"
    "s_cbranch_vccz POK_att_6\n"
    "s_add_u32 s90, s90, 1\n"
    "s_cmp_lt_u32 s90, 0x40000\n"
    "s_cbranch_scc1 PSP_att_6\n"
    "POK_att_6:\n"
    "s_waitcnt vmcnt(10)\n"
    "s_barrier\n"
    "s_add_u32 m0, s82, 0x1e000\n"
    "s_add_u32 s84, s79, 0x180000\n"
    "buffer_load_dwordx4 v104, s[36:39], s84 offen offset:0 lds\n"
    "buffer_load_dwordx4 v105, s[36:39], s84 offen offset:1024 lds\n"
    "s_waitcnt vmcnt(8)\n"
    "s_barrier\n"
    "s_cmp_lg_u32 s70, 0\n"
    "s_cbranch_scc1 PI_att_7\n"
    "s_load_dwordx16 s[8:23], s[98:99], 0x200 glc\n"
    "PI_att_7:\n"
    "s_add_u32 m0, s82, 0x4000\n"
    "s_add_u32 s84, s79, 0x0\n"
    "buffer_load_dwordx4 v104, s[40:43], s84 offen offset:0 lds\n"
    "buffer_load_dwordx4 v105, s[40:43], s84 offen offset:1024 lds\n"
    "s_add_u32 m0, s82, 0x6000\n"
    "s_add_u32 s84, s79, 0x40000\n"
    "buffer_load_dwordx4 v104, s[40:43], s84 offen offset:0 lds\n"
    "buffer_load_dwordx4 v105, s[40:43], s84 offen offset:1024 lds\n"
    "s_waitcnt lgkmcnt(0)\n"
    "s_cmp_lg_u32 s70, 0\n"
    "s_cbranch_scc1 POK_att_8\n"
    "s_mov_b32 s90, 0\n"
    "PS0_att_8:\n"
    "s_xor_b64 s[8:9], s[8:9], s[96:97]\n"
    "s_xor_b64 s[10:11], s[10:11], s[96:97]\n"
    "s_xor_b64 s[12:13], s[12:13], s[96:97]\n"
    "s_xor_b64 s[14:15], s[14:15], s[96:97]\n"
    "s_xor_b64 s[16:17], s[16:17], s[96:97]\n"
    "s_xor_b64 s[18:19], s[18:19], s[96:97]\n"
    "s_xor_b64 s[20:21], s[20:21], s[96:97]\n"
    "s_xor_b64 s[22:23], s[22:23], s[96:97]\n"
    "s_or_b64 s[8:9], s[8:9], s[10:11]\n"
    "s_or_b64 s[8:9], s[8:9], s[12:13]\n"
    "s_or_b64 s[8:9], s[8:9], s[14:15]\n"
    "s_or_b64 s[8:9], s[8:9], s[16:17]\n"
    "s_or_b64 s[8:9], s[8:9], s[18:19]\n"
    "s_or_b64 s[8:9], s[8:9], s[20:21]\n"
    "s_or_b64 s[8:9], s[8:9], s[22:23]\n"
    "s_cbranch_scc0 POK_att_8\n"
    "PS1_att_8:\n"
    "s_load_dwordx16 s[8:23], s[98:99], 0x200 glc\n"
    "s_add_u32 s90, s90, 1\n"
    "s_cmp_lt_u32 s90, 64\n"
    "s_waitcnt lgkmcnt(0)\n"
    "s_cbranch_scc1 PS0_att_8\n"
    "PSP_att_8:\n"
    "buffer_load_dwordx2 v[158:159], v156, s[48:51], 0 offen offset:512 sc1\n"
    "s_waitcnt vmcnt(0)\n"
    "v_cmp_ne_u64 vcc, s[96:97], v[158:159]\n"
    "s_nop 4\n"
    "s_cbranch_vccz POK_att_8\n"
    "s_add_u32 s90, s90, 1\n"
    "s_cmp_lt_u32 s90, 0x40000\n"
    "s_cbranch_scc1 PSP_att_8\n"
    "POK_att_8:\n"
    "v_mov_b32 v160, s96\n"
    "v_mov_b32 v161, s97\n"
    "s_waitcnt vmcnt(8)\n"
    "s_barrier\n"
    "s_add_u32 m0, s82, 0x8000\n"
    "s_add_u32 s84, s79, 0x10000\n"
    "buffer_load_dwordx4 v104, s[40:43], s84 offen offset:0 lds\n"
    "buffer_load_dwordx4 v105, s[40:43], s84 offen offset:1024 lds\n"
    "s_add_u32 m0, s82, 0xa000\n"
    "s_add_u32 s84, s79, 0x50000\n"
    "buffer_load_dwordx4 v104, s[40:43], s84 offen offset:0 lds\n"
    "buffer_load_dwordx4 v105, s[40:43], s84 offen offset:1024 lds\n"
    "s_waitcnt vmcnt(8)\n"
    "s_barrier\n"
    "s_add_u32 m0, s82, 0xc000\n"
    "s_add_u32 s84, s79, 0x20000\n"
    "buffer_load_dwordx4 v104, s[40:43], s84 offen offset:0 lds\n"
    "buffer_load_dwordx4 v105, s[40:43], s84 offen offset:1024 lds\n"
    "s_add_u32 m0, s82, 0xe000\n"
    "s_add_u32 s84, s79, 0x60000\n"
    "buffer_load_dwordx4 v104, s[40:43], s84 offen offset:0 lds\n"
    "buffer_load_dwordx4 v105, s[40:43], s84 offen offset:1024 lds\n"
    "s_waitcnt vmcnt(8)\n"
    "s_barrier\n"
    "s_waitcnt vmcnt(4)\n"
    "s_barrier\n"
    "s_add_u32 m0, s82, 0x10000\n"
    "s_add_u32 s84, s79, 0x30000\n"
    "buffer_load_dwordx4 v104, s[40:43], s84 offen offset:0 lds\n"
    "buffer_load_dwordx4 v105, s[40:43], s84 offen offset:1024 lds\n"
    "s_add_u32 m0, s82, 0x12000\n"
    "s_add_u32 s84, s79, 0x70000\n"
    "buffer_load_dwordx4 v104, s[40:43], s84 offen offset:0 lds\n"
    "buffer_load_dwordx4 v105, s[40:43], s84 offen offset:1024 lds\n"
    "s_waitcnt vmcnt(4)\n"
    "s_barrier\n"
    "s_waitcnt vmcnt(0)\n"
    "s_barrier\n"
    "buffer_store_dwordx2 v[160:161], v162, s[48:51], 0 offen offset:640\n"
    "s_barrier\n"
    "s_endpgm\n"
    "LBL_C_ffn:\n"
    "s_setprio 1\n"
    "v_lshrrev_b32 v224, 5, v255\n"
    "v_bfe_u32 v225, v255, 2, 2\n"
    "v_bfe_u32 v226, v255, 4, 1\n"
    "v_bfe_u32 v227, v255, 1, 1\n"
    "v_lshl_add_u32 v226, v226, 1, v227\n"
    "v_and_b32 v227, 1, v255\n"
    "v_lshlrev_b32 v227, 3, v227\n"
    "v_bfe_u32 v228, v255, 3, 1\n"
    "v_lshlrev_b32 v228, 2, v228\n"
    "v_lshlrev_b32 v229, 1, v224\n"
    "v_lshl_add_u32 v230, v224, 3, v225\n"
    "v_xor_b32 v231, v226, v229\n"
    "v_lshlrev_b32 v233, 6, v230\n"
    "v_lshl_add_u32 v233, v231, 4, v233\n"
    "v_add_u32 v229, v233, v227\n"
    "v_xor_b32 v231, v231, v228\n"
    "v_lshlrev_b32 v232, 7, v230\n"
    "v_lshl_add_u32 v232, v231, 4, v232\n"
    "v_add_u32 v232, v232, v227\n"
    "v_xor_b32 v226, v226, v228\n"
    "v_lshl_add_u32 v230, v224, 2, v225\n"
    "v_lshlrev_b32 v228, 7, v230\n"
    "v_lshl_add_u32 v228, v226, 4, v228\n"
    "v_add_u32 v228, v228, v227\n"
    "s_lshr_b32 s72, s70, 1\n"
    "s_mul_i32 s72, s72, 8256\n"
    "v_xor_b32 v228, s72, v228\n"
    "v_add_u32 v234, 0x10000, v232\n"
    "v_add_u32 v236, 0x20000, v232\n"
    "v_xor_b32 v233, 64, v232\n"
    "v_xor_b32 v235, 64, v234\n"
    "v_xor_b32 v237, 64, v236\n"
    "v_and_b32 v225, 31, v255\n"
    "v_bfe_u32 v226, v255, 1, 3\n"
    "v_xor_b32 v226, v226, v224\n"
    "v_lshlrev_b32 v160, 7, v225\n"
    "v_lshl_add_u32 v160, v226, 4, v160\n"
    "s_lshl_b32 s72, s70, 12\n"
    "v_add_u32 v160, s72, v160\n"
    "v_xor_b32 v161, 32, v160\n"
    "v_xor_b32 v162, 64, v160\n"
    "v_xor_b32 v163, 96, v160\n"
    "v_add_u32 v164, 0x10000, v160\n"
    "v_add_u32 v165, 0x10000, v161\n"
    "v_add_u32 v166, 0x10000, v162\n"
    "v_add_u32 v167, 0x10000, v163\n"
    "v_add_u32 v168, 0x20000, v160\n"
    "v_add_u32 v169, 0x20000, v161\n"
    "v_add_u32 v170, 0x20000, v162\n"
    "v_add_u32 v171, 0x20000, v163\n"
    "v_lshlrev_b32 v238, 4, v255\n"
    "v_lshlrev_b32 v248, 3, v225\n"
    "v_add_u32 v248, 0x27800, v248\n"
    "v_mov_b32 v240, 0\n"
    "v_mov_b32 v241, 0\n"
    "v_mov_b32 v242, 0\n"
    "v_mov_b32 v243, 0\n"
    "v_mov_b32 v244, 0\n"
    "v_mov_b32 v245, 0\n"
    "v_mov_b32 v246, 0\n"
    "v_mov_b32 v247, 0\n"
    "v_mov_b32 v252, 0\n"
    "v_mov_b32 v253, 0\n"
    "v_mov_b32 v172, 0x3f803f80\n"
    "v_add_u32 v250, -1, v224\n"
    "s_waitcnt lgkmcnt(0)\n"
    "s_mov_b32 s44, s24\n"
    "s_and_b32 s45, s25, 0xffff\n"
    "s_mov_b32 s46, 0x900000\n"
    "s_mov_b32 s47, 0x20000\n"
    "s_mov_b32 s52, s22\n"
    "s_and_b32 s53, s23, 0xffff\n"
    "s_mov_b32 s54, 0x200000\n"
    "s_mov_b32 s55, 0x20000\n"
    "s_mov_b32 s60, s20\n"
    "s_and_b32 s61, s21, 0xffff\n"
    "s_mov_b32 s62, 0x404\n"
    "s_mov_b32 s63, 0x20000\n"
    "v_and_b32 v225, 31, v255\n"
    "v_lshlrev_b32 v225, 1, v225\n"
    "s_lshr_b32 s72, s70, 1\n"
    "s_lshl_b32 s72, s72, 6\n"
    "s_lshl_b32 s73, s69, 7\n"
    "s_add_u32 s72, s72, s73\n"
    "buffer_load_ushort v230, v225, s[60:63], s72 offen\n"
    "s_lshl_b32 s72, s65, 2\n"
    "s_add_u32 s72, s72, s70\n"
    "s_mul_i32 s72, s72, 147456\n"
    "s_lshr_b32 s73, s66, 2\n"
    "s_mul_i32 s73, s73, 73728\n"
    "s_add_u32 s72, s72, s73\n"
    "s_and_b32 s73, s66, 3\n"
    "s_lshl_b32 s74, s73, 12\n"
    "s_add_u32 s85, s72, s74\n"
    "s_lshl_b32 s74, s73, 11\n"
    "s_add_u32 s86, s72, s74\n"
    "s_add_u32 s86, s86, 49152\n"
    "s_lshl_b32 s72, s68, 1\n"
    "s_and_b32 s73, s70, 1\n"
    "s_add_u32 s72, s72, s73\n"
    "s_mul_i32 s72, s72, 147456\n"
    "s_lshr_b32 s73, s70, 1\n"
    "s_mul_i32 s73, s73, 73728\n"
    "s_add_u32 s87, s72, s73\n"
    "s_barrier\n"
    "ds_read_b128 v[0:3], v160 offset:0\n"
    "ds_read_b128 v[4:7], v161 offset:0\n"
    "ds_read_b128 v[8:11], v162 offset:0\n"
    "ds_read_b128 v[12:15], v163 offset:0\n"
    "ds_read_b64_tr_b16 v[192:193], v232 offset:16384\n"
    "ds_read_b64_tr_b16 v[194:195], v232 offset:16896\n"
    "ds_read_b64_tr_b16 v[196:197], v233 offset:16384\n"
    "ds_read_b64_tr_b16 v[198:199], v233 offset:16896\n"
    "ds_read_b64_tr_b16 v[200:201], v232 offset:18432\n"
    "ds_read_b64_tr_b16 v[202:203], v232 offset:18944\n"
    "ds_read_b64_tr_b16 v[204:205], v233 offset:18432\n"
    "ds_read_b64_tr_b16 v[206:207], v233 offset:18944\n"
    "ds_read_b64_tr_b16 v[208:209], v232 offset:20480\n"
    "ds_read_b64_tr_b16 v[210:211], v232 offset:20992\n"
    "ds_read_b64_tr_b16 v[212:213], v233 offset:20480\n"
    "ds_read_b64_tr_b16 v[214:215], v233 offset:20992\n"
    "s_waitcnt lgkmcnt(10)\n"
    "v_mfma_f32_32x32x16_bf16 v[128:143], v[192:195], v[0:3], 0\n"
    "s_waitcnt lgkmcnt(8)\n"
    "ds_read_b64_tr_b16 v[216:217], v232 offset:22528\n"
    "ds_read_b64_tr_b16 v[218:219], v232 offset:23040\n"
    "v_mfma_f32_32x32x16_bf16 v[144:159], v[196:199], v[0:3], 0\n"
    "s_waitcnt lgkmcnt(8)\n"
    "ds_read_b64_tr_b16 v[220:221], v233 offset:22528\n"
    "ds_read_b64_tr_b16 v[222:223], v233 offset:23040\n"
    "v_mfma_f32_32x32x16_bf16 v[128:143], v[200:203], v[4:7], v[128:143]\n"
    "s_waitcnt lgkmcnt(8)\n"
    "v_mfma_f32_32x32x16_bf16 v[144:159], v[204:207], v[4:7], v[144:159]\n"
    "s_waitcnt lgkmcnt(6)\n"
    "v_dot2c_f32_bf16 v252, v0, v172\n"
    "v_dot2c_f32_bf16 v253, v0, v0\n"
    "v_dot2c_f32_bf16 v252, v1, v172\n"
    "v_dot2c_f32_bf16 v253, v1, v1\n"
    "v_dot2c_f32_bf16 v252, v2, v172\n"
    "v_dot2c_f32_bf16 v253, v2, v2\n"
    "v_dot2c_f32_bf16 v252, v3, v172\n"
    "v_dot2c_f32_bf16 v253, v3, v3\n"
    "v_dot2c_f32_bf16 v252, v4, v172\n"
    "v_dot2c_f32_bf16 v253, v4, v4\n"
    "v_dot2c_f32_bf16 v252, v5, v172\n"
    "v_dot2c_f32_bf16 v253, v5, v5\n"
    "v_dot2c_f32_bf16 v252, v6, v172\n"
    "v_dot2c_f32_bf16 v253, v6, v6\n"
    "v_dot2c_f32_bf16 v252, v7, v172\n"
    "v_dot2c_f32_bf16 v253, v7, v7\n"
    "v_dot2c_f32_bf16 v252, v8, v172\n"
    "v_dot2c_f32_bf16 v253, v8, v8\n"
    "v_dot2c_f32_bf16 v252, v9, v172\n"
    "v_dot2c_f32_bf16 v253, v9, v9\n"
    "v_dot2c_f32_bf16 v252, v10, v172\n"
    "v_dot2c_f32_bf16 v253, v10, v10\n"
    "v_dot2c_f32_bf16 v252, v11, v172\n"
    "v_dot2c_f32_bf16 v253, v11, v11\n"
    "v_dot2c_f32_bf16 v252, v12, v172\n"
    "v_dot2c_f32_bf16 v253, v12, v12\n"
    "v_dot2c_f32_bf16 v252, v13, v172\n"
    "v_dot2c_f32_bf16 v253, v13, v13\n"
    "v_dot2c_f32_bf16 v252, v14, v172\n"
    "v_dot2c_f32_bf16 v253, v14, v14\n"
    "v_dot2c_f32_bf16 v252, v15, v172\n"
    "v_dot2c_f32_bf16 v253, v15, v15\n"
    "v_mfma_f32_32x32x16_bf16 v[128:143], v[208:211], v[8:11], v[128:143]\n"
    "s_waitcnt lgkmcnt(0)\n"
    "s_barrier\n"
    "v_mfma_f32_32x32x16_bf16 v[144:159], v[212:215], v[8:11], v[144:159]\n"
    "ds_read_b128 v[16:19], v160 offset:32768\n"
    "ds_read_b128 v[20:23], v161 offset:32768\n"
    "ds_read_b128 v[24:27], v162 offset:32768\n"
    "ds_read_b128 v[28:31], v163 offset:32768\n"
    "ds_read_b64_tr_b16 v[192:193], v232 offset:24576\n"
    "ds_read_b64_tr_b16 v[194:195], v232 offset:25088\n"
    "ds_read_b64_tr_b16 v[196:197], v233 offset:24576\n"
    "ds_read_b64_tr_b16 v[198:199], v233 offset:25088\n"
    "v_mfma_f32_32x32x16_bf16 v[128:143], v[216:219], v[12:15], v[128:143]\n"
    "ds_read_b64_tr_b16 v[200:201], v232 offset:26624\n"
    "ds_read_b64_tr_b16 v[202:203], v232 offset:27136\n"
    "ds_read_b64_tr_b16 v[204:205], v233 offset:26624\n"
    "ds_read_b64_tr_b16 v[206:207], v233 offset:27136\n"
    "v_mfma_f32_32x32x16_bf16 v[144:159], v[220:223], v[12:15], v[144:159]\n"
    "s_waitcnt lgkmcnt(6)\n"
    "ds_read_b64_tr_b16 v[208:209], v232 offset:28672\n"
    "ds_read_b64_tr_b16 v[210:211], v232 offset:29184\n"
    "ds_read_b64_tr_b16 v[212:213], v233 offset:28672\n"
    "ds_read_b64_tr_b16 v[214:215], v233 offset:29184\n"
    "v_mfma_f32_32x32x16_bf16 v[128:143], v[192:195], v[16:19], v[128:143]\n"
    "s_waitcnt lgkmcnt(8)\n"
    "ds_read_b64_tr_b16 v[216:217], v232 offset:30720\n"
    "ds_read_b64_tr_b16 v[218:219], v232 offset:31232\n"
    "v_mfma_f32_32x32x16_bf16 v[144:159], v[196:199], v[16:19], v[144:159]\n"
    "s_waitcnt lgkmcnt(8)\n"
    "ds_read_b64_tr_b16 v[220:221], v233 offset:30720\n"
    "ds_read_b64_tr_b16 v[222:223], v233 offset:31232\n"
    "v_mfma_f32_32x32x16_bf16 v[128:143], v[200:203], v[20:23], v[128:143]\n"
    "s_waitcnt lgkmcnt(8)\n"
    "v_mfma_f32_32x32x16_bf16 v[144:159], v[204:207], v[20:23], v[144:159]\n"
    "s_waitcnt lgkmcnt(6)\n"
    "v_dot2c_f32_bf16 v252, v16, v172\n"
    "v_dot2c_f32_bf16 v253, v16, v16\n"
    "v_dot2c_f32_bf16 v252, v17, v172\n"
    "v_dot2c_f32_bf16 v253, v17, v17\n"
    "v_dot2c_f32_bf16 v252, v18, v172\n"
    "v_dot2c_f32_bf16 v253, v18, v18\n"
    "v_dot2c_f32_bf16 v252, v19, v172\n"
    "v_dot2c_f32_bf16 v253, v19, v19\n"
    "v_dot2c_f32_bf16 v252, v20, v172\n"
    "v_dot2c_f32_bf16 v253, v20, v20\n"
    "v_dot2c_f32_bf16 v252, v21, v172\n"
    "v_dot2c_f32_bf16 v253, v21, v21\n"
    "v_dot2c_f32_bf16 v252, v22, v172\n"
    "v_dot2c_f32_bf16 v253, v22, v22\n"
    "v_dot2c_f32_bf16 v252, v23, v172\n"
    "v_dot2c_f32_bf16 v253, v23, v23\n"
    "v_dot2c_f32_bf16 v252, v24, v172\n"
    "v_dot2c_f32_bf16 v253, v24, v24\n"
    "v_dot2c_f32_bf16 v252, v25, v172\n"
    "v_dot2c_f32_bf16 v253, v25, v25\n"
    "v_dot2c_f32_bf16 v252, v26, v172\n"
    "v_dot2c_f32_bf16 v253, v26, v26\n"
    "v_dot2c_f32_bf16 v252, v27, v172\n"
    "v_dot2c_f32_bf16 v253, v27, v27\n"
    "v_dot2c_f32_bf16 v252, v28, v172\n"
    "v_dot2c_f32_bf16 v253, v28, v28\n"
    "v_dot2c_f32_bf16 v252, v29, v172\n"
    "v_dot2c_f32_bf16 v253, v29, v29\n"
    "v_dot2c_f32_bf16 v252, v30, v172\n"
    "v_dot2c_f32_bf16 v253, v30, v30\n"
    "v_dot2c_f32_bf16 v252, v31, v172\n"
    "v_dot2c_f32_bf16 v253, v31, v31\n"
    "v_mfma_f32_32x32x16_bf16 v[128:143], v[208:211], v[24:27], v[128:143]\n"
    "s_waitcnt lgkmcnt(0)\n"
    "s_barrier\n"
    "v_mfma_f32_32x32x16_bf16 v[144:159], v[212:215], v[24:27], v[144:159]\n"
    "ds_read_b128 v[32:35], v160 offset:49152\n"
    "ds_read_b128 v[36:39], v161 offset:49152\n"
    "ds_read_b128 v[40:43], v162 offset:49152\n"
    "ds_read_b128 v[44:47], v163 offset:49152\n"
    "ds_read_b64_tr_b16 v[192:193], v234 offset:0\n"
    "ds_read_b64_tr_b16 v[194:195], v234 offset:512\n"
    "ds_read_b64_tr_b16 v[196:197], v235 offset:0\n"
    "ds_read_b64_tr_b16 v[198:199], v235 offset:512\n"
    "v_mfma_f32_32x32x16_bf16 v[128:143], v[216:219], v[28:31], v[128:143]\n"
    "ds_read_b64_tr_b16 v[200:201], v234 offset:2048\n"
    "ds_read_b64_tr_b16 v[202:203], v234 offset:2560\n"
    "ds_read_b64_tr_b16 v[204:205], v235 offset:2048\n"
    "ds_read_b64_tr_b16 v[206:207], v235 offset:2560\n"
    "v_mfma_f32_32x32x16_bf16 v[144:159], v[220:223], v[28:31], v[144:159]\n"
    "s_waitcnt lgkmcnt(6)\n"
    "ds_read_b64_tr_b16 v[208:209], v234 offset:4096\n"
    "ds_read_b64_tr_b16 v[210:211], v234 offset:4608\n"
    "ds_read_b64_tr_b16 v[212:213], v235 offset:4096\n"
    "ds_read_b64_tr_b16 v[214:215], v235 offset:4608\n"
    "v_mfma_f32_32x32x16_bf16 v[128:143], v[192:195], v[32:35], v[128:143]\n"
    "s_waitcnt lgkmcnt(8)\n"
    "ds_read_b64_tr_b16 v[216:217], v234 offset:6144\n"
    "ds_read_b64_tr_b16 v[218:219], v234 offset:6656\n"
    "v_mfma_f32_32x32x16_bf16 v[144:159], v[196:199], v[32:35], v[144:159]\n"
    "s_waitcnt lgkmcnt(8)\n"
    "ds_read_b64_tr_b16 v[220:221], v235 offset:6144\n"
    "ds_read_b64_tr_b16 v[222:223], v235 offset:6656\n"
    "v_mfma_f32_32x32x16_bf16 v[128:143], v[200:203], v[36:39], v[128:143]\n"
    "s_waitcnt lgkmcnt(8)\n"
    "v_mfma_f32_32x32x16_bf16 v[144:159], v[204:207], v[36:39], v[144:159]\n"
    "s_waitcnt lgkmcnt(6)\n"
    "v_dot2c_f32_bf16 v252, v32, v172\n"
    "v_dot2c_f32_bf16 v253, v32, v32\n"
    "v_dot2c_f32_bf16 v252, v33, v172\n"
    "v_dot2c_f32_bf16 v253, v33, v33\n"
    "v_dot2c_f32_bf16 v252, v34, v172\n"
    "v_dot2c_f32_bf16 v253, v34, v34\n"
    "v_dot2c_f32_bf16 v252, v35, v172\n"
    "v_dot2c_f32_bf16 v253, v35, v35\n"
    "v_dot2c_f32_bf16 v252, v36, v172\n"
    "v_dot2c_f32_bf16 v253, v36, v36\n"
    "v_dot2c_f32_bf16 v252, v37, v172\n"
    "v_dot2c_f32_bf16 v253, v37, v37\n"
    "v_dot2c_f32_bf16 v252, v38, v172\n"
    "v_dot2c_f32_bf16 v253, v38, v38\n"
    "v_dot2c_f32_bf16 v252, v39, v172\n"
    "v_dot2c_f32_bf16 v253, v39, v39\n"
    "v_dot2c_f32_bf16 v252, v40, v172\n"
    "v_dot2c_f32_bf16 v253, v40, v40\n"
    "v_dot2c_f32_bf16 v252, v41, v172\n"
    "v_dot2c_f32_bf16 v253, v41, v41\n"
    "v_dot2c_f32_bf16 v252, v42, v172\n"
    "v_dot2c_f32_bf16 v253, v42, v42\n"
    "v_dot2c_f32_bf16 v252, v43, v172\n"
    "v_dot2c_f32_bf16 v253, v43, v43\n"
    "v_dot2c_f32_bf16 v252, v44, v172\n"
    "v_dot2c_f32_bf16 v253, v44, v44\n"
    "v_dot2c_f32_bf16 v252, v45, v172\n"
    "v_dot2c_f32_bf16 v253, v45, v45\n"
    "v_dot2c_f32_bf16 v252, v46, v172\n"
    "v_dot2c_f32_bf16 v253, v46, v46\n"
    "v_dot2c_f32_bf16 v252, v47, v172\n"
    "v_dot2c_f32_bf16 v253, v47, v47\n"
    "v_mfma_f32_32x32x16_bf16 v[128:143], v[208:211], v[40:43], v[128:143]\n"
    "s_waitcnt lgkmcnt(0)\n"
    "s_barrier\n"
    "v_mfma_f32_32x32x16_bf16 v[144:159], v[212:215], v[40:43], v[144:159]\n"
    "ds_read_b128 v[48:51], v164 offset:16384\n"
    "ds_read_b128 v[52:55], v165 offset:16384\n"
    "ds_read_b128 v[56:59], v166 offset:16384\n"
    "ds_read_b128 v[60:63], v167 offset:16384\n"
    "ds_read_b64_tr_b16 v[192:193], v234 offset:8192\n"
    "ds_read_b64_tr_b16 v[194:195], v234 offset:8704\n"
    "ds_read_b64_tr_b16 v[196:197], v235 offset:8192\n"
    "ds_read_b64_tr_b16 v[198:199], v235 offset:8704\n"
    "v_mfma_f32_32x32x16_bf16 v[128:143], v[216:219], v[44:47], v[128:143]\n"
    "ds_read_b64_tr_b16 v[200:201], v234 offset:10240\n"
    "ds_read_b64_tr_b16 v[202:203], v234 offset:10752\n"
    "ds_read_b64_tr_b16 v[204:205], v235 offset:10240\n"
    "ds_read_b64_tr_b16 v[206:207], v235 offset:10752\n"
    "v_mfma_f32_32x32x16_bf16 v[144:159], v[220:223], v[44:47], v[144:159]\n"
    "s_waitcnt lgkmcnt(6)\n"
    "ds_read_b64_tr_b16 v[208:209], v234 offset:12288\n"
    "ds_read_b64_tr_b16 v[210:211], v234 offset:12800\n"
    "ds_read_b64_tr_b16 v[212:213], v235 offset:12288\n"
    "ds_read_b64_tr_b16 v[214:215], v235 offset:12800\n"
    "v_mfma_f32_32x32x16_bf16 v[128:143], v[192:195], v[48:51], v[128:143]\n"
    "s_waitcnt lgkmcnt(8)\n"
    "ds_read_b64_tr_b16 v[216:217], v234 offset:14336\n"
    "ds_read_b64_tr_b16 v[218:219], v234 offset:14848\n"
    "v_mfma_f32_32x32x16_bf16 v[144:159], v[196:199], v[48:51], v[144:159]\n"
    "s_waitcnt lgkmcnt(8)\n"
    "ds_read_b64_tr_b16 v[220:221], v235 offset:14336\n"
    "ds_read_b64_tr_b16 v[222:223], v235 offset:14848\n"
    "v_mfma_f32_32x32x16_bf16 v[128:143], v[200:203], v[52:55], v[128:143]\n"
    "s_waitcnt lgkmcnt(8)\n"
    "v_mfma_f32_32x32x16_bf16 v[144:159], v[204:207], v[52:55], v[144:159]\n"
    "s_waitcnt lgkmcnt(6)\n"
    "v_dot2c_f32_bf16 v252, v48, v172\n"
    "v_dot2c_f32_bf16 v253, v48, v48\n"
    "v_dot2c_f32_bf16 v252, v49, v172\n"
    "v_dot2c_f32_bf16 v253, v49, v49\n"
    "v_dot2c_f32_bf16 v252, v50, v172\n"
    "v_dot2c_f32_bf16 v253, v50, v50\n"
    "v_dot2c_f32_bf16 v252, v51, v172\n"
    "v_dot2c_f32_bf16 v253, v51, v51\n"
    "v_dot2c_f32_bf16 v252, v52, v172\n"
    "v_dot2c_f32_bf16 v253, v52, v52\n"
    "v_dot2c_f32_bf16 v252, v53, v172\n"
    "v_dot2c_f32_bf16 v253, v53, v53\n"
    "v_dot2c_f32_bf16 v252, v54, v172\n"
    "v_dot2c_f32_bf16 v253, v54, v54\n"
    "v_dot2c_f32_bf16 v252, v55, v172\n"
    "v_dot2c_f32_bf16 v253, v55, v55\n"
    "v_dot2c_f32_bf16 v252, v56, v172\n"
    "v_dot2c_f32_bf16 v253, v56, v56\n"
    "v_dot2c_f32_bf16 v252, v57, v172\n"
    "v_dot2c_f32_bf16 v253, v57, v57\n"
    "v_dot2c_f32_bf16 v252, v58, v172\n"
    "v_dot2c_f32_bf16 v253, v58, v58\n"
    "v_dot2c_f32_bf16 v252, v59, v172\n"
    "v_dot2c_f32_bf16 v253, v59, v59\n"
    "v_dot2c_f32_bf16 v252, v60, v172\n"
    "v_dot2c_f32_bf16 v253, v60, v60\n"
    "v_dot2c_f32_bf16 v252, v61, v172\n"
    "v_dot2c_f32_bf16 v253, v61, v61\n"
    "v_dot2c_f32_bf16 v252, v62, v172\n"
    "v_dot2c_f32_bf16 v253, v62, v62\n"
    "v_dot2c_f32_bf16 v252, v63, v172\n"
    "v_dot2c_f32_bf16 v253, v63, v63\n"
    "v_mfma_f32_32x32x16_bf16 v[128:143], v[208:211], v[56:59], v[128:143]\n"
    "s_waitcnt lgkmcnt(0)\n"
    "s_barrier\n"
    "v_mfma_f32_32x32x16_bf16 v[144:159], v[212:215], v[56:59], v[144:159]\n"
    "ds_read_b128 v[64:67], v164 offset:32768\n"
    "ds_read_b128 v[68:71], v165 offset:32768\n"
    "ds_read_b128 v[72:75], v166 offset:32768\n"
    "ds_read_b128 v[76:79], v167 offset:32768\n"
    "ds_read_b64_tr_b16 v[192:193], v234 offset:49152\n"
    "ds_read_b64_tr_b16 v[194:195], v234 offset:49664\n"
    "ds_read_b64_tr_b16 v[196:197], v235 offset:49152\n"
    "ds_read_b64_tr_b16 v[198:199], v235 offset:49664\n"
    "v_mfma_f32_32x32x16_bf16 v[128:143], v[216:219], v[60:63], v[128:143]\n"
    "ds_read_b64_tr_b16 v[200:201], v234 offset:51200\n"
    "ds_read_b64_tr_b16 v[202:203], v234 offset:51712\n"
    "ds_read_b64_tr_b16 v[204:205], v235 offset:51200\n"
    "ds_read_b64_tr_b16 v[206:207], v235 offset:51712\n"
    "v_mfma_f32_32x32x16_bf16 v[144:159], v[220:223], v[60:63], v[144:159]\n"
    "s_waitcnt lgkmcnt(6)\n"
    "ds_read_b64_tr_b16 v[208:209], v234 offset:53248\n"
    "ds_read_b64_tr_b16 v[210:211], v234 offset:53760\n"
    "ds_read_b64_tr_b16 v[212:213], v235 offset:53248\n"
    "ds_read_b64_tr_b16 v[214:215], v235 offset:53760\n"
    "v_mfma_f32_32x32x16_bf16 v[128:143], v[192:195], v[64:67], v[128:143]\n"
    "s_waitcnt lgkmcnt(8)\n"
    "ds_read_b64_tr_b16 v[216:217], v234 offset:55296\n"
    "ds_read_b64_tr_b16 v[218:219], v234 offset:55808\n"
    "v_mfma_f32_32x32x16_bf16 v[144:159], v[196:199], v[64:67], v[144:159]\n"
    "s_waitcnt lgkmcnt(8)\n"
    "ds_read_b64_tr_b16 v[220:221], v235 offset:55296\n"
    "ds_read_b64_tr_b16 v[222:223], v235 offset:55808\n"
    "v_mfma_f32_32x32x16_bf16 v[128:143], v[200:203], v[68:71], v[128:143]\n"
    "s_waitcnt lgkmcnt(8)\n"
    "v_mfma_f32_32x32x16_bf16 v[144:159], v[204:207], v[68:71], v[144:159]\n"
    "s_waitcnt lgkmcnt(6)\n"
    "v_dot2c_f32_bf16 v252, v64, v172\n"
    "v_dot2c_f32_bf16 v253, v64, v64\n"
    "v_dot2c_f32_bf16 v252, v65, v172\n"
    "v_dot2c_f32_bf16 v253, v65, v65\n"
    "v_dot2c_f32_bf16 v252, v66, v172\n"
    "v_dot2c_f32_bf16 v253, v66, v66\n"
    "v_dot2c_f32_bf16 v252, v67, v172\n"
    "v_dot2c_f32_bf16 v253, v67, v67\n"
    "v_dot2c_f32_bf16 v252, v68, v172\n"
    "v_dot2c_f32_bf16 v253, v68, v68\n"
    "v_dot2c_f32_bf16 v252, v69, v172\n"
    "v_dot2c_f32_bf16 v253, v69, v69\n"
    "v_dot2c_f32_bf16 v252, v70, v172\n"
    "v_dot2c_f32_bf16 v253, v70, v70\n"
    "v_dot2c_f32_bf16 v252, v71, v172\n"
    "v_dot2c_f32_bf16 v253, v71, v71\n"
    "v_dot2c_f32_bf16 v252, v72, v172\n"
    "v_dot2c_f32_bf16 v253, v72, v72\n"
    "v_dot2c_f32_bf16 v252, v73, v172\n"
    "v_dot2c_f32_bf16 v253, v73, v73\n"
    "v_dot2c_f32_bf16 v252, v74, v172\n"
    "v_dot2c_f32_bf16 v253, v74, v74\n"
    "v_dot2c_f32_bf16 v252, v75, v172\n"
    "v_dot2c_f32_bf16 v253, v75, v75\n"
    "v_dot2c_f32_bf16 v252, v76, v172\n"
    "v_dot2c_f32_bf16 v253, v76, v76\n"
    "v_dot2c_f32_bf16 v252, v77, v172\n"
    "v_dot2c_f32_bf16 v253, v77, v77\n"
    "v_dot2c_f32_bf16 v252, v78, v172\n"
    "v_dot2c_f32_bf16 v253, v78, v78\n"
    "v_dot2c_f32_bf16 v252, v79, v172\n"
    "v_dot2c_f32_bf16 v253, v79, v79\n"
    "v_mfma_f32_32x32x16_bf16 v[128:143], v[208:211], v[72:75], v[128:143]\n"
    "s_waitcnt lgkmcnt(0)\n"
    "s_barrier\n"
    "v_mfma_f32_32x32x16_bf16 v[144:159], v[212:215], v[72:75], v[144:159]\n"
    "ds_read_b128 v[80:83], v168 offset:0\n"
    "ds_read_b128 v[84:87], v169 offset:0\n"
    "ds_read_b128 v[88:91], v170 offset:0\n"
    "ds_read_b128 v[92:95], v171 offset:0\n"
    "ds_read_b64_tr_b16 v[192:193], v234 offset:57344\n"
    "ds_read_b64_tr_b16 v[194:195], v234 offset:57856\n"
    "ds_read_b64_tr_b16 v[196:197], v235 offset:57344\n"
    "ds_read_b64_tr_b16 v[198:199], v235 offset:57856\n"
    "v_mfma_f32_32x32x16_bf16 v[128:143], v[216:219], v[76:79], v[128:143]\n"
    "ds_read_b64_tr_b16 v[200:201], v234 offset:59392\n"
    "ds_read_b64_tr_b16 v[202:203], v234 offset:59904\n"
    "ds_read_b64_tr_b16 v[204:205], v235 offset:59392\n"
    "ds_read_b64_tr_b16 v[206:207], v235 offset:59904\n"
    "v_mfma_f32_32x32x16_bf16 v[144:159], v[220:223], v[76:79], v[144:159]\n"
    "s_waitcnt lgkmcnt(6)\n"
    "ds_read_b64_tr_b16 v[208:209], v234 offset:61440\n"
    "ds_read_b64_tr_b16 v[210:211], v234 offset:61952\n"
    "ds_read_b64_tr_b16 v[212:213], v235 offset:61440\n"
    "ds_read_b64_tr_b16 v[214:215], v235 offset:61952\n"
    "v_mfma_f32_32x32x16_bf16 v[128:143], v[192:195], v[80:83], v[128:143]\n"
    "s_waitcnt lgkmcnt(8)\n"
    "ds_read_b64_tr_b16 v[216:217], v234 offset:63488\n"
    "ds_read_b64_tr_b16 v[218:219], v234 offset:64000\n"
    "v_mfma_f32_32x32x16_bf16 v[144:159], v[196:199], v[80:83], v[144:159]\n"
    "s_waitcnt lgkmcnt(8)\n"
    "ds_read_b64_tr_b16 v[220:221], v235 offset:63488\n"
    "ds_read_b64_tr_b16 v[222:223], v235 offset:64000\n"
    "v_mfma_f32_32x32x16_bf16 v[128:143], v[200:203], v[84:87], v[128:143]\n"
    "s_waitcnt lgkmcnt(8)\n"
    "v_mfma_f32_32x32x16_bf16 v[144:159], v[204:207], v[84:87], v[144:159]\n"
    "s_waitcnt lgkmcnt(6)\n"
    "v_dot2c_f32_bf16 v252, v80, v172\n"
    "v_dot2c_f32_bf16 v253, v80, v80\n"
    "v_dot2c_f32_bf16 v252, v81, v172\n"
    "v_dot2c_f32_bf16 v253, v81, v81\n"
    "v_dot2c_f32_bf16 v252, v82, v172\n"
    "v_dot2c_f32_bf16 v253, v82, v82\n"
    "v_dot2c_f32_bf16 v252, v83, v172\n"
    "v_dot2c_f32_bf16 v253, v83, v83\n"
    "v_dot2c_f32_bf16 v252, v84, v172\n"
    "v_dot2c_f32_bf16 v253, v84, v84\n"
    "v_dot2c_f32_bf16 v252, v85, v172\n"
    "v_dot2c_f32_bf16 v253, v85, v85\n"
    "v_dot2c_f32_bf16 v252, v86, v172\n"
    "v_dot2c_f32_bf16 v253, v86, v86\n"
    "v_dot2c_f32_bf16 v252, v87, v172\n"
    "v_dot2c_f32_bf16 v253, v87, v87\n"
    "v_dot2c_f32_bf16 v252, v88, v172\n"
    "v_dot2c_f32_bf16 v253, v88, v88\n"
    "v_dot2c_f32_bf16 v252, v89, v172\n"
    "v_dot2c_f32_bf16 v253, v89, v89\n"
    "v_dot2c_f32_bf16 v252, v90, v172\n"
    "v_dot2c_f32_bf16 v253, v90, v90\n"
    "v_dot2c_f32_bf16 v252, v91, v172\n"
    "v_dot2c_f32_bf16 v253, v91, v91\n"
    "v_dot2c_f32_bf16 v252, v92, v172\n"
    "v_dot2c_f32_bf16 v253, v92, v92\n"
    "v_dot2c_f32_bf16 v252, v93, v172\n"
    "v_dot2c_f32_bf16 v253, v93, v93\n"
    "v_dot2c_f32_bf16 v252, v94, v172\n"
    "v_dot2c_f32_bf16 v253, v94, v94\n"
    "v_dot2c_f32_bf16 v252, v95, v172\n"
    "v_dot2c_f32_bf16 v253, v95, v95\n"
    "v_mfma_f32_32x32x16_bf16 v[128:143], v[208:211], v[88:91], v[128:143]\n"
    "s_waitcnt lgkmcnt(0)\n"
    "s_barrier\n"
    "v_mfma_f32_32x32x16_bf16 v[144:159], v[212:215], v[88:91], v[144:159]\n"
    "ds_read_b128 v[96:99], v160 offset:0\n"
    "ds_read_b128 v[100:103], v161 offset:0\n"
    "ds_read_b128 v[104:107], v162 offset:0\n"
    "ds_read_b128 v[108:111], v163 offset:0\n"
    "ds_read_b64_tr_b16 v[192:193], v232 offset:16384\n"
    "ds_read_b64_tr_b16 v[194:195], v232 offset:16896\n"
    "ds_read_b64_tr_b16 v[196:197], v233 offset:16384\n"
    "ds_read_b64_tr_b16 v[198:199], v233 offset:16896\n"
    "v_mfma_f32_32x32x16_bf16 v[128:143], v[216:219], v[92:95], v[128:143]\n"
    "ds_read_b64_tr_b16 v[200:201], v232 offset:18432\n"
    "ds_read_b64_tr_b16 v[202:203], v232 offset:18944\n"
    "ds_read_b64_tr_b16 v[204:205], v233 offset:18432\n"
    "ds_read_b64_tr_b16 v[206:207], v233 offset:18944\n"
    "v_mfma_f32_32x32x16_bf16 v[144:159], v[220:223], v[92:95], v[144:159]\n"
    "s_waitcnt lgkmcnt(6)\n"
    "ds_read_b64_tr_b16 v[208:209], v232 offset:20480\n"
    "ds_read_b64_tr_b16 v[210:211], v232 offset:20992\n"
    "ds_read_b64_tr_b16 v[212:213], v233 offset:20480\n"
    "ds_read_b64_tr_b16 v[214:215], v233 offset:20992\n"
    "v_mfma_f32_32x32x16_bf16 v[128:143], v[192:195], v[96:99], v[128:143]\n"
    "s_waitcnt lgkmcnt(8)\n"
    "ds_read_b64_tr_b16 v[216:217], v232 offset:22528\n"
    "ds_read_b64_tr_b16 v[218:219], v232 offset:23040\n"
    "v_mfma_f32_32x32x16_bf16 v[144:159], v[196:199], v[96:99], v[144:159]\n"
    "s_waitcnt lgkmcnt(8)\n"
    "ds_read_b64_tr_b16 v[220:221], v233 offset:22528\n"
    "ds_read_b64_tr_b16 v[222:223], v233 offset:23040\n"
    "v_mfma_f32_32x32x16_bf16 v[128:143], v[200:203], v[100:103], v[128:143]\n"
    "s_waitcnt lgkmcnt(8)\n"
    "v_mfma_f32_32x32x16_bf16 v[144:159], v[204:207], v[100:103], v[144:159]\n"
    "s_waitcnt lgkmcnt(6)\n"
    "v_dot2c_f32_bf16 v252, v96, v172\n"
    "v_dot2c_f32_bf16 v253, v96, v96\n"
    "v_dot2c_f32_bf16 v252, v97, v172\n"
    "v_dot2c_f32_bf16 v253, v97, v97\n"
    "v_dot2c_f32_bf16 v252, v98, v172\n"
    "v_dot2c_f32_bf16 v253, v98, v98\n"
    "v_dot2c_f32_bf16 v252, v99, v172\n"
    "v_dot2c_f32_bf16 v253, v99, v99\n"
    "v_dot2c_f32_bf16 v252, v100, v172\n"
    "v_dot2c_f32_bf16 v253, v100, v100\n"
    "v_dot2c_f32_bf16 v252, v101, v172\n"
    "v_dot2c_f32_bf16 v253, v101, v101\n"
    "v_dot2c_f32_bf16 v252, v102, v172\n"
    "v_dot2c_f32_bf16 v253, v102, v102\n"
    "v_dot2c_f32_bf16 v252, v103, v172\n"
    "v_dot2c_f32_bf16 v253, v103, v103\n"
    "v_dot2c_f32_bf16 v252, v104, v172\n"
    "v_dot2c_f32_bf16 v253, v104, v104\n"
    "v_dot2c_f32_bf16 v252, v105, v172\n"
    "v_dot2c_f32_bf16 v253, v105, v105\n"
    "v_dot2c_f32_bf16 v252, v106, v172\n"
    "v_dot2c_f32_bf16 v253, v106, v106\n"
    "v_dot2c_f32_bf16 v252, v107, v172\n"
    "v_dot2c_f32_bf16 v253, v107, v107\n"
    "v_dot2c_f32_bf16 v252, v108, v172\n"
    "v_dot2c_f32_bf16 v253, v108, v108\n"
    "v_dot2c_f32_bf16 v252, v109, v172\n"
    "v_dot2c_f32_bf16 v253, v109, v109\n"
    "v_dot2c_f32_bf16 v252, v110, v172\n"
    "v_dot2c_f32_bf16 v253, v110, v110\n"
    "v_dot2c_f32_bf16 v252, v111, v172\n"
    "v_dot2c_f32_bf16 v253, v111, v111\n"
    "v_mfma_f32_32x32x16_bf16 v[128:143], v[208:211], v[104:107], v[128:143]\n"
    "s_waitcnt lgkmcnt(0)\n"
    "s_barrier\n"
    "v_mfma_f32_32x32x16_bf16 v[144:159], v[212:215], v[104:107], v[144:159]\n"
    "ds_read_b128 v[112:115], v160 offset:32768\n"
    "ds_read_b128 v[116:119], v161 offset:32768\n"
    "ds_read_b128 v[120:123], v162 offset:32768\n"
    "ds_read_b128 v[124:127], v163 offset:32768\n"
    "ds_read_b64_tr_b16 v[192:193], v232 offset:24576\n"
    "ds_read_b64_tr_b16 v[194:195], v232 offset:25088\n"
    "ds_read_b64_tr_b16 v[196:197], v233 offset:24576\n"
    "ds_read_b64_tr_b16 v[198:199], v233 offset:25088\n"
    "v_mfma_f32_32x32x16_bf16 v[128:143], v[216:219], v[108:111], v[128:143]\n"
    "ds_read_b64_tr_b16 v[200:201], v232 offset:26624\n"
    "ds_read_b64_tr_b16 v[202:203], v232 offset:27136\n"
    "ds_read_b64_tr_b16 v[204:205], v233 offset:26624\n"
    "ds_read_b64_tr_b16 v[206:207], v233 offset:27136\n"
    "v_mfma_f32_32x32x16_bf16 v[144:159], v[220:223], v[108:111], v[144:159]\n"
    "s_waitcnt lgkmcnt(6)\n"
    "ds_read_b64_tr_b16 v[208:209], v232 offset:28672\n"
    "ds_read_b64_tr_b16 v[210:211], v232 offset:29184\n"
    "ds_read_b64_tr_b16 v[212:213], v233 offset:28672\n"
    "ds_read_b64_tr_b16 v[214:215], v233 offset:29184\n"
    "v_mfma_f32_32x32x16_bf16 v[128:143], v[192:195], v[112:115], v[128:143]\n"
    "s_waitcnt lgkmcnt(8)\n"
    "ds_read_b64_tr_b16 v[216:217], v232 offset:30720\n"
    "ds_read_b64_tr_b16 v[218:219], v232 offset:31232\n"
    "v_mfma_f32_32x32x16_bf16 v[144:159], v[196:199], v[112:115], v[144:159]\n"
    "s_waitcnt lgkmcnt(8)\n"
    "ds_read_b64_tr_b16 v[220:221], v233 offset:30720\n"
    "ds_read_b64_tr_b16 v[222:223], v233 offset:31232\n"
    "v_mfma_f32_32x32x16_bf16 v[128:143], v[200:203], v[116:119], v[128:143]\n"
    "s_waitcnt lgkmcnt(8)\n"
    "v_mfma_f32_32x32x16_bf16 v[144:159], v[204:207], v[116:119], v[144:159]\n"
    "s_waitcnt lgkmcnt(6)\n"
    "v_dot2c_f32_bf16 v252, v112, v172\n"
    "v_dot2c_f32_bf16 v253, v112, v112\n"
    "v_dot2c_f32_bf16 v252, v113, v172\n"
    "v_dot2c_f32_bf16 v253, v113, v113\n"
    "v_dot2c_f32_bf16 v252, v114, v172\n"
    "v_dot2c_f32_bf16 v253, v114, v114\n"
    "v_dot2c_f32_bf16 v252, v115, v172\n"
    "v_dot2c_f32_bf16 v253, v115, v115\n"
    "v_dot2c_f32_bf16 v252, v116, v172\n"
    "v_dot2c_f32_bf16 v253, v116, v116\n"
    "v_dot2c_f32_bf16 v252, v117, v172\n"
    "v_dot2c_f32_bf16 v253, v117, v117\n"
    "v_dot2c_f32_bf16 v252, v118, v172\n"
    "v_dot2c_f32_bf16 v253, v118, v118\n"
    "v_dot2c_f32_bf16 v252, v119, v172\n"
    "v_dot2c_f32_bf16 v253, v119, v119\n"
    "v_dot2c_f32_bf16 v252, v120, v172\n"
    "v_dot2c_f32_bf16 v253, v120, v120\n"
    "v_dot2c_f32_bf16 v252, v121, v172\n"
    "v_dot2c_f32_bf16 v253, v121, v121\n"
    "v_dot2c_f32_bf16 v252, v122, v172\n"
    "v_dot2c_f32_bf16 v253, v122, v122\n"
    "v_dot2c_f32_bf16 v252, v123, v172\n"
    "v_dot2c_f32_bf16 v253, v123, v123\n"
    "v_dot2c_f32_bf16 v252, v124, v172\n"
    "v_dot2c_f32_bf16 v253, v124, v124\n"
    "v_dot2c_f32_bf16 v252, v125, v172\n"
    "v_dot2c_f32_bf16 v253, v125, v125\n"
    "v_dot2c_f32_bf16 v252, v126, v172\n"
    "v_dot2c_f32_bf16 v253, v126, v126\n"
    "v_dot2c_f32_bf16 v252, v127, v172\n"
    "v_dot2c_f32_bf16 v253, v127, v127\n"
    "v_mfma_f32_32x32x16_bf16 v[128:143], v[208:211], v[120:123], v[128:143]\n"
    "s_waitcnt lgkmcnt(0)\n"
    "s_barrier\n"
    "v_mfma_f32_32x32x16_bf16 v[144:159], v[212:215], v[120:123], v[144:159]\n"
    "ds_read_b64 v[240:241], v248 offset:0\n"
    "ds_read_b64 v[242:243], v248 offset:256\n"
    "ds_read_b64_tr_b16 v[192:193], v232 offset:49152\n"
    "ds_read_b64_tr_b16 v[194:195], v232 offset:49664\n"
    "ds_read_b64_tr_b16 v[196:197], v233 offset:49152\n"
    "ds_read_b64_tr_b16 v[198:199], v233 offset:49664\n"
    "v_permlane32_swap_b32 v252, v253\n"
    "v_mfma_f32_32x32x16_bf16 v[128:143], v[216:219], v[124:127], v[128:143]\n"
    "ds_read_b64_tr_b16 v[200:201], v232 offset:51200\n"
    "ds_read_b64_tr_b16 v[202:203], v232 offset:51712\n"
    "ds_read_b64_tr_b16 v[204:205], v233 offset:51200\n"
    "ds_read_b64_tr_b16 v[206:207], v233 offset:51712\n"
    "v_add_f32 v252, v252, v253\n"
    "v_mfma_f32_32x32x16_bf16 v[144:159], v[220:223], v[124:127], v[144:159]\n"
    "s_waitcnt lgkmcnt(6)\n"
    "ds_read_b64_tr_b16 v[208:209], v232 offset:53248\n"
    "ds_read_b64_tr_b16 v[210:211], v232 offset:53760\n"
    "ds_read_b64_tr_b16 v[212:213], v233 offset:53248\n"
    "ds_read_b64_tr_b16 v[214:215], v233 offset:53760\n"
    "v_mov_b32 v253, v252\n"
    "v_mfma_f32_32x32x16_bf16 v[160:175], v[192:195], v[0:3], 0\n"
    "s_waitcnt lgkmcnt(8)\n"
    "ds_read_b64_tr_b16 v[216:217], v232 offset:55296\n"
    "ds_read_b64_tr_b16 v[218:219], v232 offset:55808\n"
    "v_permlane32_swap_b32 v252, v253\n"
    "v_mul_f32 v252, 0x3b000000, v252\n"
    "v_mul_f32 v224, v252, v252\n"
    "v_mfma_f32_32x32x16_bf16 v[176:191], v[196:199], v[0:3], 0\n"
    "s_waitcnt lgkmcnt(8)\n"
    "ds_read_b64_tr_b16 v[220:221], v233 offset:55296\n"
    "ds_read_b64_tr_b16 v[222:223], v233 offset:55808\n"
    "v_mul_f32 v253, 0x3b000000, v253\n"
    "v_sub_f32 v253, v253, v224\n"
    "v_max_f32 v253, 0, v253\n"
    "v_mfma_f32_32x32x16_bf16 v[160:175], v[200:203], v[4:7], v[160:175]\n"
    "s_waitcnt lgkmcnt(8)\n"
    "ds_read_b64_tr_b16 v[192:193], v232 offset:57344\n"
    "ds_read_b64_tr_b16 v[194:195], v232 offset:57856\n"
    "v_cvt_pk_bf16_f32 v224, v253, v253\n"
    "v_lshlrev_b32 v224, 16, v224\n"
    "v_add_f32 v224, 0x3727c5ac, v224\n"
    "v_mfma_f32_32x32x16_bf16 v[176:191], v[204:207], v[4:7], v[176:191]\n"
    "s_waitcnt lgkmcnt(8)\n"
    "ds_read_b64_tr_b16 v[196:197], v233 offset:57344\n"
    "ds_read_b64_tr_b16 v[198:199], v233 offset:57856\n"
    "v_cvt_pk_bf16_f32 v224, v224, v224\n"
    "v_lshlrev_b32 v224, 16, v224\n"
    "v_sqrt_f32 v224, v224\n"
    "v_mfma_f32_32x32x16_bf16 v[160:175], v[208:211], v[8:11], v[160:175]\n"
    "s_waitcnt lgkmcnt(8)\n"
    "ds_read_b64_tr_b16 v[200:201], v232 offset:59392\n"
    "ds_read_b64_tr_b16 v[202:203], v232 offset:59904\n"
    "v_cvt_pk_bf16_f32 v244, -v252, -v252\n"
    "v_cvt_pk_bf16_f32 v245, v224, -v252\n"
    "v_lshlrev_b32 v224, 16, v245\n"
    "v_mfma_f32_32x32x16_bf16 v[176:191], v[212:215], v[8:11], v[176:191]\n"
    "s_waitcnt lgkmcnt(8)\n"
    "ds_read_b64_tr_b16 v[204:205], v233 offset:59392\n"
    "ds_read_b64_tr_b16 v[206:207], v233 offset:59904\n"
    "v_rcp_f32 v239, v224\n"
    "v_and_b32 v244, v244, v250\n"
    "v_and_b32 v245, v245, v250\n"
    "v_mfma_f32_32x32x16_bf16 v[160:175], v[216:219], v[12:15], v[160:175]\n"
    "s_waitcnt lgkmcnt(8)\n"
    "ds_read_b64_tr_b16 v[208:209], v232 offset:61440\n"
    "ds_read_b64_tr_b16 v[210:211], v232 offset:61952\n"
    "v_mfma_f32_32x32x16_bf16 v[128:143], v[240:243], v[244:247], v[128:143]\n"
    "v_mfma_f32_32x32x16_bf16 v[144:159], v[242:245], v[244:247], v[144:159]\n"
    "v_mfma_f32_32x32x16_bf16 v[176:191], v[220:223], v[12:15], v[176:191]\n"
    "s_waitcnt lgkmcnt(8)\n"
    "ds_read_b64_tr_b16 v[212:213], v233 offset:61440\n"
    "ds_read_b64_tr_b16 v[214:215], v233 offset:61952\n"
    "v_mfma_f32_32x32x16_bf16 v[160:175], v[192:195], v[16:19], v[160:175]\n"
    "s_waitcnt lgkmcnt(8)\n"
    "ds_read_b64_tr_b16 v[216:217], v232 offset:63488\n"
    "ds_read_b64_tr_b16 v[218:219], v232 offset:64000\n"
    "v_mfma_f32_32x32x16_bf16 v[176:191], v[196:199], v[16:19], v[176:191]\n"
    "s_waitcnt lgkmcnt(8)\n"
    "ds_read_b64_tr_b16 v[220:221], v233 offset:63488\n"
    "ds_read_b64_tr_b16 v[222:223], v233 offset:64000\n"
    "v_mul_f32 v128, v239, v128\n"
    "v_mul_f32 v129, v239, v129\n"
    "v_mul_f32 v130, v239, v130\n"
    "v_mfma_f32_32x32x16_bf16 v[160:175], v[200:203], v[20:23], v[160:175]\n"
    "s_waitcnt lgkmcnt(8)\n"
    "v_mul_f32 v131, v239, v131\n"
    "v_mul_f32 v132, v239, v132\n"
    "v_mul_f32 v133, v239, v133\n"
    "v_mfma_f32_32x32x16_bf16 v[176:191], v[204:207], v[20:23], v[176:191]\n"
    "s_waitcnt lgkmcnt(6)\n"
    "v_mul_f32 v134, v239, v134\n"
    "v_mul_f32 v135, v239, v135\n"
    "v_mul_f32 v128, v128, abs(v128)\n"
    "v_mfma_f32_32x32x16_bf16 v[160:175], v[208:211], v[24:27], v[160:175]\n"
    "s_waitcnt lgkmcnt(0)\n"
    "s_barrier\n"
    "v_mfma_f32_32x32x16_bf16 v[176:191], v[212:215], v[24:27], v[176:191]\n"
    "ds_read_b64_tr_b16 v[192:193], v234 offset:0\n"
    "ds_read_b64_tr_b16 v[194:195], v234 offset:512\n"
    "ds_read_b64_tr_b16 v[196:197], v235 offset:0\n"
    "ds_read_b64_tr_b16 v[198:199], v235 offset:512\n"
    "v_mul_f32 v129, v129, abs(v129)\n"
    "v_mfma_f32_32x32x16_bf16 v[160:175], v[216:219], v[28:31], v[160:175]\n"
    "ds_read_b64_tr_b16 v[200:201], v234 offset:2048\n"
    "ds_read_b64_tr_b16 v[202:203], v234 offset:2560\n"
    "ds_read_b64_tr_b16 v[204:205], v235 offset:2048\n"
    "ds_read_b64_tr_b16 v[206:207], v235 offset:2560\n"
    "v_mul_f32 v130, v130, abs(v130)\n"
    "v_mfma_f32_32x32x16_bf16 v[176:191], v[220:223], v[28:31], v[176:191]\n"
    "s_waitcnt lgkmcnt(6)\n"
    "ds_read_b64_tr_b16 v[208:209], v234 offset:4096\n"
    "ds_read_b64_tr_b16 v[210:211], v234 offset:4608\n"
    "ds_read_b64_tr_b16 v[212:213], v235 offset:4096\n"
    "ds_read_b64_tr_b16 v[214:215], v235 offset:4608\n"
    "v_mul_f32 v131, v131, abs(v131)\n"
    "v_mfma_f32_32x32x16_bf16 v[160:175], v[192:195], v[32:35], v[160:175]\n"
    "s_waitcnt lgkmcnt(8)\n"
    "ds_read_b64_tr_b16 v[216:217], v234 offset:6144\n"
    "ds_read_b64_tr_b16 v[218:219], v234 offset:6656\n"
    "v_mul_f32 v132, v132, abs(v132)\n"
    "v_mul_f32 v133, v133, abs(v133)\n"
    "v_mul_f32 v134, v134, abs(v134)\n"
    "v_mfma_f32_32x32x16_bf16 v[176:191], v[196:199], v[32:35], v[176:191]\n"
    "s_waitcnt lgkmcnt(8)\n"
    "ds_read_b64_tr_b16 v[220:221], v235 offset:6144\n"
    "ds_read_b64_tr_b16 v[222:223], v235 offset:6656\n"
    "v_mul_f32 v135, v135, abs(v135)\n"
    "v_cvt_pk_bf16_f32 v128, v128, v129\n"
    "v_cvt_pk_bf16_f32 v129, v130, v131\n"
    "v_mfma_f32_32x32x16_bf16 v[160:175], v[200:203], v[36:39], v[160:175]\n"
    "s_waitcnt lgkmcnt(8)\n"
    "ds_read_b64_tr_b16 v[192:193], v234 offset:8192\n"
    "ds_read_b64_tr_b16 v[194:195], v234 offset:8704\n"
    "v_cvt_pk_bf16_f32 v130, v132, v133\n"
    "v_cvt_pk_bf16_f32 v131, v134, v135\n"
    "v_pk_max_i16 v128, v128, 0\n"
    "v_mfma_f32_32x32x16_bf16 v[176:191], v[204:207], v[36:39], v[176:191]\n"
    "s_waitcnt lgkmcnt(8)\n"
    "ds_read_b64_tr_b16 v[196:197], v235 offset:8192\n"
    "ds_read_b64_tr_b16 v[198:199], v235 offset:8704\n"
    "v_pk_max_i16 v129, v129, 0\n"
    "v_pk_max_i16 v130, v130, 0\n"
    "v_pk_max_i16 v131, v131, 0\n"
    "v_mfma_f32_32x32x16_bf16 v[160:175], v[208:211], v[40:43], v[160:175]\n"
    "s_waitcnt lgkmcnt(8)\n"
    "ds_read_b64_tr_b16 v[200:201], v234 offset:10240\n"
    "ds_read_b64_tr_b16 v[202:203], v234 offset:10752\n"
    "v_mul_f32 v136, v239, v136\n"
    "v_mul_f32 v137, v239, v137\n"
    "v_mul_f32 v138, v239, v138\n"
    "v_mfma_f32_32x32x16_bf16 v[176:191], v[212:215], v[40:43], v[176:191]\n"
    "s_waitcnt lgkmcnt(8)\n"
    "ds_read_b64_tr_b16 v[204:205], v235 offset:10240\n"
    "ds_read_b64_tr_b16 v[206:207], v235 offset:10752\n"
    "v_mul_f32 v139, v239, v139\n"
    "v_mul_f32 v140, v239, v140\n"
    "v_mul_f32 v141, v239, v141\n"
    "v_mfma_f32_32x32x16_bf16 v[160:175], v[216:219], v[44:47], v[160:175]\n"
    "s_waitcnt lgkmcnt(8)\n"
    "ds_read_b64_tr_b16 v[208:209], v234 offset:12288\n"
    "ds_read_b64_tr_b16 v[210:211], v234 offset:12800\n"
    "v_mul_f32 v142, v239, v142\n"
    "v_mul_f32 v143, v239, v143\n"
    "v_mul_f32 v136, v136, abs(v136)\n"
    "v_mfma_f32_32x32x16_bf16 v[176:191], v[220:223], v[44:47], v[176:191]\n"
    "s_waitcnt lgkmcnt(8)\n"
    "ds_read_b64_tr_b16 v[212:213], v235 offset:12288\n"
    "ds_read_b64_tr_b16 v[214:215], v235 offset:12800\n"
    "v_mul_f32 v137, v137, abs(v137)\n"
    "v_mul_f32 v138, v138, abs(v138)\n"
    "v_mul_f32 v139, v139, abs(v139)\n"
    "v_mfma_f32_32x32x16_bf16 v[160:175], v[192:195], v[48:51], v[160:175]\n"
    "s_waitcnt lgkmcnt(8)\n"
    "ds_read_b64_tr_b16 v[216:217], v234 offset:14336\n"
    "ds_read_b64_tr_b16 v[218:219], v234 offset:14848\n"
    "v_mul_f32 v140, v140, abs(v140)\n"
    "v_mul_f32 v141, v141, abs(v141)\n"
    "v_mul_f32 v142, v142, abs(v142)\n"
    "v_mfma_f32_32x32x16_bf16 v[176:191], v[196:199], v[48:51], v[176:191]\n"
    "s_waitcnt lgkmcnt(8)\n"
    "ds_read_b64_tr_b16 v[220:221], v235 offset:14336\n"
    "ds_read_b64_tr_b16 v[222:223], v235 offset:14848\n"
    "v_mul_f32 v143, v143, abs(v143)\n"
    "v_cvt_pk_bf16_f32 v136, v136, v137\n"
    "v_cvt_pk_bf16_f32 v137, v138, v139\n"
    "v_mfma_f32_32x32x16_bf16 v[160:175], v[200:203], v[52:55], v[160:175]\n"
    "s_waitcnt lgkmcnt(8)\n"
    "v_cvt_pk_bf16_f32 v138, v140, v141\n"
    "v_cvt_pk_bf16_f32 v139, v142, v143\n"
    "v_pk_max_i16 v136, v136, 0\n"
    "v_mfma_f32_32x32x16_bf16 v[176:191], v[204:207], v[52:55], v[176:191]\n"
    "s_waitcnt lgkmcnt(6)\n"
    "v_pk_max_i16 v137, v137, 0\n"
    "v_pk_max_i16 v138, v138, 0\n"
    "v_pk_max_i16 v139, v139, 0\n"
    "v_mfma_f32_32x32x16_bf16 v[160:175], v[208:211], v[56:59], v[160:175]\n"
    "s_waitcnt lgkmcnt(0)\n"
    "s_barrier\n"
    "v_mfma_f32_32x32x16_bf16 v[176:191], v[212:215], v[56:59], v[176:191]\n"
    "ds_read_b64_tr_b16 v[192:193], v234 offset:16384\n"
    "ds_read_b64_tr_b16 v[194:195], v234 offset:16896\n"
    "ds_read_b64_tr_b16 v[196:197], v235 offset:16384\n"
    "ds_read_b64_tr_b16 v[198:199], v235 offset:16896\n"
    "v_mul_f32 v144, v239, v144\n"
    "v_mul_f32 v145, v239, v145\n"
    "v_mfma_f32_32x32x16_bf16 v[160:175], v[216:219], v[60:63], v[160:175]\n"
    "ds_read_b64_tr_b16 v[200:201], v234 offset:18432\n"
    "ds_read_b64_tr_b16 v[202:203], v234 offset:18944\n"
    "ds_read_b64_tr_b16 v[204:205], v235 offset:18432\n"
    "ds_read_b64_tr_b16 v[206:207], v235 offset:18944\n"
    "v_mul_f32 v146, v239, v146\n"
    "v_mul_f32 v147, v239, v147\n"
    "v_mfma_f32_32x32x16_bf16 v[176:191], v[220:223], v[60:63], v[176:191]\n"
    "s_waitcnt lgkmcnt(6)\n"
    "ds_read_b64_tr_b16 v[208:209], v234 offset:20480\n"
    "ds_read_b64_tr_b16 v[210:211], v234 offset:20992\n"
    "ds_read_b64_tr_b16 v[212:213], v235 offset:20480\n"
    "ds_read_b64_tr_b16 v[214:215], v235 offset:20992\n"
    "v_mul_f32 v148, v239, v148\n"
    "v_mul_f32 v149, v239, v149\n"
    "v_mfma_f32_32x32x16_bf16 v[160:175], v[192:195], v[64:67], v[160:175]\n"
    "s_waitcnt lgkmcnt(8)\n"
    "ds_read_b64_tr_b16 v[216:217], v234 offset:22528\n"
    "ds_read_b64_tr_b16 v[218:219], v234 offset:23040\n"
    "v_mul_f32 v150, v239, v150\n"
    "v_mul_f32 v151, v239, v151\n"
    "v_mul_f32 v144, v144, abs(v144)\n"
    "v_mul_f32 v145, v145, abs(v145)\n"
    "v_mfma_f32_32x32x16_bf16 v[176:191], v[196:199], v[64:67], v[176:191]\n"
    "s_waitcnt lgkmcnt(8)\n"
    "ds_read_b64_tr_b16 v[220:221], v235 offset:22528\n"
    "ds_read_b64_tr_b16 v[222:223], v235 offset:23040\n"
    "v_mul_f32 v146, v146, abs(v146)\n"
    "v_mul_f32 v147, v147, abs(v147)\n"
    "v_mul_f32 v148, v148, abs(v148)\n"
    "v_mul_f32 v149, v149, abs(v149)\n"
    "v_mfma_f32_32x32x16_bf16 v[160:175], v[200:203], v[68:71], v[160:175]\n"
    "s_waitcnt lgkmcnt(8)\n"
    "ds_read_b64_tr_b16 v[192:193], v234 offset:24576\n"
    "ds_read_b64_tr_b16 v[194:195], v234 offset:25088\n"
    "v_mul_f32 v150, v150, abs(v150)\n"
    "v_mul_f32 v151, v151, abs(v151)\n"
    "v_cvt_pk_bf16_f32 v144, v144, v145\n"
    "v_cvt_pk_bf16_f32 v145, v146, v147\n"
    "v_mfma_f32_32x32x16_bf16 v[176:191], v[204:207], v[68:71], v[176:191]\n"
    "s_waitcnt lgkmcnt(8)\n"
    "ds_read_b64_tr_b16 v[196:197], v235 offset:24576\n"
    "ds_read_b64_tr_b16 v[198:199], v235 offset:25088\n"
    "v_cvt_pk_bf16_f32 v146, v148, v149\n"
    "v_cvt_pk_bf16_f32 v147, v150, v151\n"
    "v_pk_max_i16 v144, v144, 0\n"
    "v_pk_max_i16 v145, v145, 0\n"
    "v_mfma_f32_32x32x16_bf16 v[160:175], v[208:211], v[72:75], v[160:175]\n"
    "s_waitcnt lgkmcnt(8)\n"
    "ds_read_b64_tr_b16 v[200:201], v234 offset:26624\n"
    "ds_read_b64_tr_b16 v[202:203], v234 offset:27136\n"
    "v_pk_max_i16 v146, v146, 0\n"
    "v_pk_max_i16 v147, v147, 0\n"
    "v_mul_f32 v152, v239, v152\n"
    "v_mul_f32 v153, v239, v153\n"
    "v_mfma_f32_32x32x16_bf16 v[176:191], v[212:215], v[72:75], v[176:191]\n"
    "s_waitcnt lgkmcnt(8)\n"
    "ds_read_b64_tr_b16 v[204:205], v235 offset:26624\n"
    "ds_read_b64_tr_b16 v[206:207], v235 offset:27136\n"
    "v_mul_f32 v154, v239, v154\n"
    "v_mul_f32 v155, v239, v155\n"
    "v_mul_f32 v156, v239, v156\n"
    "v_mul_f32 v157, v239, v157\n"
    "v_mfma_f32_32x32x16_bf16 v[160:175], v[216:219], v[76:79], v[160:175]\n"
    "s_waitcnt lgkmcnt(8)\n"
    "ds_read_b64_tr_b16 v[208:209], v234 offset:28672\n"
    "ds_read_b64_tr_b16 v[210:211], v234 offset:29184\n"
    "v_mul_f32 v158, v239, v158\n"
    "v_mul_f32 v159, v239, v159\n"
    "v_mul_f32 v152, v152, abs(v152)\n"
    "v_mfma_f32_32x32x16_bf16 v[176:191], v[220:223], v[76:79], v[176:191]\n"
    "s_waitcnt lgkmcnt(8)\n"
    "ds_read_b64_tr_b16 v[212:213], v235 offset:28672\n"
    "ds_read_b64_tr_b16 v[214:215], v235 offset:29184\n"
    "v_mul_f32 v153, v153, abs(v153)\n"
    "v_mul_f32 v154, v154, abs(v154)\n"
    "v_mul_f32 v155, v155, abs(v155)\n"
    "v_mfma_f32_32x32x16_bf16 v[160:175], v[192:195], v[80:83], v[160:175]\n"
    "s_waitcnt lgkmcnt(8)\n"
    "ds_read_b64_tr_b16 v[216:217], v234 offset:30720\n"
    "ds_read_b64_tr_b16 v[218:219], v234 offset:31232\n"
    "v_mul_f32 v156, v156, abs(v156)\n"
    "v_mul_f32 v157, v157, abs(v157)\n"
    "v_mul_f32 v158, v158, abs(v158)\n"
    "v_mfma_f32_32x32x16_bf16 v[176:191], v[196:199], v[80:83], v[176:191]\n"
    "s_waitcnt lgkmcnt(8)\n"
    "ds_read_b64_tr_b16 v[220:221], v235 offset:30720\n"
    "ds_read_b64_tr_b16 v[222:223], v235 offset:31232\n"
    "v_mul_f32 v159, v159, abs(v159)\n"
    "v_cvt_pk_bf16_f32 v152, v152, v153\n"
    "v_cvt_pk_bf16_f32 v153, v154, v155\n"
    "v_mfma_f32_32x32x16_bf16 v[160:175], v[200:203], v[84:87], v[160:175]\n"
    "s_waitcnt lgkmcnt(8)\n"
    "v_cvt_pk_bf16_f32 v154, v156, v157\n"
    "v_cvt_pk_bf16_f32 v155, v158, v159\n"
    "v_pk_max_i16 v152, v152, 0\n"
    "v_mfma_f32_32x32x16_bf16 v[176:191], v[204:207], v[84:87], v[176:191]\n"
    "s_waitcnt lgkmcnt(6)\n"
    "v_pk_max_i16 v153, v153, 0\n"
    "v_pk_max_i16 v154, v154, 0\n"
    "v_pk_max_i16 v155, v155, 0\n"
    "v_mfma_f32_32x32x16_bf16 v[160:175], v[208:211], v[88:91], v[160:175]\n"
    "s_waitcnt lgkmcnt(0)\n"
    "s_barrier\n"
    "v_mfma_f32_32x32x16_bf16 v[176:191], v[212:215], v[88:91], v[176:191]\n"
    "ds_read_b64_tr_b16 v[192:193], v234 offset:32768\n"
    "ds_read_b64_tr_b16 v[194:195], v234 offset:33280\n"
    "ds_read_b64_tr_b16 v[196:197], v235 offset:32768\n"
    "ds_read_b64_tr_b16 v[198:199], v235 offset:33280\n"
    "s_add_u32 s84, s85, 0\n"
    "buffer_store_dwordx4 v[128:131], v238, s[44:47], s84 offen offset:0 sc1\n"
    "v_mfma_f32_32x32x16_bf16 v[160:175], v[216:219], v[92:95], v[160:175]\n"
    "ds_read_b64_tr_b16 v[200:201], v234 offset:34816\n"
    "ds_read_b64_tr_b16 v[202:203], v234 offset:35328\n"
    "ds_read_b64_tr_b16 v[204:205], v235 offset:34816\n"
    "ds_read_b64_tr_b16 v[206:207], v235 offset:35328\n"
    "v_mfma_f32_32x32x16_bf16 v[176:191], v[220:223], v[92:95], v[176:191]\n"
    "s_waitcnt lgkmcnt(6)\n"
    "ds_read_b64_tr_b16 v[208:209], v234 offset:36864\n"
    "ds_read_b64_tr_b16 v[210:211], v234 offset:37376\n"
    "ds_read_b64_tr_b16 v[212:213], v235 offset:36864\n"
    "ds_read_b64_tr_b16 v[214:215], v235 offset:37376\n"
    "v_mfma_f32_32x32x16_bf16 v[160:175], v[192:195], v[96:99], v[160:175]\n"
    "s_waitcnt lgkmcnt(8)\n"
    "ds_read_b64_tr_b16 v[216:217], v234 offset:38912\n"
    "ds_read_b64_tr_b16 v[218:219], v234 offset:39424\n"
    "buffer_store_dwordx4 v[136:139], v238, s[44:47], s84 offen offset:1024 sc1\n"
    "v_mfma_f32_32x32x16_bf16 v[176:191], v[196:199], v[96:99], v[176:191]\n"
    "s_waitcnt lgkmcnt(8)\n"
    "ds_read_b64_tr_b16 v[220:221], v235 offset:38912\n"
    "ds_read_b64_tr_b16 v[222:223], v235 offset:39424\n"
    "v_mfma_f32_32x32x16_bf16 v[160:175], v[200:203], v[100:103], v[160:175]\n"
    "s_waitcnt lgkmcnt(8)\n"
    "ds_read_b64_tr_b16 v[192:193], v234 offset:40960\n"
    "ds_read_b64_tr_b16 v[194:195], v234 offset:41472\n"
    "v_mfma_f32_32x32x16_bf16 v[176:191], v[204:207], v[100:103], v[176:191]\n"
    "s_waitcnt lgkmcnt(8)\n"
    "ds_read_b64_tr_b16 v[196:197], v235 offset:40960\n"
    "ds_read_b64_tr_b16 v[198:199], v235 offset:41472\n"
    "buffer_store_dwordx4 v[144:147], v238, s[44:47], s84 offen offset:2048 sc1\n"
    "v_mfma_f32_32x32x16_bf16 v[160:175], v[208:211], v[104:107], v[160:175]\n"
    "s_waitcnt lgkmcnt(8)\n"
    "ds_read_b64_tr_b16 v[200:201], v234 offset:43008\n"
    "ds_read_b64_tr_b16 v[202:203], v234 offset:43520\n"
    "v_mfma_f32_32x32x16_bf16 v[176:191], v[212:215], v[104:107], v[176:191]\n"
    "s_waitcnt lgkmcnt(8)\n"
    "ds_read_b64_tr_b16 v[204:205], v235 offset:43008\n"
    "ds_read_b64_tr_b16 v[206:207], v235 offset:43520\n"
    "v_mfma_f32_32x32x16_bf16 v[160:175], v[216:219], v[108:111], v[160:175]\n"
    "s_waitcnt lgkmcnt(8)\n"
    "ds_read_b64_tr_b16 v[208:209], v234 offset:45056\n"
    "ds_read_b64_tr_b16 v[210:211], v234 offset:45568\n"
    "buffer_store_dwordx4 v[152:155], v238, s[44:47], s84 offen offset:3072 sc1\n"
    "v_mfma_f32_32x32x16_bf16 v[176:191], v[220:223], v[108:111], v[176:191]\n"
    "s_waitcnt lgkmcnt(8)\n"
    "ds_read_b64_tr_b16 v[212:213], v235 offset:45056\n"
    "ds_read_b64_tr_b16 v[214:215], v235 offset:45568\n"
    "v_mfma_f32_32x32x16_bf16 v[160:175], v[192:195], v[112:115], v[160:175]\n"
    "s_waitcnt lgkmcnt(8)\n"
    "ds_read_b64_tr_b16 v[216:217], v234 offset:47104\n"
    "ds_read_b64_tr_b16 v[218:219], v234 offset:47616\n"
    "v_mfma_f32_32x32x16_bf16 v[176:191], v[196:199], v[112:115], v[176:191]\n"
    "s_waitcnt lgkmcnt(8)\n"
    "ds_read_b64_tr_b16 v[220:221], v235 offset:47104\n"
    "ds_read_b64_tr_b16 v[222:223], v235 offset:47616\n"
    "v_mfma_f32_32x32x16_bf16 v[160:175], v[200:203], v[116:119], v[160:175]\n"
    "s_waitcnt lgkmcnt(8)\n"
    "v_mfma_f32_32x32x16_bf16 v[176:191], v[204:207], v[116:119], v[176:191]\n"
    "s_waitcnt lgkmcnt(6)\n"
    "v_mfma_f32_32x32x16_bf16 v[160:175], v[208:211], v[120:123], v[160:175]\n"
    "s_waitcnt lgkmcnt(0)\n"
    "s_barrier\n"
    "v_mfma_f32_32x32x16_bf16 v[176:191], v[212:215], v[120:123], v[176:191]\n"
    "ds_read_b64 v[240:241], v248 offset:512\n"
    "ds_read_b64 v[242:243], v248 offset:768\n"
    "ds_read_b64_tr_b16 v[192:193], v234 offset:49152\n"
    "ds_read_b64_tr_b16 v[194:195], v234 offset:49664\n"
    "ds_read_b64_tr_b16 v[196:197], v235 offset:49152\n"
    "ds_read_b64_tr_b16 v[198:199], v235 offset:49664\n"
    "v_mfma_f32_32x32x16_bf16 v[160:175], v[216:219], v[124:127], v[160:175]\n"
    "ds_read_b64_tr_b16 v[200:201], v234 offset:51200\n"
    "ds_read_b64_tr_b16 v[202:203], v234 offset:51712\n"
    "ds_read_b64_tr_b16 v[204:205], v235 offset:51200\n"
    "ds_read_b64_tr_b16 v[206:207], v235 offset:51712\n"
    "v_mfma_f32_32x32x16_bf16 v[176:191], v[220:223], v[124:127], v[176:191]\n"
    "s_waitcnt lgkmcnt(6)\n"
    "ds_read_b64_tr_b16 v[208:209], v234 offset:53248\n"
    "ds_read_b64_tr_b16 v[210:211], v234 offset:53760\n"
    "ds_read_b64_tr_b16 v[212:213], v235 offset:53248\n"
    "ds_read_b64_tr_b16 v[214:215], v235 offset:53760\n"
    "v_mfma_f32_32x32x16_bf16 v[160:175], v[240:243], v[244:247], v[160:175]\n"
    "v_mfma_f32_32x32x16_bf16 v[128:143], v[192:195], v[0:3], 0\n"
    "s_waitcnt lgkmcnt(8)\n"
    "ds_read_b64_tr_b16 v[216:217], v234 offset:55296\n"
    "ds_read_b64_tr_b16 v[218:219], v234 offset:55808\n"
    "v_mfma_f32_32x32x16_bf16 v[176:191], v[242:245], v[244:247], v[176:191]\n"
    "v_mfma_f32_32x32x16_bf16 v[144:159], v[196:199], v[0:3], 0\n"
    "s_waitcnt lgkmcnt(8)\n"
    "ds_read_b64_tr_b16 v[220:221], v235 offset:55296\n"
    "ds_read_b64_tr_b16 v[222:223], v235 offset:55808\n"
    "v_mfma_f32_32x32x16_bf16 v[128:143], v[200:203], v[4:7], v[128:143]\n"
    "s_waitcnt lgkmcnt(8)\n"
    "ds_read_b64_tr_b16 v[192:193], v234 offset:57344\n"
    "ds_read_b64_tr_b16 v[194:195], v234 offset:57856\n"
    "v_mfma_f32_32x32x16_bf16 v[144:159], v[204:207], v[4:7], v[144:159]\n"
    "s_waitcnt lgkmcnt(8)\n"
    "ds_read_b64_tr_b16 v[196:197], v235 offset:57344\n"
    "ds_read_b64_tr_b16 v[198:199], v235 offset:57856\n"
    "v_mul_f32 v160, v239, v160\n"
    "v_mul_f32 v161, v239, v161\n"
    "v_mul_f32 v162, v239, v162\n"
    "v_mfma_f32_32x32x16_bf16 v[128:143], v[208:211], v[8:11], v[128:143]\n"
    "s_waitcnt lgkmcnt(8)\n"
    "ds_read_b64_tr_b16 v[200:201], v234 offset:59392\n"
    "ds_read_b64_tr_b16 v[202:203], v234 offset:59904\n"
    "v_mul_f32 v163, v239, v163\n"
    "v_mul_f32 v164, v239, v164\n"
    "v_mul_f32 v165, v239, v165\n"
    "v_mfma_f32_32x32x16_bf16 v[144:159], v[212:215], v[8:11], v[144:159]\n"
    "s_waitcnt lgkmcnt(8)\n"
    "ds_read_b64_tr_b16 v[204:205], v235 offset:59392\n"
    "ds_read_b64_tr_b16 v[206:207], v235 offset:59904\n"
    "v_mul_f32 v166, v239, v166\n"
    "v_mul_f32 v167, v239, v167\n"
    "v_mul_f32 v160, v160, abs(v160)\n"
    "v_mfma_f32_32x32x16_bf16 v[128:143], v[216:219], v[12:15], v[128:143]\n"
    "s_waitcnt lgkmcnt(8)\n"
    "ds_read_b64_tr_b16 v[208:209], v234 offset:61440\n"
    "ds_read_b64_tr_b16 v[210:211], v234 offset:61952\n"
    "v_mul_f32 v161, v161, abs(v161)\n"
    "v_mul_f32 v162, v162, abs(v162)\n"
    "v_mul_f32 v163, v163, abs(v163)\n"
    "v_mfma_f32_32x32x16_bf16 v[144:159], v[220:223], v[12:15], v[144:159]\n"
    "s_waitcnt lgkmcnt(8)\n"
    "ds_read_b64_tr_b16 v[212:213], v235 offset:61440\n"
    "ds_read_b64_tr_b16 v[214:215], v235 offset:61952\n"
    "v_mul_f32 v164, v164, abs(v164)\n"
    "v_mul_f32 v165, v165, abs(v165)\n"
    "v_mul_f32 v166, v166, abs(v166)\n"
    "v_mfma_f32_32x32x16_bf16 v[128:143], v[192:195], v[16:19], v[128:143]\n"
    "s_waitcnt lgkmcnt(8)\n"
    "ds_read_b64_tr_b16 v[216:217], v234 offset:63488\n"
    "ds_read_b64_tr_b16 v[218:219], v234 offset:64000\n"
    "v_mul_f32 v167, v167, abs(v167)\n"
    "v_cvt_pk_bf16_f32 v160, v160, v161\n"
    "v_cvt_pk_bf16_f32 v161, v162, v163\n"
    "v_mfma_f32_32x32x16_bf16 v[144:159], v[196:199], v[16:19], v[144:159]\n"
    "s_waitcnt lgkmcnt(8)\n"
    "ds_read_b64_tr_b16 v[220:221], v235 offset:63488\n"
    "ds_read_b64_tr_b16 v[222:223], v235 offset:64000\n"
    "v_cvt_pk_bf16_f32 v162, v164, v165\n"
    "v_cvt_pk_bf16_f32 v163, v166, v167\n"
    "v_pk_max_i16 v160, v160, 0\n"
    "v_mfma_f32_32x32x16_bf16 v[128:143], v[200:203], v[20:23], v[128:143]\n"
    "s_waitcnt lgkmcnt(8)\n"
    "v_pk_max_i16 v161, v161, 0\n"
    "v_pk_max_i16 v162, v162, 0\n"
    "v_pk_max_i16 v163, v163, 0\n"
    "v_mfma_f32_32x32x16_bf16 v[144:159], v[204:207], v[20:23], v[144:159]\n"
    "s_waitcnt lgkmcnt(6)\n"
    "v_mul_f32 v168, v239, v168\n"
    "v_mul_f32 v169, v239, v169\n"
    "v_mul_f32 v170, v239, v170\n"
    "v_mfma_f32_32x32x16_bf16 v[128:143], v[208:211], v[24:27], v[128:143]\n"
    "v_mul_f32 v171, v239, v171\n"
    "v_mul_f32 v172, v239, v172\n"
    "v_mul_f32 v173, v239, v173\n"
    "s_waitcnt vmcnt(0)\n"
    "s_waitcnt lgkmcnt(0)\n"
    "s_barrier\n"
    "v_mfma_f32_32x32x16_bf16 v[144:159], v[212:215], v[24:27], v[144:159]\n"
    "ds_read_b64_tr_b16 v[192:193], v236 offset:0\n"
    "ds_read_b64_tr_b16 v[194:195], v236 offset:512\n"
    "ds_read_b64_tr_b16 v[196:197], v237 offset:0\n"
    "ds_read_b64_tr_b16 v[198:199], v237 offset:512\n"
    "v_mul_f32 v174, v239, v174\n"
    "v_mfma_f32_32x32x16_bf16 v[128:143], v[216:219], v[28:31], v[128:143]\n"
    "ds_read_b64_tr_b16 v[200:201], v236 offset:2048\n"
    "ds_read_b64_tr_b16 v[202:203], v236 offset:2560\n"
    "ds_read_b64_tr_b16 v[204:205], v237 offset:2048\n"
    "ds_read_b64_tr_b16 v[206:207], v237 offset:2560\n"
    "v_mul_f32 v175, v239, v175\n"
    "v_mfma_f32_32x32x16_bf16 v[144:159], v[220:223], v[28:31], v[144:159]\n"
    "s_waitcnt lgkmcnt(6)\n"
    "ds_read_b64_tr_b16 v[208:209], v236 offset:4096\n"
    "ds_read_b64_tr_b16 v[210:211], v236 offset:4608\n"
    "ds_read_b64_tr_b16 v[212:213], v237 offset:4096\n"
    "ds_read_b64_tr_b16 v[214:215], v237 offset:4608\n"
    "v_mul_f32 v168, v168, abs(v168)\n"
    "v_mfma_f32_32x32x16_bf16 v[128:143], v[192:195], v[32:35], v[128:143]\n"
    "s_waitcnt lgkmcnt(8)\n"
    "ds_read_b64_tr_b16 v[216:217], v236 offset:6144\n"
    "ds_read_b64_tr_b16 v[218:219], v236 offset:6656\n"
    "v_mul_f32 v169, v169, abs(v169)\n"
    "v_mul_f32 v170, v170, abs(v170)\n"
    "v_mul_f32 v171, v171, abs(v171)\n"
    "v_mfma_f32_32x32x16_bf16 v[144:159], v[196:199], v[32:35], v[144:159]\n"
    "s_waitcnt lgkmcnt(8)\n"
    "ds_read_b64_tr_b16 v[220:221], v237 offset:6144\n"
    "ds_read_b64_tr_b16 v[222:223], v237 offset:6656\n"
    "v_mul_f32 v172, v172, abs(v172)\n"
    "v_mul_f32 v173, v173, abs(v173)\n"
    "v_mul_f32 v174, v174, abs(v174)\n"
    "v_mfma_f32_32x32x16_bf16 v[128:143], v[200:203], v[36:39], v[128:143]\n"
    "s_waitcnt lgkmcnt(8)\n"
    "ds_read_b64_tr_b16 v[192:193], v236 offset:8192\n"
    "ds_read_b64_tr_b16 v[194:195], v236 offset:8704\n"
    "v_mul_f32 v175, v175, abs(v175)\n"
    "v_cvt_pk_bf16_f32 v168, v168, v169\n"
    "v_cvt_pk_bf16_f32 v169, v170, v171\n"
    "v_mfma_f32_32x32x16_bf16 v[144:159], v[204:207], v[36:39], v[144:159]\n"
    "s_waitcnt lgkmcnt(8)\n"
    "ds_read_b64_tr_b16 v[196:197], v237 offset:8192\n"
    "ds_read_b64_tr_b16 v[198:199], v237 offset:8704\n"
    "v_cvt_pk_bf16_f32 v170, v172, v173\n"
    "v_cvt_pk_bf16_f32 v171, v174, v175\n"
    "v_pk_max_i16 v168, v168, 0\n"
    "v_mfma_f32_32x32x16_bf16 v[128:143], v[208:211], v[40:43], v[128:143]\n"
    "s_waitcnt lgkmcnt(8)\n"
    "ds_read_b64_tr_b16 v[200:201], v236 offset:10240\n"
    "ds_read_b64_tr_b16 v[202:203], v236 offset:10752\n"
    "v_pk_max_i16 v169, v169, 0\n"
    "v_pk_max_i16 v170, v170, 0\n"
    "v_pk_max_i16 v171, v171, 0\n"
    "v_mfma_f32_32x32x16_bf16 v[144:159], v[212:215], v[40:43], v[144:159]\n"
    "s_waitcnt lgkmcnt(8)\n"
    "ds_read_b64_tr_b16 v[204:205], v237 offset:10240\n"
    "ds_read_b64_tr_b16 v[206:207], v237 offset:10752\n"
    "v_mul_f32 v176, v239, v176\n"
    "v_mul_f32 v177, v239, v177\n"
    "v_mul_f32 v178, v239, v178\n"
    "v_mfma_f32_32x32x16_bf16 v[128:143], v[216:219], v[44:47], v[128:143]\n"
    "s_waitcnt lgkmcnt(8)\n"
    "ds_read_b64_tr_b16 v[208:209], v236 offset:12288\n"
    "ds_read_b64_tr_b16 v[210:211], v236 offset:12800\n"
    "v_mul_f32 v179, v239, v179\n"
    "v_mul_f32 v180, v239, v180\n"
    "v_mul_f32 v181, v239, v181\n"
    "v_mfma_f32_32x32x16_bf16 v[144:159], v[220:223], v[44:47], v[144:159]\n"
    "s_waitcnt lgkmcnt(8)\n"
    "ds_read_b64_tr_b16 v[212:213], v237 offset:12288\n"
    "ds_read_b64_tr_b16 v[214:215], v237 offset:12800\n"
    "v_mul_f32 v182, v239, v182\n"
    "v_mul_f32 v183, v239, v183\n"
    "v_mfma_f32_32x32x16_bf16 v[128:143], v[192:195], v[48:51], v[128:143]\n"
    "s_waitcnt lgkmcnt(8)\n"
    "ds_read_b64_tr_b16 v[216:217], v236 offset:14336\n"
    "ds_read_b64_tr_b16 v[218:219], v236 offset:14848\n"
    "v_mul_f32 v176, v176, abs(v176)\n"
    "v_mul_f32 v177, v177, abs(v177)\n"
    "v_mfma_f32_32x32x16_bf16 v[144:159], v[196:199], v[48:51], v[144:159]\n"
    "s_waitcnt lgkmcnt(8)\n"
    "ds_read_b64_tr_b16 v[220:221], v237 offset:14336\n"
    "ds_read_b64_tr_b16 v[222:223], v237 offset:14848\n"
    "v_mul_f32 v178, v178, abs(v178)\n"
    "v_mul_f32 v179, v179, abs(v179)\n"
    "v_mfma_f32_32x32x16_bf16 v[128:143], v[200:203], v[52:55], v[128:143]\n"
    "s_waitcnt lgkmcnt(8)\n"
    "v_mul_f32 v180, v180, abs(v180)\n"
    "v_mul_f32 v181, v181, abs(v181)\n"
    "v_mfma_f32_32x32x16_bf16 v[144:159], v[204:207], v[52:55], v[144:159]\n"
    "s_waitcnt lgkmcnt(6)\n"
    "v_mul_f32 v182, v182, abs(v182)\n"
    "v_mul_f32 v183, v183, abs(v183)\n"
    "v_mfma_f32_32x32x16_bf16 v[128:143], v[208:211], v[56:59], v[128:143]\n"
    "s_waitcnt lgkmcnt(0)\n"
    "s_barrier\n"
    "v_mfma_f32_32x32x16_bf16 v[144:159], v[212:215], v[56:59], v[144:159]\n"
    "ds_read_b64_tr_b16 v[192:193], v232 offset:0\n"
    "ds_read_b64_tr_b16 v[194:195], v232 offset:512\n"
    "ds_read_b64_tr_b16 v[196:197], v233 offset:0\n"
    "ds_read_b64_tr_b16 v[198:199], v233 offset:512\n"
    "v_cvt_pk_bf16_f32 v176, v176, v177\n"
    "v_mfma_f32_32x32x16_bf16 v[128:143], v[216:219], v[60:63], v[128:143]\n"
    "ds_read_b64_tr_b16 v[200:201], v232 offset:2048\n"
    "ds_read_b64_tr_b16 v[202:203], v232 offset:2560\n"
    "ds_read_b64_tr_b16 v[204:205], v233 offset:2048\n"
    "ds_read_b64_tr_b16 v[206:207], v233 offset:2560\n"
    "v_cvt_pk_bf16_f32 v177, v178, v179\n"
    "v_mfma_f32_32x32x16_bf16 v[144:159], v[220:223], v[60:63], v[144:159]\n"
    "s_waitcnt lgkmcnt(6)\n"
    "ds_read_b64_tr_b16 v[208:209], v232 offset:4096\n"
    "ds_read_b64_tr_b16 v[210:211], v232 offset:4608\n"
    "ds_read_b64_tr_b16 v[212:213], v233 offset:4096\n"
    "ds_read_b64_tr_b16 v[214:215], v233 offset:4608\n"
    "v_cvt_pk_bf16_f32 v178, v180, v181\n"
    "v_mfma_f32_32x32x16_bf16 v[128:143], v[192:195], v[64:67], v[128:143]\n"
    "s_waitcnt lgkmcnt(8)\n"
    "ds_read_b64_tr_b16 v[216:217], v232 offset:6144\n"
    "ds_read_b64_tr_b16 v[218:219], v232 offset:6656\n"
    "v_cvt_pk_bf16_f32 v179, v182, v183\n"
    "v_pk_max_i16 v176, v176, 0\n"
    "v_pk_max_i16 v177, v177, 0\n"
    "v_mfma_f32_32x32x16_bf16 v[144:159], v[196:199], v[64:67], v[144:159]\n"
    "s_waitcnt lgkmcnt(8)\n"
    "ds_read_b64_tr_b16 v[220:221], v233 offset:6144\n"
    "ds_read_b64_tr_b16 v[222:223], v233 offset:6656\n"
    "v_pk_max_i16 v178, v178, 0\n"
    "v_pk_max_i16 v179, v179, 0\n"
    "v_mul_f32 v184, v239, v184\n"
    "v_mfma_f32_32x32x16_bf16 v[128:143], v[200:203], v[68:71], v[128:143]\n"
    "s_waitcnt lgkmcnt(8)\n"
    "ds_read_b64_tr_b16 v[192:193], v232 offset:8192\n"
    "ds_read_b64_tr_b16 v[194:195], v232 offset:8704\n"
    "v_mul_f32 v185, v239, v185\n"
    "v_mul_f32 v186, v239, v186\n"
    "v_mul_f32 v187, v239, v187\n"
    "v_mfma_f32_32x32x16_bf16 v[144:159], v[204:207], v[68:71], v[144:159]\n"
    "s_waitcnt lgkmcnt(8)\n"
    "ds_read_b64_tr_b16 v[196:197], v233 offset:8192\n"
    "ds_read_b64_tr_b16 v[198:199], v233 offset:8704\n"
    "v_mul_f32 v188, v239, v188\n"
    "v_mul_f32 v189, v239, v189\n"
    "v_mul_f32 v190, v239, v190\n"
    "v_mfma_f32_32x32x16_bf16 v[128:143], v[208:211], v[72:75], v[128:143]\n"
    "s_waitcnt lgkmcnt(8)\n"
    "ds_read_b64_tr_b16 v[200:201], v232 offset:10240\n"
    "ds_read_b64_tr_b16 v[202:203], v232 offset:10752\n"
    "v_mul_f32 v191, v239, v191\n"
    "v_mul_f32 v184, v184, abs(v184)\n"
    "v_mul_f32 v185, v185, abs(v185)\n"
    "v_mfma_f32_32x32x16_bf16 v[144:159], v[212:215], v[72:75], v[144:159]\n"
    "s_waitcnt lgkmcnt(8)\n"
    "ds_read_b64_tr_b16 v[204:205], v233 offset:10240\n"
    "ds_read_b64_tr_b16 v[206:207], v233 offset:10752\n"
    "v_mul_f32 v186, v186, abs(v186)\n"
    "v_mul_f32 v187, v187, abs(v187)\n"
    "v_mfma_f32_32x32x16_bf16 v[128:143], v[216:219], v[76:79], v[128:143]\n"
    "s_waitcnt lgkmcnt(8)\n"
    "ds_read_b64_tr_b16 v[208:209], v232 offset:12288\n"
    "ds_read_b64_tr_b16 v[210:211], v232 offset:12800\n"
    "v_mul_f32 v188, v188, abs(v188)\n"
    "v_mul_f32 v189, v189, abs(v189)\n"
    "v_mfma_f32_32x32x16_bf16 v[144:159], v[220:223], v[76:79], v[144:159]\n"
    "s_waitcnt lgkmcnt(8)\n"
    "ds_read_b64_tr_b16 v[212:213], v233 offset:12288\n"
    "ds_read_b64_tr_b16 v[214:215], v233 offset:12800\n"
    "v_mul_f32 v190, v190, abs(v190)\n"
    "v_mul_f32 v191, v191, abs(v191)\n"
    "v_mfma_f32_32x32x16_bf16 v[128:143], v[192:195], v[80:83], v[128:143]\n"
    "s_waitcnt lgkmcnt(8)\n"
    "ds_read_b64_tr_b16 v[216:217], v232 offset:14336\n"
    "ds_read_b64_tr_b16 v[218:219], v232 offset:14848\n"
    "v_cvt_pk_bf16_f32 v184, v184, v185\n"
    "v_cvt_pk_bf16_f32 v185, v186, v187\n"
    "v_mfma_f32_32x32x16_bf16 v[144:159], v[196:199], v[80:83], v[144:159]\n"
    "s_waitcnt lgkmcnt(8)\n"
    "ds_read_b64_tr_b16 v[220:221], v233 offset:14336\n"
    "ds_read_b64_tr_b16 v[222:223], v233 offset:14848\n"
    "v_cvt_pk_bf16_f32 v186, v188, v189\n"
    "v_cvt_pk_bf16_f32 v187, v190, v191\n"
    "v_mfma_f32_32x32x16_bf16 v[128:143], v[200:203], v[84:87], v[128:143]\n"
    "s_waitcnt lgkmcnt(8)\n"
    "v_pk_max_i16 v184, v184, 0\n"
    "v_pk_max_i16 v185, v185, 0\n"
    "v_mfma_f32_32x32x16_bf16 v[144:159], v[204:207], v[84:87], v[144:159]\n"
    "s_waitcnt lgkmcnt(6)\n"
    "v_pk_max_i16 v186, v186, 0\n"
    "v_pk_max_i16 v187, v187, 0\n"
    "v_mfma_f32_32x32x16_bf16 v[128:143], v[208:211], v[88:91], v[128:143]\n"
    "s_waitcnt lgkmcnt(0)\n"
    "s_barrier\n"
    "v_mfma_f32_32x32x16_bf16 v[144:159], v[212:215], v[88:91], v[144:159]\n"
    "ds_read_b64_tr_b16 v[192:193], v232 offset:16384\n"
    "ds_read_b64_tr_b16 v[194:195], v232 offset:16896\n"
    "ds_read_b64_tr_b16 v[196:197], v233 offset:16384\n"
    "ds_read_b64_tr_b16 v[198:199], v233 offset:16896\n"
    "s_add_u32 s84, s85, 16384\n"
    "buffer_store_dwordx4 v[160:163], v238, s[44:47], s84 offen offset:0 sc1\n"
    "v_mfma_f32_32x32x16_bf16 v[128:143], v[216:219], v[92:95], v[128:143]\n"
    "ds_read_b64_tr_b16 v[200:201], v232 offset:18432\n"
    "ds_read_b64_tr_b16 v[202:203], v232 offset:18944\n"
    "ds_read_b64_tr_b16 v[204:205], v233 offset:18432\n"
    "ds_read_b64_tr_b16 v[206:207], v233 offset:18944\n"
    "v_mfma_f32_32x32x16_bf16 v[144:159], v[220:223], v[92:95], v[144:159]\n"
    "s_waitcnt lgkmcnt(6)\n"
    "ds_read_b64_tr_b16 v[208:209], v232 offset:20480\n"
    "ds_read_b64_tr_b16 v[210:211], v232 offset:20992\n"
    "ds_read_b64_tr_b16 v[212:213], v233 offset:20480\n"
    "ds_read_b64_tr_b16 v[214:215], v233 offset:20992\n"
    "v_mfma_f32_32x32x16_bf16 v[128:143], v[192:195], v[96:99], v[128:143]\n"
    "s_waitcnt lgkmcnt(8)\n"
    "ds_read_b64_tr_b16 v[216:217], v232 offset:22528\n"
    "ds_read_b64_tr_b16 v[218:219], v232 offset:23040\n"
    "buffer_store_dwordx4 v[168:171], v238, s[44:47], s84 offen offset:1024 sc1\n"
    "v_mfma_f32_32x32x16_bf16 v[144:159], v[196:199], v[96:99], v[144:159]\n"
    "s_waitcnt lgkmcnt(8)\n"
    "ds_read_b64_tr_b16 v[220:221], v233 offset:22528\n"
    "ds_read_b64_tr_b16 v[222:223], v233 offset:23040\n"
    "v_mfma_f32_32x32x16_bf16 v[128:143], v[200:203], v[100:103], v[128:143]\n"
    "s_waitcnt lgkmcnt(8)\n"
    "ds_read_b64_tr_b16 v[192:193], v232 offset:24576\n"
    "ds_read_b64_tr_b16 v[194:195], v232 offset:25088\n"
    "v_mfma_f32_32x32x16_bf16 v[144:159], v[204:207], v[100:103], v[144:159]\n"
    "s_waitcnt lgkmcnt(8)\n"
    "ds_read_b64_tr_b16 v[196:197], v233 offset:24576\n"
    "ds_read_b64_tr_b16 v[198:199], v233 offset:25088\n"
    "buffer_store_dwordx4 v[176:179], v238, s[44:47], s84 offen offset:2048 sc1\n"
    "v_mfma_f32_32x32x16_bf16 v[128:143], v[208:211], v[104:107], v[128:143]\n"
    "s_waitcnt lgkmcnt(8)\n"
    "ds_read_b64_tr_b16 v[200:201], v232 offset:26624\n"
    "ds_read_b64_tr_b16 v[202:203], v232 offset:27136\n"
    "v_mfma_f32_32x32x16_bf16 v[144:159], v[212:215], v[104:107], v[144:159]\n"
    "s_waitcnt lgkmcnt(8)\n"
    "ds_read_b64_tr_b16 v[204:205], v233 offset:26624\n"
    "ds_read_b64_tr_b16 v[206:207], v233 offset:27136\n"
    "v_mfma_f32_32x32x16_bf16 v[128:143], v[216:219], v[108:111], v[128:143]\n"
    "s_waitcnt lgkmcnt(8)\n"
    "ds_read_b64_tr_b16 v[208:209], v232 offset:28672\n"
    "ds_read_b64_tr_b16 v[210:211], v232 offset:29184\n"
    "buffer_store_dwordx4 v[184:187], v238, s[44:47], s84 offen offset:3072 sc1\n"
    "v_mfma_f32_32x32x16_bf16 v[144:159], v[220:223], v[108:111], v[144:159]\n"
    "s_waitcnt lgkmcnt(8)\n"
    "ds_read_b64_tr_b16 v[212:213], v233 offset:28672\n"
    "ds_read_b64_tr_b16 v[214:215], v233 offset:29184\n"
    "v_mfma_f32_32x32x16_bf16 v[128:143], v[192:195], v[112:115], v[128:143]\n"
    "s_waitcnt lgkmcnt(8)\n"
    "ds_read_b64_tr_b16 v[216:217], v232 offset:30720\n"
    "ds_read_b64_tr_b16 v[218:219], v232 offset:31232\n"
    "v_mfma_f32_32x32x16_bf16 v[144:159], v[196:199], v[112:115], v[144:159]\n"
    "s_waitcnt lgkmcnt(8)\n"
    "ds_read_b64_tr_b16 v[220:221], v233 offset:30720\n"
    "ds_read_b64_tr_b16 v[222:223], v233 offset:31232\n"
    "v_mfma_f32_32x32x16_bf16 v[128:143], v[200:203], v[116:119], v[128:143]\n"
    "s_waitcnt lgkmcnt(8)\n"
    "v_mfma_f32_32x32x16_bf16 v[144:159], v[204:207], v[116:119], v[144:159]\n"
    "s_waitcnt lgkmcnt(6)\n"
    "v_mfma_f32_32x32x16_bf16 v[128:143], v[208:211], v[120:123], v[128:143]\n"
    "s_waitcnt lgkmcnt(0)\n"
    "s_barrier\n"
    "v_mfma_f32_32x32x16_bf16 v[144:159], v[212:215], v[120:123], v[144:159]\n"
    "v_mov_b32 v232, v229\n"
    "v_add_u32 v234, 0x10000, v229\n"
    "v_add_u32 v236, 0x20000, v229\n"
    "ds_read_b64 v[240:241], v248 offset:1024\n"
    "ds_read_b64 v[242:243], v248 offset:1280\n"
    "ds_read_b64_tr_b16 v[192:193], v232 offset:32768\n"
    "ds_read_b64_tr_b16 v[194:195], v232 offset:33024\n"
    "ds_read_b64_tr_b16 v[196:197], v232 offset:33792\n"
    "ds_read_b64_tr_b16 v[198:199], v232 offset:34048\n"
    "v_mfma_f32_32x32x16_bf16 v[128:143], v[216:219], v[124:127], v[128:143]\n"
    "ds_read_b64_tr_b16 v[200:201], v232 offset:34816\n"
    "ds_read_b64_tr_b16 v[202:203], v232 offset:35072\n"
    "ds_read_b64_tr_b16 v[204:205], v232 offset:35840\n"
    "ds_read_b64_tr_b16 v[206:207], v232 offset:36096\n"
    "v_mfma_f32_32x32x16_bf16 v[144:159], v[220:223], v[124:127], v[144:159]\n"
    "s_waitcnt lgkmcnt(6)\n"
    "ds_read_b64_tr_b16 v[208:209], v232 offset:36864\n"
    "ds_read_b64_tr_b16 v[210:211], v232 offset:37120\n"
    "ds_read_b64_tr_b16 v[212:213], v232 offset:37888\n"
    "ds_read_b64_tr_b16 v[214:215], v232 offset:38144\n"
    "v_mfma_f32_32x32x16_bf16 v[128:143], v[240:243], v[244:247], v[128:143]\n"
    "v_mfma_f32_32x32x16_bf16 v[144:159], v[242:245], v[244:247], v[144:159]\n"
    "v_mfma_f32_32x32x16_bf16 v[160:175], v[192:195], v[0:3], 0\n"
    "s_waitcnt lgkmcnt(8)\n"
    "ds_read_b64_tr_b16 v[216:217], v232 offset:38912\n"
    "ds_read_b64_tr_b16 v[218:219], v232 offset:39168\n"
    "v_mfma_f32_32x32x16_bf16 v[160:175], v[196:199], v[4:7], v[160:175]\n"
    "s_waitcnt lgkmcnt(8)\n"
    "ds_read_b64_tr_b16 v[220:221], v232 offset:39936\n"
    "ds_read_b64_tr_b16 v[222:223], v232 offset:40192\n"
    "v_mfma_f32_32x32x16_bf16 v[160:175], v[200:203], v[8:11], v[160:175]\n"
    "s_waitcnt lgkmcnt(8)\n"
    "ds_read_b64_tr_b16 v[192:193], v232 offset:40960\n"
    "ds_read_b64_tr_b16 v[194:195], v232 offset:41216\n"
    "v_mfma_f32_32x32x16_bf16 v[160:175], v[204:207], v[12:15], v[160:175]\n"
    "s_waitcnt lgkmcnt(8)\n"
    "ds_read_b64_tr_b16 v[196:197], v232 offset:41984\n"
    "ds_read_b64_tr_b16 v[198:199], v232 offset:42240\n"
    "v_mul_f32 v128, v239, v128\n"
    "v_mul_f32 v129, v239, v129\n"
    "v_mul_f32 v130, v239, v130\n"
    "v_mul_f32 v131, v239, v131\n"
    "v_mfma_f32_32x32x16_bf16 v[160:175], v[208:211], v[16:19], v[160:175]\n"
    "s_waitcnt lgkmcnt(8)\n"
    "ds_read_b64_tr_b16 v[200:201], v232 offset:43008\n"
    "ds_read_b64_tr_b16 v[202:203], v232 offset:43264\n"
    "v_mul_f32 v132, v239, v132\n"
    "v_mul_f32 v133, v239, v133\n"
    "v_mul_f32 v134, v239, v134\n"
    "v_mul_f32 v135, v239, v135\n"
    "v_mfma_f32_32x32x16_bf16 v[160:175], v[212:215], v[20:23], v[160:175]\n"
    "s_waitcnt lgkmcnt(8)\n"
    "ds_read_b64_tr_b16 v[204:205], v232 offset:44032\n"
    "ds_read_b64_tr_b16 v[206:207], v232 offset:44288\n"
    "v_mul_f32 v128, v128, abs(v128)\n"
    "v_mul_f32 v129, v129, abs(v129)\n"
    "v_mul_f32 v130, v130, abs(v130)\n"
    "v_mul_f32 v131, v131, abs(v131)\n"
    "v_mfma_f32_32x32x16_bf16 v[160:175], v[216:219], v[24:27], v[160:175]\n"
    "s_waitcnt lgkmcnt(8)\n"
    "ds_read_b64_tr_b16 v[208:209], v232 offset:45056\n"
    "ds_read_b64_tr_b16 v[210:211], v232 offset:45312\n"
    "v_mul_f32 v132, v132, abs(v132)\n"
    "v_mul_f32 v133, v133, abs(v133)\n"
    "v_mul_f32 v134, v134, abs(v134)\n"
    "v_mul_f32 v135, v135, abs(v135)\n"
    "v_mfma_f32_32x32x16_bf16 v[160:175], v[220:223], v[28:31], v[160:175]\n"
    "s_waitcnt lgkmcnt(8)\n"
    "ds_read_b64_tr_b16 v[212:213], v232 offset:46080\n"
    "ds_read_b64_tr_b16 v[214:215], v232 offset:46336\n"
    "v_cvt_pk_bf16_f32 v128, v128, v129\n"
    "v_cvt_pk_bf16_f32 v129, v130, v131\n"
    "v_cvt_pk_bf16_f32 v130, v132, v133\n"
    "v_cvt_pk_bf16_f32 v131, v134, v135\n"
    "v_mfma_f32_32x32x16_bf16 v[160:175], v[192:195], v[32:35], v[160:175]\n"
    "s_waitcnt lgkmcnt(8)\n"
    "ds_read_b64_tr_b16 v[216:217], v232 offset:47104\n"
    "ds_read_b64_tr_b16 v[218:219], v232 offset:47360\n"
    "v_pk_max_i16 v128, v128, 0\n"
    "v_pk_max_i16 v129, v129, 0\n"
    "v_pk_max_i16 v130, v130, 0\n"
    "v_pk_max_i16 v131, v131, 0\n"
    "v_mfma_f32_32x32x16_bf16 v[160:175], v[196:199], v[36:39], v[160:175]\n"
    "s_waitcnt lgkmcnt(8)\n"
    "ds_read_b64_tr_b16 v[220:221], v232 offset:48128\n"
    "ds_read_b64_tr_b16 v[222:223], v232 offset:48384\n"
    "s_add_u32 s84, s85, 32768\n"
    "buffer_store_dwordx4 v[128:131], v238, s[44:47], s84 offen offset:0 sc1\n"
    "v_mul_f32 v136, v239, v136\n"
    "v_mul_f32 v137, v239, v137\n"
    "v_mul_f32 v138, v239, v138\n"
    "v_mfma_f32_32x32x16_bf16 v[160:175], v[200:203], v[40:43], v[160:175]\n"
    "s_waitcnt lgkmcnt(8)\n"
    "v_mul_f32 v139, v239, v139\n"
    "v_mul_f32 v140, v239, v140\n"
    "v_mul_f32 v141, v239, v141\n"
    "v_mul_f32 v142, v239, v142\n"
    "v_mfma_f32_32x32x16_bf16 v[160:175], v[204:207], v[44:47], v[160:175]\n"
    "s_waitcnt lgkmcnt(6)\n"
    "v_mul_f32 v143, v239, v143\n"
    "v_mul_f32 v136, v136, abs(v136)\n"
    "v_mul_f32 v137, v137, abs(v137)\n"
    "v_mul_f32 v138, v138, abs(v138)\n"
    "v_mfma_f32_32x32x16_bf16 v[160:175], v[208:211], v[48:51], v[160:175]\n"
    "v_mul_f32 v139, v139, abs(v139)\n"
    "v_mul_f32 v140, v140, abs(v140)\n"
    "v_mul_f32 v141, v141, abs(v141)\n"
    "v_mul_f32 v142, v142, abs(v142)\n"
    "s_waitcnt vmcnt(1)\n"
    "s_waitcnt lgkmcnt(0)\n"
    "s_barrier\n"
    "v_mfma_f32_32x32x16_bf16 v[160:175], v[212:215], v[52:55], v[160:175]\n"
    "ds_read_b64_tr_b16 v[192:193], v232 offset:49152\n"
    "ds_read_b64_tr_b16 v[194:195], v232 offset:49408\n"
    "ds_read_b64_tr_b16 v[196:197], v232 offset:50176\n"
    "ds_read_b64_tr_b16 v[198:199], v232 offset:50432\n"
    "s_add_u32 s76, s87, 0\n"
    "buffer_load_dwordx4 v[0:3], v238, s[44:47], s76 offen offset:0 sc1\n"
    "v_mul_f32 v143, v143, abs(v143)\n"
    "v_cvt_pk_bf16_f32 v136, v136, v137\n"
    "v_cvt_pk_bf16_f32 v137, v138, v139\n"
    "v_cvt_pk_bf16_f32 v138, v140, v141\n"
    "v_cvt_pk_bf16_f32 v139, v142, v143\n"
    "v_mfma_f32_32x32x16_bf16 v[160:175], v[216:219], v[56:59], v[160:175]\n"
    "ds_read_b64_tr_b16 v[200:201], v232 offset:51200\n"
    "ds_read_b64_tr_b16 v[202:203], v232 offset:51456\n"
    "ds_read_b64_tr_b16 v[204:205], v232 offset:52224\n"
    "ds_read_b64_tr_b16 v[206:207], v232 offset:52480\n"
    "buffer_load_dwordx4 v[4:7], v238, s[44:47], s76 offen offset:1024 sc1\n"
    "v_pk_max_i16 v136, v136, 0\n"
    "v_pk_max_i16 v137, v137, 0\n"
    "v_pk_max_i16 v138, v138, 0\n"
    "v_pk_max_i16 v139, v139, 0\n"
    "buffer_store_dwordx4 v[136:139], v238, s[44:47], s84 offen offset:1024 sc1\n"
    "v_mfma_f32_32x32x16_bf16 v[160:175], v[220:223], v[60:63], v[160:175]\n"
    "s_waitcnt lgkmcnt(6)\n"
    "ds_read_b64_tr_b16 v[208:209], v232 offset:53248\n"
    "ds_read_b64_tr_b16 v[210:211], v232 offset:53504\n"
    "ds_read_b64_tr_b16 v[212:213], v232 offset:54272\n"
    "ds_read_b64_tr_b16 v[214:215], v232 offset:54528\n"
    "buffer_load_dwordx4 v[8:11], v238, s[44:47], s76 offen offset:2048 sc1\n"
    "v_mul_f32 v144, v239, v144\n"
    "v_mul_f32 v145, v239, v145\n"
    "v_mul_f32 v146, v239, v146\n"
    "v_mul_f32 v147, v239, v147\n"
    "v_mul_f32 v148, v239, v148\n"
    "v_mfma_f32_32x32x16_bf16 v[160:175], v[192:195], v[64:67], v[160:175]\n"
    "s_waitcnt lgkmcnt(8)\n"
    "ds_read_b64_tr_b16 v[216:217], v232 offset:55296\n"
    "ds_read_b64_tr_b16 v[218:219], v232 offset:55552\n"
    "buffer_load_dwordx4 v[12:15], v238, s[44:47], s76 offen offset:3072 sc1\n"
    "v_mul_f32 v149, v239, v149\n"
    "v_mul_f32 v150, v239, v150\n"
    "v_mul_f32 v151, v239, v151\n"
    "v_mul_f32 v144, v144, abs(v144)\n"
    "v_mul_f32 v145, v145, abs(v145)\n"
    "v_mfma_f32_32x32x16_bf16 v[160:175], v[196:199], v[68:71], v[160:175]\n"
    "s_waitcnt lgkmcnt(8)\n"
    "ds_read_b64_tr_b16 v[220:221], v232 offset:56320\n"
    "ds_read_b64_tr_b16 v[222:223], v232 offset:56576\n"
    "s_add_u32 s76, s87, 4096\n"
    "buffer_load_dwordx4 v[16:19], v238, s[44:47], s76 offen offset:0 sc1\n"
    "v_mul_f32 v146, v146, abs(v146)\n"
    "v_mul_f32 v147, v147, abs(v147)\n"
    "v_mul_f32 v148, v148, abs(v148)\n"
    "v_mul_f32 v149, v149, abs(v149)\n"
    "v_mfma_f32_32x32x16_bf16 v[160:175], v[200:203], v[72:75], v[160:175]\n"
    "s_waitcnt lgkmcnt(8)\n"
    "ds_read_b64_tr_b16 v[192:193], v232 offset:57344\n"
    "ds_read_b64_tr_b16 v[194:195], v232 offset:57600\n"
    "buffer_load_dwordx4 v[20:23], v238, s[44:47], s76 offen offset:1024 sc1\n"
    "v_mul_f32 v150, v150, abs(v150)\n"
    "v_mul_f32 v151, v151, abs(v151)\n"
    "v_cvt_pk_bf16_f32 v144, v144, v145\n"
    "v_cvt_pk_bf16_f32 v145, v146, v147\n"
    "v_mfma_f32_32x32x16_bf16 v[160:175], v[204:207], v[76:79], v[160:175]\n"
    "s_waitcnt lgkmcnt(8)\n"
    "ds_read_b64_tr_b16 v[196:197], v232 offset:58368\n"
    "ds_read_b64_tr_b16 v[198:199], v232 offset:58624\n"
    "buffer_load_dwordx4 v[24:27], v238, s[44:47], s76 offen offset:2048 sc1\n"
    "v_cvt_pk_bf16_f32 v146, v148, v149\n"
    "v_cvt_pk_bf16_f32 v147, v150, v151\n"
    "v_pk_max_i16 v144, v144, 0\n"
    "v_pk_max_i16 v145, v145, 0\n"
    "v_mfma_f32_32x32x16_bf16 v[160:175], v[208:211], v[80:83], v[160:175]\n"
    "s_waitcnt lgkmcnt(8)\n"
    "ds_read_b64_tr_b16 v[200:201], v232 offset:59392\n"
    "ds_read_b64_tr_b16 v[202:203], v232 offset:59648\n"
    "buffer_load_dwordx4 v[28:31], v238, s[44:47], s76 offen offset:3072 sc1\n"
    "v_pk_max_i16 v146, v146, 0\n"
    "v_pk_max_i16 v147, v147, 0\n"
    "buffer_store_dwordx4 v[144:147], v238, s[44:47], s84 offen offset:2048 sc1\n"
    "v_mul_f32 v152, v239, v152\n"
    "v_mfma_f32_32x32x16_bf16 v[160:175], v[212:215], v[84:87], v[160:175]\n"
    "s_waitcnt lgkmcnt(8)\n"
    "ds_read_b64_tr_b16 v[204:205], v232 offset:60416\n"
    "ds_read_b64_tr_b16 v[206:207], v232 offset:60672\n"
    "s_add_u32 s76, s87, 8192\n"
    "buffer_load_dwordx4 v[32:35], v238, s[44:47], s76 offen offset:0 sc1\n"
    "v_mul_f32 v153, v239, v153\n"
    "v_mul_f32 v154, v239, v154\n"
    "v_mul_f32 v155, v239, v155\n"
    "v_mul_f32 v156, v239, v156\n"
    "v_mfma_f32_32x32x16_bf16 v[160:175], v[216:219], v[88:91], v[160:175]\n"
    "s_waitcnt lgkmcnt(8)\n"
    "ds_read_b64_tr_b16 v[208:209], v232 offset:61440\n"
    "ds_read_b64_tr_b16 v[210:211], v232 offset:61696\n"
    "buffer_load_dwordx4 v[36:39], v238, s[44:47], s76 offen offset:1024 sc1\n"
    "v_mul_f32 v157, v239, v157\n"
    "v_mul_f32 v158, v239, v158\n"
    "v_mul_f32 v159, v239, v159\n"
    "v_mul_f32 v152, v152, abs(v152)\n"
    "v_mfma_f32_32x32x16_bf16 v[160:175], v[220:223], v[92:95], v[160:175]\n"
    "s_waitcnt lgkmcnt(8)\n"
    "ds_read_b64_tr_b16 v[212:213], v232 offset:62464\n"
    "ds_read_b64_tr_b16 v[214:215], v232 offset:62720\n"
    "buffer_load_dwordx4 v[40:43], v238, s[44:47], s76 offen offset:2048 sc1\n"
    "v_mul_f32 v153, v153, abs(v153)\n"
    "v_mul_f32 v154, v154, abs(v154)\n"
    "v_mul_f32 v155, v155, abs(v155)\n"
    "v_mul_f32 v156, v156, abs(v156)\n"
    "v_mfma_f32_32x32x16_bf16 v[160:175], v[192:195], v[96:99], v[160:175]\n"
    "s_waitcnt lgkmcnt(8)\n"
    "ds_read_b64_tr_b16 v[216:217], v232 offset:63488\n"
    "ds_read_b64_tr_b16 v[218:219], v232 offset:63744\n"
    "buffer_load_dwordx4 v[44:47], v238, s[44:47], s76 offen offset:3072 sc1\n"
    "v_mul_f32 v157, v157, abs(v157)\n"
    "v_mul_f32 v158, v158, abs(v158)\n"
    "v_mul_f32 v159, v159, abs(v159)\n"
    "v_cvt_pk_bf16_f32 v152, v152, v153\n"
    "v_mfma_f32_32x32x16_bf16 v[160:175], v[196:199], v[100:103], v[160:175]\n"
    "s_waitcnt lgkmcnt(8)\n"
    "ds_read_b64_tr_b16 v[220:221], v232 offset:64512\n"
    "ds_read_b64_tr_b16 v[222:223], v232 offset:64768\n"
    "v_cvt_pk_bf16_f32 v153, v154, v155\n"
    "v_cvt_pk_bf16_f32 v154, v156, v157\n"
    "v_cvt_pk_bf16_f32 v155, v158, v159\n"
    "v_pk_max_i16 v152, v152, 0\n"
    "v_mfma_f32_32x32x16_bf16 v[160:175], v[200:203], v[104:107], v[160:175]\n"
    "s_waitcnt lgkmcnt(8)\n"
    "v_pk_max_i16 v153, v153, 0\n"
    "v_pk_max_i16 v154, v154, 0\n"
    "v_pk_max_i16 v155, v155, 0\n"
    "buffer_store_dwordx4 v[152:155], v238, s[44:47], s84 offen offset:3072 sc1\n"
    "v_mfma_f32_32x32x16_bf16 v[160:175], v[204:207], v[108:111], v[160:175]\n"
    "s_waitcnt lgkmcnt(6)\n"
    "v_mov_b32 v224, v230\n"
    "v_mov_b32 v225, 0\n"
    "v_mov_b32 v226, 0\n"
    "v_mov_b32 v227, 0\n"
    "v_mfma_f32_32x32x16_bf16 v[160:175], v[208:211], v[112:115], v[160:175]\n"
    "s_waitcnt lgkmcnt(0)\n"
    "s_barrier\n"
    "v_mfma_f32_32x32x16_bf16 v[160:175], v[212:215], v[116:119], v[160:175]\n"
    "v_mov_b32 v232, v228\n"
    "v_add_u32 v234, 0x10000, v228\n"
    "v_add_u32 v236, 0x20000, v228\n"
    "v_xor_b32 v233, 64, v232\n"
    "v_xor_b32 v235, 64, v234\n"
    "v_xor_b32 v237, 64, v236\n"
    "ds_read_b64 v[240:241], v248 offset:1536\n"
    "s_setprio 3\n"
    "v_mov_b32 v228, 0x3f80\n"
    "v_cmp_gt_u32 vcc, 32, v255\n"
    "v_mov_b32 v229, 0\n"
    "v_mov_b32 v230, 0\n"
    "v_mov_b32 v231, 0\n"
    "v_cndmask_b32 v228, 0, v228, vcc\n"
    "s_nop 1\n"
    "v_mfma_f32_32x32x16_bf16 v[64:79], v[224:227], v[228:231], 0\n"
    "ds_read_b64_tr_b16 v[192:193], v234 offset:0\n"
    "ds_read_b64_tr_b16 v[194:195], v234 offset:1024\n"
    "ds_read_b64_tr_b16 v[196:197], v235 offset:0\n"
    "ds_read_b64_tr_b16 v[198:199], v235 offset:1024\n"
    "v_mfma_f32_32x32x16_bf16 v[160:175], v[216:219], v[120:123], v[160:175]\n"
    "ds_read_b64_tr_b16 v[200:201], v234 offset:2048\n"
    "ds_read_b64_tr_b16 v[202:203], v234 offset:3072\n"
    "ds_read_b64_tr_b16 v[204:205], v235 offset:2048\n"
    "ds_read_b64_tr_b16 v[206:207], v235 offset:3072\n"
    "v_mfma_f32_32x32x16_bf16 v[160:175], v[220:223], v[124:127], v[160:175]\n"
    "s_waitcnt vmcnt(14) lgkmcnt(6)\n"
    "ds_read_b64_tr_b16 v[208:209], v234 offset:4096\n"
    "ds_read_b64_tr_b16 v[210:211], v234 offset:5120\n"
    "ds_read_b64_tr_b16 v[212:213], v235 offset:4096\n"
    "ds_read_b64_tr_b16 v[214:215], v235 offset:5120\n"
    "v_mfma_f32_32x32x16_bf16 v[160:175], v[240:243], v[244:247], v[160:175]\n"
    "v_mfma_f32_32x32x16_bf16 v[64:79], v[192:195], v[0:3], v[64:79]\n"
    "s_waitcnt lgkmcnt(8)\n"
    "ds_read_b64_tr_b16 v[216:217], v234 offset:6144\n"
    "ds_read_b64_tr_b16 v[218:219], v234 offset:7168\n"
    "v_mfma_f32_32x32x16_bf16 v[80:95], v[196:199], v[0:3], 0\n"
    "s_waitcnt vmcnt(13) lgkmcnt(8)\n"
    "ds_read_b64_tr_b16 v[220:221], v235 offset:6144\n"
    "ds_read_b64_tr_b16 v[222:223], v235 offset:7168\n"
    "v_mfma_f32_32x32x16_bf16 v[64:79], v[200:203], v[4:7], v[64:79]\n"
    "s_waitcnt lgkmcnt(8)\n"
    "s_nop 1\n"
    "v_mul_f32 v160, v239, v160\n"
    "v_mul_f32 v161, v239, v161\n"
    "v_mul_f32 v162, v239, v162\n"
    "v_mul_f32 v163, v239, v163\n"
    "v_mul_f32 v164, v239, v164\n"
    "v_mfma_f32_32x32x16_bf16 v[80:95], v[204:207], v[4:7], v[80:95]\n"
    "s_waitcnt vmcnt(11) lgkmcnt(6)\n"
    "v_mul_f32 v165, v239, v165\n"
    "v_mul_f32 v166, v239, v166\n"
    "v_mul_f32 v167, v239, v167\n"
    "v_mul_f32 v160, v160, abs(v160)\n"
    "v_mul_f32 v161, v161, abs(v161)\n"
    "v_mfma_f32_32x32x16_bf16 v[64:79], v[208:211], v[8:11], v[64:79]\n"
    "s_waitcnt lgkmcnt(0)\n"
    "s_barrier\n"
    "v_mfma_f32_32x32x16_bf16 v[80:95], v[212:215], v[8:11], v[80:95]\n"
    "s_waitcnt vmcnt(10)\n"
    "ds_read_b64_tr_b16 v[192:193], v234 offset:16384\n"
    "ds_read_b64_tr_b16 v[194:195], v234 offset:17408\n"
    "ds_read_b64_tr_b16 v[196:197], v235 offset:16384\n"
    "ds_read_b64_tr_b16 v[198:199], v235 offset:17408\n"
    "s_add_u32 s76, s87, 12288\n"
    "buffer_load_dwordx4 v[48:51], v238, s[44:47], s76 offen offset:0 sc1\n"
    "v_mul_f32 v162, v162, abs(v162)\n"
    "v_mul_f32 v163, v163, abs(v163)\n"
    "v_mul_f32 v164, v164, abs(v164)\n"
    "v_mul_f32 v165, v165, abs(v165)\n"
    "v_mul_f32 v166, v166, abs(v166)\n"
    "v_mul_f32 v167, v167, abs(v167)\n"
    "v_mfma_f32_32x32x16_bf16 v[64:79], v[216:219], v[12:15], v[64:79]\n"
    "ds_read_b64_tr_b16 v[200:201], v234 offset:18432\n"
    "ds_read_b64_tr_b16 v[202:203], v234 offset:19456\n"
    "ds_read_b64_tr_b16 v[204:205], v235 offset:18432\n"
    "ds_read_b64_tr_b16 v[206:207], v235 offset:19456\n"
    "buffer_load_dwordx4 v[52:55], v238, s[44:47], s76 offen offset:1024 sc1\n"
    "v_cvt_pk_bf16_f32 v160, v160, v161\n"
    "v_cvt_pk_bf16_f32 v161, v162, v163\n"
    "v_cvt_pk_bf16_f32 v162, v164, v165\n"
    "v_cvt_pk_bf16_f32 v163, v166, v167\n"
    "v_pk_max_i16 v160, v160, 0\n"
    "v_pk_max_i16 v161, v161, 0\n"
    "v_mfma_f32_32x32x16_bf16 v[80:95], v[220:223], v[12:15], v[80:95]\n"
    "s_waitcnt vmcnt(11) lgkmcnt(6)\n"
    "ds_read_b64_tr_b16 v[208:209], v234 offset:20480\n"
    "ds_read_b64_tr_b16 v[210:211], v234 offset:21504\n"
    "ds_read_b64_tr_b16 v[212:213], v235 offset:20480\n"
    "ds_read_b64_tr_b16 v[214:215], v235 offset:21504\n"
    "buffer_load_dwordx4 v[56:59], v238, s[44:47], s76 offen offset:2048 sc1\n"
    "v_pk_max_i16 v162, v162, 0\n"
    "v_pk_max_i16 v163, v163, 0\n"
    "buffer_store_dwordx4 v[160:163], v238, s[44:47], s86 offen offset:0 sc1\n"
    "v_mul_f32 v168, v239, v168\n"
    "v_mul_f32 v169, v239, v169\n"
    "v_mul_f32 v170, v239, v170\n"
    "v_mfma_f32_32x32x16_bf16 v[64:79], v[192:195], v[16:19], v[64:79]\n"
    "s_waitcnt lgkmcnt(8)\n"
    "ds_read_b64_tr_b16 v[216:217], v234 offset:22528\n"
    "ds_read_b64_tr_b16 v[218:219], v234 offset:23552\n"
    "buffer_load_dwordx4 v[60:63], v238, s[44:47], s76 offen offset:3072 sc1\n"
    "v_mul_f32 v171, v239, v171\n"
    "v_mul_f32 v172, v239, v172\n"
    "v_mul_f32 v173, v239, v173\n"
    "v_mul_f32 v174, v239, v174\n"
    "v_mul_f32 v175, v239, v175\n"
    "v_mul_f32 v168, v168, abs(v168)\n"
    "v_mfma_f32_32x32x16_bf16 v[80:95], v[196:199], v[16:19], v[80:95]\n"
    "s_waitcnt vmcnt(13) lgkmcnt(8)\n"
    "ds_read_b64_tr_b16 v[220:221], v235 offset:22528\n"
    "ds_read_b64_tr_b16 v[222:223], v235 offset:23552\n"
    "v_mul_f32 v169, v169, abs(v169)\n"
    "v_mul_f32 v170, v170, abs(v170)\n"
    "v_mul_f32 v171, v171, abs(v171)\n"
    "v_mul_f32 v172, v172, abs(v172)\n"
    "v_mul_f32 v173, v173, abs(v173)\n"
    "v_mul_f32 v174, v174, abs(v174)\n"
    "v_mfma_f32_32x32x16_bf16 v[64:79], v[200:203], v[20:23], v[64:79]\n"
    "s_waitcnt lgkmcnt(8)\n"
    "v_mul_f32 v175, v175, abs(v175)\n"
    "v_cvt_pk_bf16_f32 v168, v168, v169\n"
    "v_cvt_pk_bf16_f32 v169, v170, v171\n"
    "v_cvt_pk_bf16_f32 v170, v172, v173\n"
    "v_cvt_pk_bf16_f32 v171, v174, v175\n"
    "v_mfma_f32_32x32x16_bf16 v[80:95], v[204:207], v[20:23], v[80:95]\n"
    "s_waitcnt vmcnt(12) lgkmcnt(6)\n"
    "v_pk_max_i16 v168, v168, 0\n"
    "v_pk_max_i16 v169, v169, 0\n"
    "v_pk_max_i16 v170, v170, 0\n"
    "v_pk_max_i16 v171, v171, 0\n"
    "buffer_store_dwordx4 v[168:171], v238, s[44:47], s86 offen offset:1024 sc1\n"
    "v_mfma_f32_32x32x16_bf16 v[64:79], v[208:211], v[24:27], v[64:79]\n"
    "s_waitcnt vmcnt(6)\n"
    "s_waitcnt lgkmcnt(0)\n"
    "s_barrier\n"
    "v_mfma_f32_32x32x16_bf16 v[80:95], v[212:215], v[24:27], v[80:95]\n"
    "ds_read_b64_tr_b16 v[192:193], v234 offset:32768\n"
    "ds_read_b64_tr_b16 v[194:195], v234 offset:33792\n"
    "ds_read_b64_tr_b16 v[196:197], v235 offset:32768\n"
    "ds_read_b64_tr_b16 v[198:199], v235 offset:33792\n"
    "s_add_u32 s76, s87, 16384\n"
    "buffer_load_dwordx4 v[0:3], v238, s[44:47], s76 offen offset:0 sc1\n"
    "v_mfma_f32_32x32x16_bf16 v[64:79], v[216:219], v[28:31], v[64:79]\n"
    "ds_read_b64_tr_b16 v[200:201], v234 offset:34816\n"
    "ds_read_b64_tr_b16 v[202:203], v234 offset:35840\n"
    "ds_read_b64_tr_b16 v[204:205], v235 offset:34816\n"
    "ds_read_b64_tr_b16 v[206:207], v235 offset:35840\n"
    "buffer_load_dwordx4 v[4:7], v238, s[44:47], s76 offen offset:1024 sc1\n"
    "v_mfma_f32_32x32x16_bf16 v[80:95], v[220:223], v[28:31], v[80:95]\n"
    "s_waitcnt lgkmcnt(6)\n"
    "ds_read_b64_tr_b16 v[208:209], v234 offset:36864\n"
    "ds_read_b64_tr_b16 v[210:211], v234 offset:37888\n"
    "ds_read_b64_tr_b16 v[212:213], v235 offset:36864\n"
    "ds_read_b64_tr_b16 v[214:215], v235 offset:37888\n"
    "buffer_load_dwordx4 v[8:11], v238, s[44:47], s76 offen offset:2048 sc1\n"
    "v_mfma_f32_32x32x16_bf16 v[64:79], v[192:195], v[32:35], v[64:79]\n"
    "s_waitcnt lgkmcnt(8)\n"
    "ds_read_b64_tr_b16 v[216:217], v234 offset:38912\n"
    "ds_read_b64_tr_b16 v[218:219], v234 offset:39936\n"
    "buffer_load_dwordx4 v[12:15], v238, s[44:47], s76 offen offset:3072 sc1\n"
    "v_mfma_f32_32x32x16_bf16 v[80:95], v[196:199], v[32:35], v[80:95]\n"
    "s_waitcnt lgkmcnt(8)\n"
    "ds_read_b64_tr_b16 v[220:221], v235 offset:38912\n"
    "ds_read_b64_tr_b16 v[222:223], v235 offset:39936\n"
    "v_mfma_f32_32x32x16_bf16 v[64:79], v[200:203], v[36:39], v[64:79]\n"
    "s_waitcnt lgkmcnt(8)\n"
    "v_mfma_f32_32x32x16_bf16 v[80:95], v[204:207], v[36:39], v[80:95]\n"
    "s_waitcnt lgkmcnt(6)\n"
    "v_mfma_f32_32x32x16_bf16 v[64:79], v[208:211], v[40:43], v[64:79]\n"
    "s_waitcnt lgkmcnt(0)\n"
    "s_barrier\n"
    "v_mfma_f32_32x32x16_bf16 v[80:95], v[212:215], v[40:43], v[80:95]\n"
    "ds_read_b64_tr_b16 v[192:193], v234 offset:49152\n"
    "ds_read_b64_tr_b16 v[194:195], v234 offset:50176\n"
    "ds_read_b64_tr_b16 v[196:197], v235 offset:49152\n"
    "ds_read_b64_tr_b16 v[198:199], v235 offset:50176\n"
    "s_add_u32 s76, s87, 20480\n"
    "buffer_load_dwordx4 v[16:19], v238, s[44:47], s76 offen offset:0 sc1\n"
    "v_mfma_f32_32x32x16_bf16 v[64:79], v[216:219], v[44:47], v[64:79]\n"
    "ds_read_b64_tr_b16 v[200:201], v234 offset:51200\n"
    "ds_read_b64_tr_b16 v[202:203], v234 offset:52224\n"
    "ds_read_b64_tr_b16 v[204:205], v235 offset:51200\n"
    "ds_read_b64_tr_b16 v[206:207], v235 offset:52224\n"
    "buffer_load_dwordx4 v[20:23], v238, s[44:47], s76 offen offset:1024 sc1\n"
    "v_mfma_f32_32x32x16_bf16 v[80:95], v[220:223], v[44:47], v[80:95]\n"
    "s_waitcnt vmcnt(11) lgkmcnt(6)\n"
    "ds_read_b64_tr_b16 v[208:209], v234 offset:53248\n"
    "ds_read_b64_tr_b16 v[210:211], v234 offset:54272\n"
    "ds_read_b64_tr_b16 v[212:213], v235 offset:53248\n"
    "ds_read_b64_tr_b16 v[214:215], v235 offset:54272\n"
    "buffer_load_dwordx4 v[24:27], v238, s[44:47], s76 offen offset:2048 sc1\n"
    "v_mfma_f32_32x32x16_bf16 v[64:79], v[192:195], v[48:51], v[64:79]\n"
    "s_waitcnt lgkmcnt(8)\n"
    "ds_read_b64_tr_b16 v[216:217], v234 offset:55296\n"
    "ds_read_b64_tr_b16 v[218:219], v234 offset:56320\n"
    "buffer_load_dwordx4 v[28:31], v238, s[44:47], s76 offen offset:3072 sc1\n"
    "v_mfma_f32_32x32x16_bf16 v[80:95], v[196:199], v[48:51], v[80:95]\n"
    "s_waitcnt vmcnt(12) lgkmcnt(8)\n"
    "ds_read_b64_tr_b16 v[220:221], v235 offset:55296\n"
    "ds_read_b64_tr_b16 v[222:223], v235 offset:56320\n"
    "v_mfma_f32_32x32x16_bf16 v[64:79], v[200:203], v[52:55], v[64:79]\n"
    "s_waitcnt lgkmcnt(8)\n"
    "v_mfma_f32_32x32x16_bf16 v[80:95], v[204:207], v[52:55], v[80:95]\n"
    "s_waitcnt vmcnt(11) lgkmcnt(6)\n"
    "v_mfma_f32_32x32x16_bf16 v[64:79], v[208:211], v[56:59], v[64:79]\n"
    "s_waitcnt vmcnt(8)\n"
    "s_waitcnt lgkmcnt(0)\n"
    "s_barrier\n"
    "v_mfma_f32_32x32x16_bf16 v[80:95], v[212:215], v[56:59], v[80:95]\n"
    "ds_read_b64_tr_b16 v[192:193], v236 offset:0\n"
    "ds_read_b64_tr_b16 v[194:195], v236 offset:1024\n"
    "ds_read_b64_tr_b16 v[196:197], v237 offset:0\n"
    "ds_read_b64_tr_b16 v[198:199], v237 offset:1024\n"
    "s_add_u32 s76, s87, 24576\n"
    "buffer_load_dwordx4 v[32:35], v238, s[44:47], s76 offen offset:0 sc1\n"
    "v_mfma_f32_32x32x16_bf16 v[64:79], v[216:219], v[60:63], v[64:79]\n"
    "ds_read_b64_tr_b16 v[200:201], v236 offset:2048\n"
    "ds_read_b64_tr_b16 v[202:203], v236 offset:3072\n"
    "ds_read_b64_tr_b16 v[204:205], v237 offset:2048\n"
    "ds_read_b64_tr_b16 v[206:207], v237 offset:3072\n"
    "buffer_load_dwordx4 v[36:39], v238, s[44:47], s76 offen offset:1024 sc1\n"
    "v_mfma_f32_32x32x16_bf16 v[80:95], v[220:223], v[60:63], v[80:95]\n"
    "s_waitcnt vmcnt(9) lgkmcnt(6)\n"
    "ds_read_b64_tr_b16 v[208:209], v236 offset:4096\n"
    "ds_read_b64_tr_b16 v[210:211], v236 offset:5120\n"
    "ds_read_b64_tr_b16 v[212:213], v237 offset:4096\n"
    "ds_read_b64_tr_b16 v[214:215], v237 offset:5120\n"
    "buffer_load_dwordx4 v[40:43], v238, s[44:47], s76 offen offset:2048 sc1\n"
    "v_mfma_f32_32x32x16_bf16 v[64:79], v[192:195], v[0:3], v[64:79]\n"
    "s_waitcnt lgkmcnt(8)\n"
    "ds_read_b64_tr_b16 v[216:217], v236 offset:6144\n"
    "ds_read_b64_tr_b16 v[218:219], v236 offset:7168\n"
    "buffer_load_dwordx4 v[44:47], v238, s[44:47], s76 offen offset:3072 sc1\n"
    "v_mfma_f32_32x32x16_bf16 v[80:95], v[196:199], v[0:3], v[80:95]\n"
    "s_waitcnt vmcnt(10) lgkmcnt(8)\n"
    "ds_read_b64_tr_b16 v[220:221], v237 offset:6144\n"
    "ds_read_b64_tr_b16 v[222:223], v237 offset:7168\n"
    "v_mfma_f32_32x32x16_bf16 v[64:79], v[200:203], v[4:7], v[64:79]\n"
    "s_waitcnt lgkmcnt(8)\n"
    "v_mfma_f32_32x32x16_bf16 v[80:95], v[204:207], v[4:7], v[80:95]\n"
    "s_waitcnt vmcnt(9) lgkmcnt(6)\n"
    "v_mfma_f32_32x32x16_bf16 v[64:79], v[208:211], v[8:11], v[64:79]\n"
    "s_waitcnt lgkmcnt(0)\n"
    "s_barrier\n"
    "v_mfma_f32_32x32x16_bf16 v[80:95], v[212:215], v[8:11], v[80:95]\n"
    "s_waitcnt vmcnt(8)\n"
    "ds_read_b64_tr_b16 v[192:193], v232 offset:0\n"
    "ds_read_b64_tr_b16 v[194:195], v232 offset:1024\n"
    "ds_read_b64_tr_b16 v[196:197], v233 offset:0\n"
    "ds_read_b64_tr_b16 v[198:199], v233 offset:1024\n"
    "s_add_u32 s76, s87, 28672\n"
    "buffer_load_dwordx4 v[48:51], v238, s[44:47], s76 offen offset:0 sc1\n"
    "v_mfma_f32_32x32x16_bf16 v[64:79], v[216:219], v[12:15], v[64:79]\n"
    "ds_read_b64_tr_b16 v[200:201], v232 offset:2048\n"
    "ds_read_b64_tr_b16 v[202:203], v232 offset:3072\n"
    "ds_read_b64_tr_b16 v[204:205], v233 offset:2048\n"
    "ds_read_b64_tr_b16 v[206:207], v233 offset:3072\n"
    "buffer_load_dwordx4 v[52:55], v238, s[44:47], s76 offen offset:1024 sc1\n"
    "v_mfma_f32_32x32x16_bf16 v[80:95], v[220:223], v[12:15], v[80:95]\n"
    "s_waitcnt vmcnt(9) lgkmcnt(6)\n"
    "ds_read_b64_tr_b16 v[208:209], v232 offset:4096\n"
    "ds_read_b64_tr_b16 v[210:211], v232 offset:5120\n"
    "ds_read_b64_tr_b16 v[212:213], v233 offset:4096\n"
    "ds_read_b64_tr_b16 v[214:215], v233 offset:5120\n"
    "buffer_load_dwordx4 v[56:59], v238, s[44:47], s76 offen offset:2048 sc1\n"
    "v_mfma_f32_32x32x16_bf16 v[64:79], v[192:195], v[16:19], v[64:79]\n"
    "s_waitcnt lgkmcnt(8)\n"
    "ds_read_b64_tr_b16 v[216:217], v232 offset:6144\n"
    "ds_read_b64_tr_b16 v[218:219], v232 offset:7168\n"
    "buffer_load_dwordx4 v[60:63], v238, s[44:47], s76 offen offset:3072 sc1\n"
    "v_mfma_f32_32x32x16_bf16 v[80:95], v[196:199], v[16:19], v[80:95]\n"
    "s_waitcnt vmcnt(10) lgkmcnt(8)\n"
    "ds_read_b64_tr_b16 v[220:221], v233 offset:6144\n"
    "ds_read_b64_tr_b16 v[222:223], v233 offset:7168\n"
    "v_mfma_f32_32x32x16_bf16 v[64:79], v[200:203], v[20:23], v[64:79]\n"
    "s_waitcnt lgkmcnt(8)\n"
    "v_mfma_f32_32x32x16_bf16 v[80:95], v[204:207], v[20:23], v[80:95]\n"
    "s_waitcnt vmcnt(9) lgkmcnt(6)\n"
    "v_mfma_f32_32x32x16_bf16 v[64:79], v[208:211], v[24:27], v[64:79]\n"
    "s_waitcnt lgkmcnt(0)\n"
    "s_barrier\n"
    "v_mfma_f32_32x32x16_bf16 v[80:95], v[212:215], v[24:27], v[80:95]\n"
    "s_waitcnt vmcnt(8)\n"
    "ds_read_b64_tr_b16 v[192:193], v232 offset:16384\n"
    "ds_read_b64_tr_b16 v[194:195], v232 offset:17408\n"
    "ds_read_b64_tr_b16 v[196:197], v233 offset:16384\n"
    "ds_read_b64_tr_b16 v[198:199], v233 offset:17408\n"
    "s_add_u32 s76, s87, 32768\n"
    "buffer_load_dwordx4 v[0:3], v238, s[44:47], s76 offen offset:0 sc1\n"
    "v_mfma_f32_32x32x16_bf16 v[64:79], v[216:219], v[28:31], v[64:79]\n"
    "ds_read_b64_tr_b16 v[200:201], v232 offset:18432\n"
    "ds_read_b64_tr_b16 v[202:203], v232 offset:19456\n"
    "ds_read_b64_tr_b16 v[204:205], v233 offset:18432\n"
    "ds_read_b64_tr_b16 v[206:207], v233 offset:19456\n"
    "buffer_load_dwordx4 v[4:7], v238, s[44:47], s76 offen offset:1024 sc1\n"
    "v_mfma_f32_32x32x16_bf16 v[80:95], v[220:223], v[28:31], v[80:95]\n"
    "s_waitcnt vmcnt(9) lgkmcnt(6)\n"
    "ds_read_b64_tr_b16 v[208:209], v232 offset:20480\n"
    "ds_read_b64_tr_b16 v[210:211], v232 offset:21504\n"
    "ds_read_b64_tr_b16 v[212:213], v233 offset:20480\n"
    "ds_read_b64_tr_b16 v[214:215], v233 offset:21504\n"
    "buffer_load_dwordx4 v[8:11], v238, s[44:47], s76 offen offset:2048 sc1\n"
    "v_mfma_f32_32x32x16_bf16 v[64:79], v[192:195], v[32:35], v[64:79]\n"
    "s_waitcnt lgkmcnt(8)\n"
    "ds_read_b64_tr_b16 v[216:217], v232 offset:22528\n"
    "ds_read_b64_tr_b16 v[218:219], v232 offset:23552\n"
    "buffer_load_dwordx4 v[12:15], v238, s[44:47], s76 offen offset:3072 sc1\n"
    "v_mfma_f32_32x32x16_bf16 v[80:95], v[196:199], v[32:35], v[80:95]\n"
    "s_waitcnt vmcnt(10) lgkmcnt(8)\n"
    "ds_read_b64_tr_b16 v[220:221], v233 offset:22528\n"
    "ds_read_b64_tr_b16 v[222:223], v233 offset:23552\n"
    "v_mfma_f32_32x32x16_bf16 v[64:79], v[200:203], v[36:39], v[64:79]\n"
    "s_waitcnt lgkmcnt(8)\n"
    "v_mfma_f32_32x32x16_bf16 v[80:95], v[204:207], v[36:39], v[80:95]\n"
    "s_waitcnt vmcnt(9) lgkmcnt(6)\n"
    "v_mfma_f32_32x32x16_bf16 v[64:79], v[208:211], v[40:43], v[64:79]\n"
    "s_waitcnt lgkmcnt(0)\n"
    "s_barrier\n"
    "v_mfma_f32_32x32x16_bf16 v[80:95], v[212:215], v[40:43], v[80:95]\n"
    "s_waitcnt vmcnt(8)\n"
    "ds_read_b64_tr_b16 v[192:193], v232 offset:32768\n"
    "ds_read_b64_tr_b16 v[194:195], v232 offset:33792\n"
    "ds_read_b64_tr_b16 v[196:197], v233 offset:32768\n"
    "ds_read_b64_tr_b16 v[198:199], v233 offset:33792\n"
    "s_add_u32 s76, s87, 36864\n"
    "buffer_load_dwordx4 v[16:19], v238, s[44:47], s76 offen offset:0 sc1\n"
    "v_mfma_f32_32x32x16_bf16 v[64:79], v[216:219], v[44:47], v[64:79]\n"
    "ds_read_b64_tr_b16 v[200:201], v232 offset:34816\n"
    "ds_read_b64_tr_b16 v[202:203], v232 offset:35840\n"
    "ds_read_b64_tr_b16 v[204:205], v233 offset:34816\n"
    "ds_read_b64_tr_b16 v[206:207], v233 offset:35840\n"
    "buffer_load_dwordx4 v[20:23], v238, s[44:47], s76 offen offset:1024 sc1\n"
    "v_mfma_f32_32x32x16_bf16 v[80:95], v[220:223], v[44:47], v[80:95]\n"
    "s_waitcnt vmcnt(9) lgkmcnt(6)\n"
    "ds_read_b64_tr_b16 v[208:209], v232 offset:36864\n"
    "ds_read_b64_tr_b16 v[210:211], v232 offset:37888\n"
    "ds_read_b64_tr_b16 v[212:213], v233 offset:36864\n"
    "ds_read_b64_tr_b16 v[214:215], v233 offset:37888\n"
    "buffer_load_dwordx4 v[24:27], v238, s[44:47], s76 offen offset:2048 sc1\n"
    "v_mfma_f32_32x32x16_bf16 v[64:79], v[192:195], v[48:51], v[64:79]\n"
    "s_waitcnt lgkmcnt(8)\n"
    "ds_read_b64_tr_b16 v[216:217], v232 offset:38912\n"
    "ds_read_b64_tr_b16 v[218:219], v232 offset:39936\n"
    "buffer_load_dwordx4 v[28:31], v238, s[44:47], s76 offen offset:3072 sc1\n"
    "v_mfma_f32_32x32x16_bf16 v[80:95], v[196:199], v[48:51], v[80:95]\n"
    "s_waitcnt vmcnt(10) lgkmcnt(8)\n"
    "ds_read_b64_tr_b16 v[220:221], v233 offset:38912\n"
    "ds_read_b64_tr_b16 v[222:223], v233 offset:39936\n"
    "v_mfma_f32_32x32x16_bf16 v[64:79], v[200:203], v[52:55], v[64:79]\n"
    "s_waitcnt lgkmcnt(8)\n"
    "v_mfma_f32_32x32x16_bf16 v[80:95], v[204:207], v[52:55], v[80:95]\n"
    "s_waitcnt vmcnt(9) lgkmcnt(6)\n"
    "v_mfma_f32_32x32x16_bf16 v[64:79], v[208:211], v[56:59], v[64:79]\n"
    "s_waitcnt lgkmcnt(0)\n"
    "s_barrier\n"
    "v_mfma_f32_32x32x16_bf16 v[80:95], v[212:215], v[56:59], v[80:95]\n"
    "s_waitcnt vmcnt(8)\n"
    "ds_read_b64_tr_b16 v[192:193], v232 offset:49152\n"
    "ds_read_b64_tr_b16 v[194:195], v232 offset:50176\n"
    "ds_read_b64_tr_b16 v[196:197], v233 offset:49152\n"
    "ds_read_b64_tr_b16 v[198:199], v233 offset:50176\n"
    "s_add_u32 s76, s87, 40960\n"
    "buffer_load_dwordx4 v[32:35], v238, s[44:47], s76 offen offset:0 sc1\n"
    "v_mfma_f32_32x32x16_bf16 v[64:79], v[216:219], v[60:63], v[64:79]\n"
    "ds_read_b64_tr_b16 v[200:201], v232 offset:51200\n"
    "ds_read_b64_tr_b16 v[202:203], v232 offset:52224\n"
    "ds_read_b64_tr_b16 v[204:205], v233 offset:51200\n"
    "ds_read_b64_tr_b16 v[206:207], v233 offset:52224\n"
    "buffer_load_dwordx4 v[36:39], v238, s[44:47], s76 offen offset:1024 sc1\n"
    "v_mfma_f32_32x32x16_bf16 v[80:95], v[220:223], v[60:63], v[80:95]\n"
    "s_waitcnt vmcnt(9) lgkmcnt(6)\n"
    "ds_read_b64_tr_b16 v[208:209], v232 offset:53248\n"
    "ds_read_b64_tr_b16 v[210:211], v232 offset:54272\n"
    "ds_read_b64_tr_b16 v[212:213], v233 offset:53248\n"
    "ds_read_b64_tr_b16 v[214:215], v233 offset:54272\n"
    "buffer_load_dwordx4 v[40:43], v238, s[44:47], s76 offen offset:2048 sc1\n"
    "v_mfma_f32_32x32x16_bf16 v[64:79], v[192:195], v[0:3], v[64:79]\n"
    "s_waitcnt lgkmcnt(8)\n"
    "ds_read_b64_tr_b16 v[216:217], v232 offset:55296\n"
    "ds_read_b64_tr_b16 v[218:219], v232 offset:56320\n"
    "buffer_load_dwordx4 v[44:47], v238, s[44:47], s76 offen offset:3072 sc1\n"
    "v_mfma_f32_32x32x16_bf16 v[80:95], v[196:199], v[0:3], v[80:95]\n"
    "s_waitcnt vmcnt(10) lgkmcnt(8)\n"
    "ds_read_b64_tr_b16 v[220:221], v233 offset:55296\n"
    "ds_read_b64_tr_b16 v[222:223], v233 offset:56320\n"
    "v_mfma_f32_32x32x16_bf16 v[64:79], v[200:203], v[4:7], v[64:79]\n"
    "s_waitcnt lgkmcnt(8)\n"
    "v_mfma_f32_32x32x16_bf16 v[80:95], v[204:207], v[4:7], v[80:95]\n"
    "s_waitcnt vmcnt(9) lgkmcnt(6)\n"
    "v_mfma_f32_32x32x16_bf16 v[64:79], v[208:211], v[8:11], v[64:79]\n"
    "s_waitcnt lgkmcnt(0)\n"
    "s_barrier\n"
    "v_mfma_f32_32x32x16_bf16 v[80:95], v[212:215], v[8:11], v[80:95]\n"
    "s_waitcnt vmcnt(8)\n"
    "ds_read_b64_tr_b16 v[192:193], v234 offset:0\n"
    "ds_read_b64_tr_b16 v[194:195], v234 offset:1024\n"
    "ds_read_b64_tr_b16 v[196:197], v235 offset:0\n"
    "ds_read_b64_tr_b16 v[198:199], v235 offset:1024\n"
    "s_add_u32 s76, s87, 45056\n"
    "buffer_load_dwordx4 v[48:51], v238, s[44:47], s76 offen offset:0 sc1\n"
    "v_and_b32 v96, 31, v255\n"
    "v_bfe_u32 v97, v255, 1, 3\n"
    "v_mfma_f32_32x32x16_bf16 v[64:79], v[216:219], v[12:15], v[64:79]\n"
    "ds_read_b64_tr_b16 v[200:201], v234 offset:2048\n"
    "ds_read_b64_tr_b16 v[202:203], v234 offset:3072\n"
    "ds_read_b64_tr_b16 v[204:205], v235 offset:2048\n"
    "ds_read_b64_tr_b16 v[206:207], v235 offset:3072\n"
    "buffer_load_dwordx4 v[52:55], v238, s[44:47], s76 offen offset:1024 sc1\n"
    "s_lshr_b32 s72, s70, 1\n"
    "s_lshl_b32 s72, s72, 2\n"
    "v_mfma_f32_32x32x16_bf16 v[80:95], v[220:223], v[12:15], v[80:95]\n"
    "s_waitcnt vmcnt(9) lgkmcnt(6)\n"
    "ds_read_b64_tr_b16 v[208:209], v234 offset:4096\n"
    "ds_read_b64_tr_b16 v[210:211], v234 offset:5120\n"
    "ds_read_b64_tr_b16 v[212:213], v235 offset:4096\n"
    "ds_read_b64_tr_b16 v[214:215], v235 offset:5120\n"
    "buffer_load_dwordx4 v[56:59], v238, s[44:47], s76 offen offset:2048 sc1\n"
    "v_xor_b32 v97, s72, v97\n"
    "v_lshlrev_b32 v98, 7, v96\n"
    "v_mfma_f32_32x32x16_bf16 v[64:79], v[192:195], v[16:19], v[64:79]\n"
    "s_waitcnt lgkmcnt(8)\n"
    "ds_read_b64_tr_b16 v[216:217], v234 offset:6144\n"
    "ds_read_b64_tr_b16 v[218:219], v234 offset:7168\n"
    "buffer_load_dwordx4 v[60:63], v238, s[44:47], s76 offen offset:3072 sc1\n"
    "v_lshl_add_u32 v98, v97, 4, v98\n"
    "v_lshrrev_b32 v99, 5, v255\n"
    "v_lshl_add_u32 v98, v99, 3, v98\n"
    "s_and_b32 s72, s70, 1\n"
    "v_mfma_f32_32x32x16_bf16 v[80:95], v[196:199], v[16:19], v[80:95]\n"
    "s_waitcnt vmcnt(10) lgkmcnt(8)\n"
    "ds_read_b64_tr_b16 v[220:221], v235 offset:6144\n"
    "ds_read_b64_tr_b16 v[222:223], v235 offset:7168\n"
    "s_lshl_b32 s72, s72, 12\n"
    "s_add_u32 s72, s72, 0x24000\n"
    "v_add_u32 v100, s72, v98\n"
    "v_xor_b32 v101, 16, v100\n"
    "v_mfma_f32_32x32x16_bf16 v[64:79], v[200:203], v[20:23], v[64:79]\n"
    "s_waitcnt lgkmcnt(8)\n"
    "v_xor_b32 v102, 32, v100\n"
    "v_xor_b32 v103, 48, v100\n"
    "ds_read_b64 v[104:105], v100\n"
    "ds_read_b64 v[106:107], v101\n"
    "v_mfma_f32_32x32x16_bf16 v[80:95], v[204:207], v[20:23], v[80:95]\n"
    "s_waitcnt vmcnt(9) lgkmcnt(8)\n"
    "ds_read_b64 v[108:109], v102\n"
    "ds_read_b64 v[110:111], v103\n"
    "s_waitcnt lgkmcnt(3)\n"
    "v_lshlrev_b32 v144, 16, v104\n"
    "v_and_b32 v145, 0xffff0000, v104\n"
    "v_mfma_f32_32x32x16_bf16 v[64:79], v[208:211], v[24:27], v[64:79]\n"
    "s_waitcnt lgkmcnt(0)\n"
    "s_barrier\n"
    "v_mfma_f32_32x32x16_bf16 v[80:95], v[212:215], v[24:27], v[80:95]\n"
    "s_waitcnt vmcnt(8)\n"
    "ds_read_b64_tr_b16 v[192:193], v234 offset:16384\n"
    "ds_read_b64_tr_b16 v[194:195], v234 offset:17408\n"
    "ds_read_b64_tr_b16 v[196:197], v235 offset:16384\n"
    "ds_read_b64_tr_b16 v[198:199], v235 offset:17408\n"
    "s_add_u32 s76, s87, 49152\n"
    "buffer_load_dwordx4 v[0:3], v238, s[44:47], s76 offen offset:0 sc1\n"
    "v_lshlrev_b32 v146, 16, v105\n"
    "v_and_b32 v147, 0xffff0000, v105\n"
    "v_mfma_f32_32x32x16_bf16 v[64:79], v[216:219], v[28:31], v[64:79]\n"
    "ds_read_b64_tr_b16 v[200:201], v234 offset:18432\n"
    "ds_read_b64_tr_b16 v[202:203], v234 offset:19456\n"
    "ds_read_b64_tr_b16 v[204:205], v235 offset:18432\n"
    "ds_read_b64_tr_b16 v[206:207], v235 offset:19456\n"
    "buffer_load_dwordx4 v[4:7], v238, s[44:47], s76 offen offset:1024 sc1\n"
    "v_lshlrev_b32 v148, 16, v106\n"
    "v_and_b32 v149, 0xffff0000, v106\n"
    "v_mfma_f32_32x32x16_bf16 v[80:95], v[220:223], v[28:31], v[80:95]\n"
    "s_waitcnt vmcnt(9) lgkmcnt(6)\n"
    "ds_read_b64_tr_b16 v[208:209], v234 offset:20480\n"
    "ds_read_b64_tr_b16 v[210:211], v234 offset:21504\n"
    "ds_read_b64_tr_b16 v[212:213], v235 offset:20480\n"
    "ds_read_b64_tr_b16 v[214:215], v235 offset:21504\n"
    "buffer_load_dwordx4 v[8:11], v238, s[44:47], s76 offen offset:2048 sc1\n"
    "v_lshlrev_b32 v150, 16, v107\n"
    "v_and_b32 v151, 0xffff0000, v107\n"
    "v_lshlrev_b32 v152, 16, v108\n"
    "v_and_b32 v153, 0xffff0000, v108\n"
    "v_lshlrev_b32 v154, 16, v109\n"
    "v_mfma_f32_32x32x16_bf16 v[64:79], v[192:195], v[32:35], v[64:79]\n"
    "s_waitcnt lgkmcnt(8)\n"
    "ds_read_b64_tr_b16 v[216:217], v234 offset:22528\n"
    "ds_read_b64_tr_b16 v[218:219], v234 offset:23552\n"
    "buffer_load_dwordx4 v[12:15], v238, s[44:47], s76 offen offset:3072 sc1\n"
    "v_and_b32 v155, 0xffff0000, v109\n"
    "v_lshlrev_b32 v156, 16, v110\n"
    "v_and_b32 v157, 0xffff0000, v110\n"
    "v_lshlrev_b32 v158, 16, v111\n"
    "v_mfma_f32_32x32x16_bf16 v[80:95], v[196:199], v[32:35], v[80:95]\n"
    "s_waitcnt vmcnt(10) lgkmcnt(8)\n"
    "ds_read_b64_tr_b16 v[220:221], v235 offset:22528\n"
    "ds_read_b64_tr_b16 v[222:223], v235 offset:23552\n"
    "v_and_b32 v159, 0xffff0000, v111\n"
    "v_lshlrev_b32 v96, 11, v96\n"
    "v_lshl_add_u32 v96, v99, 4, v96\n"
    "s_lshl_b32 s72, s68, 16\n"
    "v_mfma_f32_32x32x16_bf16 v[64:79], v[200:203], v[36:39], v[64:79]\n"
    "s_waitcnt lgkmcnt(8)\n"
    "s_and_b32 s73, s70, 1\n"
    "s_lshl_b32 s73, s73, 10\n"
    "s_add_u32 s72, s72, s73\n"
    "s_lshl_b32 s73, s69, 7\n"
    "v_mfma_f32_32x32x16_bf16 v[80:95], v[204:207], v[36:39], v[80:95]\n"
    "s_waitcnt vmcnt(9) lgkmcnt(6)\n"
    "s_add_u32 s72, s72, s73\n"
    "s_lshr_b32 s73, s70, 1\n"
    "s_lshl_b32 s73, s73, 6\n"
    "s_add_u32 s84, s72, s73\n"
    "v_mfma_f32_32x32x16_bf16 v[64:79], v[208:211], v[40:43], v[64:79]\n"
    "s_waitcnt lgkmcnt(0)\n"
    "s_barrier\n"
    "v_mfma_f32_32x32x16_bf16 v[80:95], v[212:215], v[40:43], v[80:95]\n"
    "s_waitcnt vmcnt(8)\n"
    "ds_read_b64_tr_b16 v[192:193], v234 offset:32768\n"
    "ds_read_b64_tr_b16 v[194:195], v234 offset:33792\n"
    "ds_read_b64_tr_b16 v[196:197], v235 offset:32768\n"
    "ds_read_b64_tr_b16 v[198:199], v235 offset:33792\n"
    "s_add_u32 s76, s87, 53248\n"
    "buffer_load_dwordx4 v[16:19], v238, s[44:47], s76 offen offset:0 sc1\n"
    "v_mfma_f32_32x32x16_bf16 v[64:79], v[216:219], v[44:47], v[64:79]\n"
    "ds_read_b64_tr_b16 v[200:201], v234 offset:34816\n"
    "ds_read_b64_tr_b16 v[202:203], v234 offset:35840\n"
    "ds_read_b64_tr_b16 v[204:205], v235 offset:34816\n"
    "ds_read_b64_tr_b16 v[206:207], v235 offset:35840\n"
    "buffer_load_dwordx4 v[20:23], v238, s[44:47], s76 offen offset:1024 sc1\n"
    "v_mfma_f32_32x32x16_bf16 v[80:95], v[220:223], v[44:47], v[80:95]\n"
    "s_waitcnt vmcnt(9) lgkmcnt(6)\n"
    "ds_read_b64_tr_b16 v[208:209], v234 offset:36864\n"
    "ds_read_b64_tr_b16 v[210:211], v234 offset:37888\n"
    "ds_read_b64_tr_b16 v[212:213], v235 offset:36864\n"
    "ds_read_b64_tr_b16 v[214:215], v235 offset:37888\n"
    "buffer_load_dwordx4 v[24:27], v238, s[44:47], s76 offen offset:2048 sc1\n"
    "v_mfma_f32_32x32x16_bf16 v[64:79], v[192:195], v[48:51], v[64:79]\n"
    "s_waitcnt lgkmcnt(8)\n"
    "ds_read_b64_tr_b16 v[216:217], v234 offset:38912\n"
    "ds_read_b64_tr_b16 v[218:219], v234 offset:39936\n"
    "buffer_load_dwordx4 v[28:31], v238, s[44:47], s76 offen offset:3072 sc1\n"
    "s_lshl_b32 s72, s70, 12\n"
    "v_mfma_f32_32x32x16_bf16 v[80:95], v[196:199], v[48:51], v[80:95]\n"
    "s_waitcnt vmcnt(10) lgkmcnt(8)\n"
    "ds_read_b64_tr_b16 v[220:221], v235 offset:38912\n"
    "ds_read_b64_tr_b16 v[222:223], v235 offset:39936\n"
    "s_xor_b32 s73, s72, 0x2000\n"
    "v_mfma_f32_32x32x16_bf16 v[64:79], v[200:203], v[52:55], v[64:79]\n"
    "s_waitcnt lgkmcnt(8)\n"
    "v_add_u32 v248, s72, v238\n"
    "v_mfma_f32_32x32x16_bf16 v[80:95], v[204:207], v[52:55], v[80:95]\n"
    "s_waitcnt vmcnt(9) lgkmcnt(6)\n"
    "v_add_u32 v249, s73, v238\n"
    "v_mfma_f32_32x32x16_bf16 v[64:79], v[208:211], v[56:59], v[64:79]\n"
    "s_waitcnt lgkmcnt(0)\n"
    "s_barrier\n"
    "v_mfma_f32_32x32x16_bf16 v[80:95], v[212:215], v[56:59], v[80:95]\n"
    "s_waitcnt vmcnt(8)\n"
    "ds_read_b64_tr_b16 v[192:193], v234 offset:49152\n"
    "ds_read_b64_tr_b16 v[194:195], v234 offset:50176\n"
    "ds_read_b64_tr_b16 v[196:197], v235 offset:49152\n"
    "ds_read_b64_tr_b16 v[198:199], v235 offset:50176\n"
    "s_add_u32 s76, s87, 57344\n"
    "buffer_load_dwordx4 v[32:35], v238, s[44:47], s76 offen offset:0 sc1\n"
    "v_mfma_f32_32x32x16_bf16 v[64:79], v[216:219], v[60:63], v[64:79]\n"
    "ds_read_b64_tr_b16 v[200:201], v234 offset:51200\n"
    "ds_read_b64_tr_b16 v[202:203], v234 offset:52224\n"
    "ds_read_b64_tr_b16 v[204:205], v235 offset:51200\n"
    "ds_read_b64_tr_b16 v[206:207], v235 offset:52224\n"
    "buffer_load_dwordx4 v[36:39], v238, s[44:47], s76 offen offset:1024 sc1\n"
    "v_mfma_f32_32x32x16_bf16 v[80:95], v[220:223], v[60:63], v[80:95]\n"
    "s_waitcnt vmcnt(9) lgkmcnt(6)\n"
    "ds_read_b64_tr_b16 v[208:209], v234 offset:53248\n"
    "ds_read_b64_tr_b16 v[210:211], v234 offset:54272\n"
    "ds_read_b64_tr_b16 v[212:213], v235 offset:53248\n"
    "ds_read_b64_tr_b16 v[214:215], v235 offset:54272\n"
    "buffer_load_dwordx4 v[40:43], v238, s[44:47], s76 offen offset:2048 sc1\n"
    "v_mfma_f32_32x32x16_bf16 v[64:79], v[192:195], v[0:3], v[64:79]\n"
    "s_waitcnt lgkmcnt(8)\n"
    "ds_read_b64_tr_b16 v[216:217], v234 offset:55296\n"
    "ds_read_b64_tr_b16 v[218:219], v234 offset:56320\n"
    "buffer_load_dwordx4 v[44:47], v238, s[44:47], s76 offen offset:3072 sc1\n"
    "v_add_u32 v252, 0x0, v248\n"
    "v_mfma_f32_32x32x16_bf16 v[80:95], v[196:199], v[0:3], v[80:95]\n"
    "s_waitcnt vmcnt(10) lgkmcnt(8)\n"
    "ds_read_b64_tr_b16 v[220:221], v235 offset:55296\n"
    "ds_read_b64_tr_b16 v[222:223], v235 offset:56320\n"
    "v_add_u32 v253, 0x0, v249\n"
    "v_mfma_f32_32x32x16_bf16 v[64:79], v[200:203], v[4:7], v[64:79]\n"
    "s_waitcnt lgkmcnt(8)\n"
    "v_add_u32 v248, 0x10000, v248\n"
    "v_mfma_f32_32x32x16_bf16 v[80:95], v[204:207], v[4:7], v[80:95]\n"
    "s_waitcnt vmcnt(9) lgkmcnt(6)\n"
    "v_add_u32 v249, 0x10000, v249\n"
    "v_mfma_f32_32x32x16_bf16 v[64:79], v[208:211], v[8:11], v[64:79]\n"
    "s_waitcnt lgkmcnt(0)\n"
    "s_barrier\n"
    "v_mfma_f32_32x32x16_bf16 v[80:95], v[212:215], v[8:11], v[80:95]\n"
    "s_waitcnt vmcnt(8)\n"
    "ds_read_b64_tr_b16 v[192:193], v236 offset:0\n"
    "ds_read_b64_tr_b16 v[194:195], v236 offset:1024\n"
    "ds_read_b64_tr_b16 v[196:197], v237 offset:0\n"
    "ds_read_b64_tr_b16 v[198:199], v237 offset:1024\n"
    "s_add_u32 s76, s87, 61440\n"
    "buffer_load_dwordx4 v[48:51], v238, s[44:47], s76 offen offset:0 sc1\n"
    "v_mfma_f32_32x32x16_bf16 v[64:79], v[216:219], v[12:15], v[64:79]\n"
    "ds_read_b64_tr_b16 v[200:201], v236 offset:2048\n"
    "ds_read_b64_tr_b16 v[202:203], v236 offset:3072\n"
    "ds_read_b64_tr_b16 v[204:205], v237 offset:2048\n"
    "ds_read_b64_tr_b16 v[206:207], v237 offset:3072\n"
    "buffer_load_dwordx4 v[52:55], v238, s[44:47], s76 offen offset:1024 sc1\n"
    "v_mfma_f32_32x32x16_bf16 v[80:95], v[220:223], v[12:15], v[80:95]\n"
    "s_waitcnt vmcnt(9) lgkmcnt(6)\n"
    "ds_read_b64_tr_b16 v[208:209], v236 offset:4096\n"
    "ds_read_b64_tr_b16 v[210:211], v236 offset:5120\n"
    "ds_read_b64_tr_b16 v[212:213], v237 offset:4096\n"
    "ds_read_b64_tr_b16 v[214:215], v237 offset:5120\n"
    "buffer_load_dwordx4 v[56:59], v238, s[44:47], s76 offen offset:2048 sc1\n"
    "v_mfma_f32_32x32x16_bf16 v[64:79], v[192:195], v[16:19], v[64:79]\n"
    "s_waitcnt lgkmcnt(8)\n"
    "ds_read_b64_tr_b16 v[216:217], v236 offset:6144\n"
    "ds_read_b64_tr_b16 v[218:219], v236 offset:7168\n"
    "buffer_load_dwordx4 v[60:63], v238, s[44:47], s76 offen offset:3072 sc1\n"
    "v_mfma_f32_32x32x16_bf16 v[80:95], v[196:199], v[16:19], v[80:95]\n"
    "s_waitcnt vmcnt(10) lgkmcnt(8)\n"
    "ds_read_b64_tr_b16 v[220:221], v237 offset:6144\n"
    "ds_read_b64_tr_b16 v[222:223], v237 offset:7168\n"
    "v_mfma_f32_32x32x16_bf16 v[64:79], v[200:203], v[20:23], v[64:79]\n"
    "s_waitcnt lgkmcnt(8)\n"
    "v_mfma_f32_32x32x16_bf16 v[80:95], v[204:207], v[20:23], v[80:95]\n"
    "s_waitcnt vmcnt(9) lgkmcnt(6)\n"
    "v_mfma_f32_32x32x16_bf16 v[64:79], v[208:211], v[24:27], v[64:79]\n"
    "s_waitcnt lgkmcnt(0)\n"
    "s_barrier\n"
    "v_mfma_f32_32x32x16_bf16 v[80:95], v[212:215], v[24:27], v[80:95]\n"
    "s_waitcnt vmcnt(8)\n"
    "ds_read_b64_tr_b16 v[192:193], v232 offset:0\n"
    "ds_read_b64_tr_b16 v[194:195], v232 offset:4096\n"
    "ds_read_b64_tr_b16 v[196:197], v233 offset:0\n"
    "ds_read_b64_tr_b16 v[198:199], v233 offset:4096\n"
    "s_add_u32 s76, s87, 65536\n"
    "buffer_load_dwordx4 v[0:3], v238, s[44:47], s76 offen offset:0 sc1\n"
    "v_mfma_f32_32x32x16_bf16 v[64:79], v[216:219], v[28:31], v[64:79]\n"
    "ds_read_b64_tr_b16 v[200:201], v232 offset:1024\n"
    "ds_read_b64_tr_b16 v[202:203], v232 offset:5120\n"
    "ds_read_b64_tr_b16 v[204:205], v233 offset:1024\n"
    "ds_read_b64_tr_b16 v[206:207], v233 offset:5120\n"
    "buffer_load_dwordx4 v[4:7], v238, s[44:47], s76 offen offset:1024 sc1\n"
    "v_mfma_f32_32x32x16_bf16 v[80:95], v[220:223], v[28:31], v[80:95]\n"
    "s_waitcnt vmcnt(9) lgkmcnt(6)\n"
    "ds_read_b64_tr_b16 v[208:209], v232 offset:2048\n"
    "ds_read_b64_tr_b16 v[210:211], v232 offset:6144\n"
    "ds_read_b64_tr_b16 v[212:213], v233 offset:2048\n"
    "ds_read_b64_tr_b16 v[214:215], v233 offset:6144\n"
    "buffer_load_dwordx4 v[8:11], v238, s[44:47], s76 offen offset:2048 sc1\n"
    "v_mfma_f32_32x32x16_bf16 v[160:175], v[192:195], v[32:35], 0\n"
    "s_waitcnt lgkmcnt(8)\n"
    "ds_read_b64_tr_b16 v[216:217], v232 offset:3072\n"
    "ds_read_b64_tr_b16 v[218:219], v232 offset:7168\n"
    "buffer_load_dwordx4 v[12:15], v238, s[44:47], s76 offen offset:3072 sc1\n"
    "v_mfma_f32_32x32x16_bf16 v[176:191], v[196:199], v[32:35], 0\n"
    "s_waitcnt vmcnt(10) lgkmcnt(8)\n"
    "ds_read_b64_tr_b16 v[220:221], v233 offset:3072\n"
    "ds_read_b64_tr_b16 v[222:223], v233 offset:7168\n"
    "ds_write_b128 v248, v[80:83] offset:0\n"
    "v_mfma_f32_32x32x16_bf16 v[160:175], v[200:203], v[36:39], v[160:175]\n"
    "s_waitcnt lgkmcnt(9)\n"
    "ds_write_b128 v248, v[84:87] offset:1024\n"
    "v_mfma_f32_32x32x16_bf16 v[176:191], v[204:207], v[36:39], v[176:191]\n"
    "s_waitcnt vmcnt(9) lgkmcnt(8)\n"
    "ds_write_b128 v248, v[88:91] offset:2048\n"
    "v_mfma_f32_32x32x16_bf16 v[160:175], v[208:211], v[40:43], v[160:175]\n"
    "ds_write_b128 v248, v[92:95] offset:3072\n"
    "s_waitcnt lgkmcnt(0)\n"
    "s_barrier\n"
    "v_mfma_f32_32x32x16_bf16 v[176:191], v[212:215], v[40:43], v[176:191]\n"
    "s_waitcnt vmcnt(8)\n"
    "ds_read_b128 v[128:131], v249 offset:0\n"
    "ds_read_b128 v[132:135], v249 offset:1024\n"
    "ds_read_b128 v[136:139], v249 offset:2048\n"
    "ds_read_b128 v[140:143], v249 offset:3072\n"
    "ds_read_b64_tr_b16 v[192:193], v232 offset:16384\n"
    "ds_read_b64_tr_b16 v[194:195], v232 offset:20480\n"
    "ds_read_b64_tr_b16 v[196:197], v233 offset:16384\n"
    "ds_read_b64_tr_b16 v[198:199], v233 offset:20480\n"
    "s_add_u32 s76, s87, 69632\n"
    "buffer_load_dwordx4 v[16:19], v238, s[44:47], s76 offen offset:0 sc1\n"
    "s_waitcnt lgkmcnt(7)\n"
    "v_add_f32 v64, v64, v128\n"
    "v_add_f32 v65, v65, v129\n"
    "v_add_f32 v66, v66, v130\n"
    "v_add_f32 v67, v67, v131\n"
    "s_waitcnt lgkmcnt(6)\n"
    "v_add_f32 v68, v68, v132\n"
    "v_mfma_f32_32x32x16_bf16 v[160:175], v[216:219], v[44:47], v[160:175]\n"
    "ds_read_b64_tr_b16 v[200:201], v232 offset:17408\n"
    "ds_read_b64_tr_b16 v[202:203], v232 offset:21504\n"
    "ds_read_b64_tr_b16 v[204:205], v233 offset:17408\n"
    "ds_read_b64_tr_b16 v[206:207], v233 offset:21504\n"
    "buffer_load_dwordx4 v[20:23], v238, s[44:47], s76 offen offset:1024 sc1\n"
    "v_add_f32 v69, v69, v133\n"
    "v_add_f32 v70, v70, v134\n"
    "v_add_f32 v71, v71, v135\n"
    "s_waitcnt lgkmcnt(9)\n"
    "v_add_f32 v72, v72, v136\n"
    "v_add_f32 v73, v73, v137\n"
    "v_mfma_f32_32x32x16_bf16 v[176:191], v[220:223], v[44:47], v[176:191]\n"
    "s_waitcnt vmcnt(9) lgkmcnt(6)\n"
    "ds_read_b64_tr_b16 v[208:209], v232 offset:18432\n"
    "ds_read_b64_tr_b16 v[210:211], v232 offset:22528\n"
    "ds_read_b64_tr_b16 v[212:213], v233 offset:18432\n"
    "ds_read_b64_tr_b16 v[214:215], v233 offset:22528\n"
    "buffer_load_dwordx4 v[24:27], v238, s[44:47], s76 offen offset:2048 sc1\n"
    "v_add_f32 v74, v74, v138\n"
    "v_add_f32 v75, v75, v139\n"
    "v_add_f32 v76, v76, v140\n"
    "v_add_f32 v77, v77, v141\n"
    "v_add_f32 v78, v78, v142\n"
    "v_mfma_f32_32x32x16_bf16 v[160:175], v[192:195], v[48:51], v[160:175]\n"
    "s_waitcnt lgkmcnt(8)\n"
    "ds_read_b64_tr_b16 v[216:217], v232 offset:19456\n"
    "ds_read_b64_tr_b16 v[218:219], v232 offset:23552\n"
    "buffer_load_dwordx4 v[28:31], v238, s[44:47], s76 offen offset:3072 sc1\n"
    "v_add_f32 v79, v79, v143\n"
    "v_mul_f32 v128, 0xbfb8aa3b, v64\n"
    "v_mul_f32 v129, 0xbfb8aa3b, v65\n"
    "v_mul_f32 v130, 0xbfb8aa3b, v66\n"
    "v_mul_f32 v131, 0xbfb8aa3b, v67\n"
    "v_mfma_f32_32x32x16_bf16 v[176:191], v[196:199], v[48:51], v[176:191]\n"
    "s_waitcnt vmcnt(10) lgkmcnt(8)\n"
    "ds_read_b64_tr_b16 v[220:221], v233 offset:19456\n"
    "ds_read_b64_tr_b16 v[222:223], v233 offset:23552\n"
    "v_mul_f32 v132, 0xbfb8aa3b, v68\n"
    "v_mul_f32 v133, 0xbfb8aa3b, v69\n"
    "v_mul_f32 v134, 0xbfb8aa3b, v70\n"
    "v_mul_f32 v135, 0xbfb8aa3b, v71\n"
    "v_mfma_f32_32x32x16_bf16 v[160:175], v[200:203], v[52:55], v[160:175]\n"
    "s_waitcnt lgkmcnt(8)\n"
    "v_mul_f32 v136, 0xbfb8aa3b, v72\n"
    "v_mul_f32 v137, 0xbfb8aa3b, v73\n"
    "v_mul_f32 v138, 0xbfb8aa3b, v74\n"
    "v_mul_f32 v139, 0xbfb8aa3b, v75\n"
    "v_mfma_f32_32x32x16_bf16 v[176:191], v[204:207], v[52:55], v[176:191]\n"
    "s_waitcnt vmcnt(9) lgkmcnt(6)\n"
    "v_mul_f32 v140, 0xbfb8aa3b, v76\n"
    "v_mul_f32 v141, 0xbfb8aa3b, v77\n"
    "v_mul_f32 v142, 0xbfb8aa3b, v78\n"
    "v_mul_f32 v143, 0xbfb8aa3b, v79\n"
    "v_mfma_f32_32x32x16_bf16 v[160:175], v[208:211], v[56:59], v[160:175]\n"
    "s_waitcnt lgkmcnt(0)\n"
    "s_barrier\n"
    "v_mfma_f32_32x32x16_bf16 v[176:191], v[212:215], v[56:59], v[176:191]\n"
    "s_waitcnt vmcnt(8)\n"
    "ds_read_b64_tr_b16 v[192:193], v232 offset:32768\n"
    "ds_read_b64_tr_b16 v[194:195], v232 offset:36864\n"
    "ds_read_b64_tr_b16 v[196:197], v233 offset:32768\n"
    "ds_read_b64_tr_b16 v[198:199], v233 offset:36864\n"
    "v_exp_f32 v128, v128\n"
    "v_exp_f32 v129, v129\n"
    "v_exp_f32 v130, v130\n"
    "v_exp_f32 v131, v131\n"
    "v_exp_f32 v132, v132\n"
    "v_mfma_f32_32x32x16_bf16 v[160:175], v[216:219], v[60:63], v[160:175]\n"
    "ds_read_b64_tr_b16 v[200:201], v232 offset:33792\n"
    "ds_read_b64_tr_b16 v[202:203], v232 offset:37888\n"
    "ds_read_b64_tr_b16 v[204:205], v233 offset:33792\n"
    "ds_read_b64_tr_b16 v[206:207], v233 offset:37888\n"
    "v_exp_f32 v133, v133\n"
    "v_exp_f32 v134, v134\n"
    "v_exp_f32 v135, v135\n"
    "v_exp_f32 v136, v136\n"
    "v_exp_f32 v137, v137\n"
    "v_mfma_f32_32x32x16_bf16 v[176:191], v[220:223], v[60:63], v[176:191]\n"
    "s_waitcnt vmcnt(7) lgkmcnt(6)\n"
    "ds_read_b64_tr_b16 v[208:209], v232 offset:34816\n"
    "ds_read_b64_tr_b16 v[210:211], v232 offset:38912\n"
    "ds_read_b64_tr_b16 v[212:213], v233 offset:34816\n"
    "ds_read_b64_tr_b16 v[214:215], v233 offset:38912\n"
    "v_exp_f32 v138, v138\n"
    "v_exp_f32 v139, v139\n"
    "v_exp_f32 v140, v140\n"
    "v_exp_f32 v141, v141\n"
    "v_exp_f32 v142, v142\n"
    "v_mfma_f32_32x32x16_bf16 v[160:175], v[192:195], v[0:3], v[160:175]\n"
    "s_waitcnt lgkmcnt(8)\n"
    "ds_read_b64_tr_b16 v[216:217], v232 offset:35840\n"
    "ds_read_b64_tr_b16 v[218:219], v232 offset:39936\n"
    "v_exp_f32 v143, v143\n"
    "v_add_f32 v128, 1.0, v128\n"
    "v_add_f32 v129, 1.0, v129\n"
    "v_add_f32 v130, 1.0, v130\n"
    "v_add_f32 v131, 1.0, v131\n"
    "v_mfma_f32_32x32x16_bf16 v[176:191], v[196:199], v[0:3], v[176:191]\n"
    "s_waitcnt vmcnt(6) lgkmcnt(8)\n"
    "ds_read_b64_tr_b16 v[220:221], v233 offset:35840\n"
    "ds_read_b64_tr_b16 v[222:223], v233 offset:39936\n"
    "v_add_f32 v132, 1.0, v132\n"
    "v_add_f32 v133, 1.0, v133\n"
    "v_add_f32 v134, 1.0, v134\n"
    "v_add_f32 v135, 1.0, v135\n"
    "v_mfma_f32_32x32x16_bf16 v[160:175], v[200:203], v[4:7], v[160:175]\n"
    "s_waitcnt lgkmcnt(8)\n"
    "v_add_f32 v136, 1.0, v136\n"
    "v_add_f32 v137, 1.0, v137\n"
    "v_add_f32 v138, 1.0, v138\n"
    "v_add_f32 v139, 1.0, v139\n"
    "v_mfma_f32_32x32x16_bf16 v[176:191], v[204:207], v[4:7], v[176:191]\n"
    "s_waitcnt vmcnt(5) lgkmcnt(6)\n"
    "v_add_f32 v140, 1.0, v140\n"
    "v_add_f32 v141, 1.0, v141\n"
    "v_add_f32 v142, 1.0, v142\n"
    "v_add_f32 v143, 1.0, v143\n"
    "v_mfma_f32_32x32x16_bf16 v[160:175], v[208:211], v[8:11], v[160:175]\n"
    "s_waitcnt lgkmcnt(0)\n"
    "s_barrier\n"
    "v_mfma_f32_32x32x16_bf16 v[176:191], v[212:215], v[8:11], v[176:191]\n"
    "s_waitcnt vmcnt(4)\n"
    "ds_read_b64_tr_b16 v[192:193], v233 offset:49152\n"
    "ds_read_b64_tr_b16 v[194:195], v233 offset:53248\n"
    "ds_read_b64_tr_b16 v[196:197], v233 offset:50176\n"
    "ds_read_b64_tr_b16 v[198:199], v233 offset:54272\n"
    "v_rcp_f32 v128, v128\n"
    "v_rcp_f32 v129, v129\n"
    "v_rcp_f32 v130, v130\n"
    "v_rcp_f32 v131, v131\n"
    "v_rcp_f32 v132, v132\n"
    "v_mfma_f32_32x32x16_bf16 v[160:175], v[216:219], v[12:15], v[160:175]\n"
    "ds_read_b64_tr_b16 v[200:201], v233 offset:51200\n"
    "ds_read_b64_tr_b16 v[202:203], v233 offset:55296\n"
    "ds_read_b64_tr_b16 v[204:205], v233 offset:52224\n"
    "ds_read_b64_tr_b16 v[206:207], v233 offset:56320\n"
    "v_rcp_f32 v133, v133\n"
    "v_rcp_f32 v134, v134\n"
    "v_rcp_f32 v135, v135\n"
    "v_rcp_f32 v136, v136\n"
    "v_rcp_f32 v137, v137\n"
    "v_mfma_f32_32x32x16_bf16 v[176:191], v[220:223], v[12:15], v[176:191]\n"
    "s_waitcnt vmcnt(3) lgkmcnt(6)\n"
    "ds_read_b64_tr_b16 v[208:209], v232 offset:49152\n"
    "ds_read_b64_tr_b16 v[210:211], v232 offset:53248\n"
    "ds_read_b64_tr_b16 v[212:213], v232 offset:50176\n"
    "ds_read_b64_tr_b16 v[214:215], v232 offset:54272\n"
    "v_rcp_f32 v138, v138\n"
    "v_rcp_f32 v139, v139\n"
    "v_rcp_f32 v140, v140\n"
    "v_rcp_f32 v141, v141\n"
    "v_rcp_f32 v142, v142\n"
    "v_mfma_f32_32x32x16_bf16 v[176:191], v[192:195], v[16:19], v[176:191]\n"
    "s_waitcnt vmcnt(2) lgkmcnt(8)\n"
    "ds_read_b64_tr_b16 v[216:217], v232 offset:51200\n"
    "ds_read_b64_tr_b16 v[218:219], v232 offset:55296\n"
    "v_rcp_f32 v143, v143\n"
    "v_mul_f32 v64, v64, v128\n"
    "v_mul_f32 v65, v65, v129\n"
    "v_mul_f32 v66, v66, v130\n"
    "v_mul_f32 v67, v67, v131\n"
    "v_mfma_f32_32x32x16_bf16 v[176:191], v[196:199], v[20:23], v[176:191]\n"
    "s_waitcnt vmcnt(1) lgkmcnt(8)\n"
    "ds_read_b64_tr_b16 v[220:221], v232 offset:52224\n"
    "ds_read_b64_tr_b16 v[222:223], v232 offset:56320\n"
    "v_mul_f32 v68, v68, v132\n"
    "v_mul_f32 v69, v69, v133\n"
    "v_mul_f32 v70, v70, v134\n"
    "v_mul_f32 v71, v71, v135\n"
    "v_mfma_f32_32x32x16_bf16 v[176:191], v[200:203], v[24:27], v[176:191]\n"
    "s_waitcnt vmcnt(0) lgkmcnt(8)\n"
    "v_mul_f32 v72, v72, v136\n"
    "v_mul_f32 v73, v73, v137\n"
    "v_mul_f32 v74, v74, v138\n"
    "v_mul_f32 v75, v75, v139\n"
    "v_mfma_f32_32x32x16_bf16 v[176:191], v[204:207], v[28:31], v[176:191]\n"
    "s_waitcnt lgkmcnt(6)\n"
    "v_mul_f32 v76, v76, v140\n"
    "v_mul_f32 v77, v77, v141\n"
    "v_mul_f32 v78, v78, v142\n"
    "v_mul_f32 v79, v79, v143\n"
    "v_mfma_f32_32x32x16_bf16 v[160:175], v[208:211], v[16:19], v[160:175]\n"
    "s_waitcnt lgkmcnt(4)\n"
    "v_mfma_f32_32x32x16_bf16 v[160:175], v[212:215], v[20:23], v[160:175]\n"
    "s_waitcnt lgkmcnt(2)\n"
    "s_nop 2\n"
    "ds_write_b128 v252, v[176:179] offset:0\n"
    "ds_write_b128 v252, v[180:183] offset:1024\n"
    "v_mfma_f32_32x32x16_bf16 v[160:175], v[216:219], v[24:27], v[160:175]\n"
    "s_waitcnt lgkmcnt(2)\n"
    "ds_write_b128 v252, v[184:187] offset:2048\n"
    "ds_write_b128 v252, v[188:191] offset:3072\n"
    "v_mfma_f32_32x32x16_bf16 v[160:175], v[220:223], v[28:31], v[160:175]\n"
    "s_waitcnt lgkmcnt(0)\n"
    "s_barrier\n"
    "ds_read_b128 v[128:131], v253 offset:0\n"
    "ds_read_b128 v[132:135], v253 offset:1024\n"
    "ds_read_b128 v[136:139], v253 offset:2048\n"
    "ds_read_b128 v[140:143], v253 offset:3072\n"
    "s_nop 5\n"
    "v_fma_f32 v144, v160, v64, v144\n"
    "v_fma_f32 v145, v161, v65, v145\n"
    "v_fma_f32 v146, v162, v66, v146\n"
    "v_fma_f32 v147, v163, v67, v147\n"
    "v_fma_f32 v148, v164, v68, v148\n"
    "v_fma_f32 v149, v165, v69, v149\n"
    "v_fma_f32 v150, v166, v70, v150\n"
    "v_fma_f32 v151, v167, v71, v151\n"
    "v_fma_f32 v152, v168, v72, v152\n"
    "v_fma_f32 v153, v169, v73, v153\n"
    "v_fma_f32 v154, v170, v74, v154\n"
    "v_fma_f32 v155, v171, v75, v155\n"
    "v_fma_f32 v156, v172, v76, v156\n"
    "v_fma_f32 v157, v173, v77, v157\n"
    "v_fma_f32 v158, v174, v78, v158\n"
    "v_fma_f32 v159, v175, v79, v159\n"
    "s_waitcnt lgkmcnt(3)\n"
    "v_fma_f32 v144, v128, v64, v144\n"
    "v_fma_f32 v145, v129, v65, v145\n"
    "v_fma_f32 v146, v130, v66, v146\n"
    "v_fma_f32 v147, v131, v67, v147\n"
    "s_waitcnt lgkmcnt(2)\n"
    "v_fma_f32 v148, v132, v68, v148\n"
    "v_fma_f32 v149, v133, v69, v149\n"
    "v_fma_f32 v150, v134, v70, v150\n"
    "v_fma_f32 v151, v135, v71, v151\n"
    "s_waitcnt lgkmcnt(1)\n"
    "v_fma_f32 v152, v136, v72, v152\n"
    "v_fma_f32 v153, v137, v73, v153\n"
    "v_fma_f32 v154, v138, v74, v154\n"
    "v_fma_f32 v155, v139, v75, v155\n"
    "s_waitcnt lgkmcnt(0)\n"
    "v_fma_f32 v156, v140, v76, v156\n"
    "v_fma_f32 v157, v141, v77, v157\n"
    "v_fma_f32 v158, v142, v78, v158\n"
    "v_fma_f32 v159, v143, v79, v159\n"
    "v_cvt_pk_bf16_f32 v128, v144, v145\n"
    "v_cvt_pk_bf16_f32 v129, v146, v147\n"
    "v_cvt_pk_bf16_f32 v130, v148, v149\n"
    "v_cvt_pk_bf16_f32 v131, v150, v151\n"
    "v_cvt_pk_bf16_f32 v132, v152, v153\n"
    "v_cvt_pk_bf16_f32 v133, v154, v155\n"
    "v_cvt_pk_bf16_f32 v134, v156, v157\n"
    "v_cvt_pk_bf16_f32 v135, v158, v159\n"
    "v_permlane32_swap_b32 v128, v130\n"
    "v_permlane32_swap_b32 v129, v131\n"
    "v_permlane32_swap_b32 v132, v134\n"
    "v_permlane32_swap_b32 v133, v135\n"
    "buffer_store_dwordx4 v[128:131], v96, s[52:55], s84 offen\n"
    "buffer_store_dwordx4 v[132:135], v96, s[52:55], s84 offen offset:32\n"
    "s_endpgm\n"
    "LBL_C_att:\n"
    "s_setprio 1\n"
    "v_lshrrev_b32 v224, 5, v255\n"
    "v_bfe_u32 v225, v255, 2, 2\n"
    "v_bfe_u32 v226, v255, 4, 1\n"
    "v_bfe_u32 v227, v255, 1, 1\n"
    "v_lshl_add_u32 v226, v226, 1, v227\n"
    "v_and_b32 v227, 1, v255\n"
    "v_lshlrev_b32 v227, 3, v227\n"
    "v_bfe_u32 v228, v255, 3, 1\n"
    "v_lshlrev_b32 v228, 2, v228\n"
    "v_lshlrev_b32 v229, 1, v224\n"
    "v_lshl_add_u32 v230, v224, 3, v225\n"
    "v_xor_b32 v231, v226, v229\n"
    "v_lshlrev_b32 v233, 6, v230\n"
    "v_lshl_add_u32 v233, v231, 4, v233\n"
    "v_add_u32 v229, v233, v227\n"
    "v_xor_b32 v231, v231, v228\n"
    "v_lshlrev_b32 v232, 7, v230\n"
    "v_lshl_add_u32 v232, v231, 4, v232\n"
    "v_add_u32 v232, v232, v227\n"
    "v_xor_b32 v226, v226, v228\n"
    "v_lshl_add_u32 v230, v224, 2, v225\n"
    "v_lshlrev_b32 v228, 7, v230\n"
    "v_lshl_add_u32 v228, v226, 4, v228\n"
    "v_add_u32 v228, v228, v227\n"
    "s_lshr_b32 s72, s70, 1\n"
    "s_mul_i32 s72, s72, 8256\n"
    "v_xor_b32 v228, s72, v228\n"
    "v_add_u32 v234, 0x10000, v232\n"
    "v_add_u32 v236, 0x20000, v232\n"
    "v_xor_b32 v233, 64, v232\n"
    "v_xor_b32 v235, 64, v234\n"
    "v_xor_b32 v237, 64, v236\n"
    "v_and_b32 v225, 31, v255\n"
    "v_bfe_u32 v226, v255, 1, 3\n"
    "v_xor_b32 v226, v226, v224\n"
    "v_lshlrev_b32 v160, 7, v225\n"
    "v_lshl_add_u32 v160, v226, 4, v160\n"
    "s_lshl_b32 s72, s70, 12\n"
    "v_add_u32 v160, s72, v160\n"
    "v_xor_b32 v161, 32, v160\n"
    "v_xor_b32 v162, 64, v160\n"
    "v_xor_b32 v163, 96, v160\n"
    "v_add_u32 v164, 0x10000, v160\n"
    "v_add_u32 v165, 0x10000, v161\n"
    "v_add_u32 v166, 0x10000, v162\n"
    "v_add_u32 v167, 0x10000, v163\n"
    "v_add_u32 v168, 0x20000, v160\n"
    "v_add_u32 v169, 0x20000, v161\n"
    "v_add_u32 v170, 0x20000, v162\n"
    "v_add_u32 v171, 0x20000, v163\n"
    "v_lshlrev_b32 v238, 4, v255\n"
    "v_lshlrev_b32 v248, 3, v225\n"
    "v_add_u32 v248, 0x27800, v248\n"
    "v_mov_b32 v240, 0\n"
    "v_mov_b32 v241, 0\n"
    "v_mov_b32 v242, 0\n"
    "v_mov_b32 v243, 0\n"
    "v_mov_b32 v244, 0\n"
    "v_mov_b32 v245, 0\n"
    "v_mov_b32 v246, 0\n"
    "v_mov_b32 v247, 0\n"
    "v_mov_b32 v252, 0\n"
    "v_mov_b32 v253, 0\n"
    "v_mov_b32 v172, 0x3f803f80\n"
    "v_add_u32 v250, -1, v224\n"
    "v_mul_u32_u24 v254, 144, v225\n"
    "v_lshl_add_u32 v254, v224, 3, v254\n"
    "s_and_b32 s72, s70, 1\n"
    "s_mul_i32 s72, s72, 10368\n"
    "s_lshr_b32 s73, s70, 1\n"
    "s_mul_i32 s73, s73, 4608\n"
    "s_add_u32 s72, s72, s73\n"
    "s_add_u32 s72, s72, 142208\n"
    "v_add_u32 v254, s72, v254\n"
    "s_waitcnt lgkmcnt(0)\n"
    "s_mov_b32 s44, s24\n"
    "s_and_b32 s45, s25, 0xffff\n"
    "s_mov_b32 s46, 0x900000\n"
    "s_mov_b32 s47, 0x20000\n"
    "s_mov_b32 s52, s22\n"
    "s_and_b32 s53, s23, 0xffff\n"
    "s_mov_b32 s54, 0x200000\n"
    "s_mov_b32 s55, 0x20000\n"
    "s_mov_b32 s60, s20\n"
    "s_and_b32 s61, s21, 0xffff\n"
    "s_mov_b32 s62, 0x404\n"
    "s_mov_b32 s63, 0x20000\n"
    "v_and_b32 v225, 31, v255\n"
    "v_lshlrev_b32 v225, 1, v225\n"
    "s_lshr_b32 s72, s70, 1\n"
    "s_lshl_b32 s72, s72, 6\n"
    "s_lshl_b32 s73, s69, 7\n"
    "s_add_u32 s72, s72, s73\n"
    "buffer_load_ushort v230, v225, s[60:63], s72 offen\n"
    "s_lshl_b32 s72, s68, 1\n"
    "s_and_b32 s73, s70, 1\n"
    "s_add_u32 s72, s72, s73\n"
    "s_mul_i32 s72, s72, 147456\n"
    "s_lshr_b32 s73, s70, 1\n"
    "s_mul_i32 s73, s73, 73728\n"
    "s_add_u32 s87, s72, s73\n"
    "s_barrier\n"
    "ds_read_b128 v[0:3], v160 offset:0\n"
    "ds_read_b128 v[4:7], v161 offset:0\n"
    "ds_read_b128 v[8:11], v162 offset:0\n"
    "ds_read_b128 v[12:15], v163 offset:0\n"
    "ds_read_b64_tr_b16 v[192:193], v232 offset:16384\n"
    "ds_read_b64_tr_b16 v[194:195], v232 offset:16896\n"
    "ds_read_b64_tr_b16 v[196:197], v233 offset:16384\n"
    "ds_read_b64_tr_b16 v[198:199], v233 offset:16896\n"
    "ds_read_b64_tr_b16 v[200:201], v232 offset:18432\n"
    "ds_read_b64_tr_b16 v[202:203], v232 offset:18944\n"
    "ds_read_b64_tr_b16 v[204:205], v233 offset:18432\n"
    "ds_read_b64_tr_b16 v[206:207], v233 offset:18944\n"
    "ds_read_b64_tr_b16 v[208:209], v232 offset:20480\n"
    "ds_read_b64_tr_b16 v[210:211], v232 offset:20992\n"
    "ds_read_b64_tr_b16 v[212:213], v233 offset:20480\n"
    "ds_read_b64_tr_b16 v[214:215], v233 offset:20992\n"
    "s_waitcnt lgkmcnt(10)\n"
    "v_mfma_f32_32x32x16_bf16 v[128:143], v[192:195], v[0:3], 0\n"
    "s_waitcnt lgkmcnt(8)\n"
    "ds_read_b64_tr_b16 v[216:217], v232 offset:22528\n"
    "ds_read_b64_tr_b16 v[218:219], v232 offset:23040\n"
    "v_mfma_f32_32x32x16_bf16 v[144:159], v[196:199], v[0:3], 0\n"
    "s_waitcnt lgkmcnt(8)\n"
    "ds_read_b64_tr_b16 v[220:221], v233 offset:22528\n"
    "ds_read_b64_tr_b16 v[222:223], v233 offset:23040\n"
    "v_mfma_f32_32x32x16_bf16 v[128:143], v[200:203], v[4:7], v[128:143]\n"
    "s_waitcnt lgkmcnt(8)\n"
    "v_mfma_f32_32x32x16_bf16 v[144:159], v[204:207], v[4:7], v[144:159]\n"
    "s_waitcnt lgkmcnt(6)\n"
    "v_dot2c_f32_bf16 v252, v0, v172\n"
    "v_dot2c_f32_bf16 v253, v0, v0\n"
    "v_dot2c_f32_bf16 v252, v1, v172\n"
    "v_dot2c_f32_bf16 v253, v1, v1\n"
    "v_dot2c_f32_bf16 v252, v2, v172\n"
    "v_dot2c_f32_bf16 v253, v2, v2\n"
    "v_dot2c_f32_bf16 v252, v3, v172\n"
    "v_dot2c_f32_bf16 v253, v3, v3\n"
    "v_dot2c_f32_bf16 v252, v4, v172\n"
    "v_dot2c_f32_bf16 v253, v4, v4\n"
    "v_dot2c_f32_bf16 v252, v5, v172\n"
    "v_dot2c_f32_bf16 v253, v5, v5\n"
    "v_dot2c_f32_bf16 v252, v6, v172\n"
    "v_dot2c_f32_bf16 v253, v6, v6\n"
    "v_dot2c_f32_bf16 v252, v7, v172\n"
    "v_dot2c_f32_bf16 v253, v7, v7\n"
    "v_dot2c_f32_bf16 v252, v8, v172\n"
    "v_dot2c_f32_bf16 v253, v8, v8\n"
    "v_dot2c_f32_bf16 v252, v9, v172\n"
    "v_dot2c_f32_bf16 v253, v9, v9\n"
    "v_dot2c_f32_bf16 v252, v10, v172\n"
    "v_dot2c_f32_bf16 v253, v10, v10\n"
    "v_dot2c_f32_bf16 v252, v11, v172\n"
    "v_dot2c_f32_bf16 v253, v11, v11\n"
    "v_dot2c_f32_bf16 v252, v12, v172\n"
    "v_dot2c_f32_bf16 v253, v12, v12\n"
    "v_dot2c_f32_bf16 v252, v13, v172\n"
    "v_dot2c_f32_bf16 v253, v13, v13\n"
    "v_dot2c_f32_bf16 v252, v14, v172\n"
    "v_dot2c_f32_bf16 v253, v14, v14\n"
    "v_dot2c_f32_bf16 v252, v15, v172\n"
    "v_dot2c_f32_bf16 v253, v15, v15\n"
    "v_mfma_f32_32x32x16_bf16 v[128:143], v[208:211], v[8:11], v[128:143]\n"
    "s_waitcnt lgkmcnt(0)\n"
    "s_barrier\n"
    "v_mfma_f32_32x32x16_bf16 v[144:159], v[212:215], v[8:11], v[144:159]\n"
    "ds_read_b128 v[16:19], v160 offset:32768\n"
    "ds_read_b128 v[20:23], v161 offset:32768\n"
    "ds_read_b128 v[24:27], v162 offset:32768\n"
    "ds_read_b128 v[28:31], v163 offset:32768\n"
    "ds_read_b64_tr_b16 v[192:193], v232 offset:24576\n"
    "ds_read_b64_tr_b16 v[194:195], v232 offset:25088\n"
    "ds_read_b64_tr_b16 v[196:197], v233 offset:24576\n"
    "ds_read_b64_tr_b16 v[198:199], v233 offset:25088\n"
    "v_mfma_f32_32x32x16_bf16 v[128:143], v[216:219], v[12:15], v[128:143]\n"
    "ds_read_b64_tr_b16 v[200:201], v232 offset:26624\n"
    "ds_read_b64_tr_b16 v[202:203], v232 offset:27136\n"
    "ds_read_b64_tr_b16 v[204:205], v233 offset:26624\n"
    "ds_read_b64_tr_b16 v[206:207], v233 offset:27136\n"
    "v_mfma_f32_32x32x16_bf16 v[144:159], v[220:223], v[12:15], v[144:159]\n"
    "s_waitcnt lgkmcnt(6)\n"
    "ds_read_b64_tr_b16 v[208:209], v232 offset:28672\n"
    "ds_read_b64_tr_b16 v[210:211], v232 offset:29184\n"
    "ds_read_b64_tr_b16 v[212:213], v233 offset:28672\n"
    "ds_read_b64_tr_b16 v[214:215], v233 offset:29184\n"
    "v_mfma_f32_32x32x16_bf16 v[128:143], v[192:195], v[16:19], v[128:143]\n"
    "s_waitcnt lgkmcnt(8)\n"
    "ds_read_b64_tr_b16 v[216:217], v232 offset:30720\n"
    "ds_read_b64_tr_b16 v[218:219], v232 offset:31232\n"
    "v_mfma_f32_32x32x16_bf16 v[144:159], v[196:199], v[16:19], v[144:159]\n"
    "s_waitcnt lgkmcnt(8)\n"
    "ds_read_b64_tr_b16 v[220:221], v233 offset:30720\n"
    "ds_read_b64_tr_b16 v[222:223], v233 offset:31232\n"
    "v_mfma_f32_32x32x16_bf16 v[128:143], v[200:203], v[20:23], v[128:143]\n"
    "s_waitcnt lgkmcnt(8)\n"
    "v_mfma_f32_32x32x16_bf16 v[144:159], v[204:207], v[20:23], v[144:159]\n"
    "s_waitcnt lgkmcnt(6)\n"
    "v_dot2c_f32_bf16 v252, v16, v172\n"
    "v_dot2c_f32_bf16 v253, v16, v16\n"
    "v_dot2c_f32_bf16 v252, v17, v172\n"
    "v_dot2c_f32_bf16 v253, v17, v17\n"
    "v_dot2c_f32_bf16 v252, v18, v172\n"
    "v_dot2c_f32_bf16 v253, v18, v18\n"
    "v_dot2c_f32_bf16 v252, v19, v172\n"
    "v_dot2c_f32_bf16 v253, v19, v19\n"
    "v_dot2c_f32_bf16 v252, v20, v172\n"
    "v_dot2c_f32_bf16 v253, v20, v20\n"
    "v_dot2c_f32_bf16 v252, v21, v172\n"
    "v_dot2c_f32_bf16 v253, v21, v21\n"
    "v_dot2c_f32_bf16 v252, v22, v172\n"
    "v_dot2c_f32_bf16 v253, v22, v22\n"
    "v_dot2c_f32_bf16 v252, v23, v172\n"
    "v_dot2c_f32_bf16 v253, v23, v23\n"
    "v_dot2c_f32_bf16 v252, v24, v172\n"
    "v_dot2c_f32_bf16 v253, v24, v24\n"
    "v_dot2c_f32_bf16 v252, v25, v172\n"
    "v_dot2c_f32_bf16 v253, v25, v25\n"
    "v_dot2c_f32_bf16 v252, v26, v172\n"
    "v_dot2c_f32_bf16 v253, v26, v26\n"
    "v_dot2c_f32_bf16 v252, v27, v172\n"
    "v_dot2c_f32_bf16 v253, v27, v27\n"
    "v_dot2c_f32_bf16 v252, v28, v172\n"
    "v_dot2c_f32_bf16 v253, v28, v28\n"
    "v_dot2c_f32_bf16 v252, v29, v172\n"
    "v_dot2c_f32_bf16 v253, v29, v29\n"
    "v_dot2c_f32_bf16 v252, v30, v172\n"
    "v_dot2c_f32_bf16 v253, v30, v30\n"
    "v_dot2c_f32_bf16 v252, v31, v172\n"
    "v_dot2c_f32_bf16 v253, v31, v31\n"
    "v_mfma_f32_32x32x16_bf16 v[128:143], v[208:211], v[24:27], v[128:143]\n"
    "s_waitcnt lgkmcnt(0)\n"
    "s_barrier\n"
    "v_mfma_f32_32x32x16_bf16 v[144:159], v[212:215], v[24:27], v[144:159]\n"
    "ds_read_b128 v[32:35], v160 offset:49152\n"
    "ds_read_b128 v[36:39], v161 offset:49152\n"
    "ds_read_b128 v[40:43], v162 offset:49152\n"
    "ds_read_b128 v[44:47], v163 offset:49152\n"
    "ds_read_b64_tr_b16 v[192:193], v234 offset:0\n"
    "ds_read_b64_tr_b16 v[194:195], v234 offset:512\n"
    "ds_read_b64_tr_b16 v[196:197], v235 offset:0\n"
    "ds_read_b64_tr_b16 v[198:199], v235 offset:512\n"
    "v_mfma_f32_32x32x16_bf16 v[128:143], v[216:219], v[28:31], v[128:143]\n"
    "ds_read_b64_tr_b16 v[200:201], v234 offset:2048\n"
    "ds_read_b64_tr_b16 v[202:203], v234 offset:2560\n"
    "ds_read_b64_tr_b16 v[204:205], v235 offset:2048\n"
    "ds_read_b64_tr_b16 v[206:207], v235 offset:2560\n"
    "v_mfma_f32_32x32x16_bf16 v[144:159], v[220:223], v[28:31], v[144:159]\n"
    "s_waitcnt lgkmcnt(6)\n"
    "ds_read_b64_tr_b16 v[208:209], v234 offset:4096\n"
    "ds_read_b64_tr_b16 v[210:211], v234 offset:4608\n"
    "ds_read_b64_tr_b16 v[212:213], v235 offset:4096\n"
    "ds_read_b64_tr_b16 v[214:215], v235 offset:4608\n"
    "v_mfma_f32_32x32x16_bf16 v[128:143], v[192:195], v[32:35], v[128:143]\n"
    "s_waitcnt lgkmcnt(8)\n"
    "ds_read_b64_tr_b16 v[216:217], v234 offset:6144\n"
    "ds_read_b64_tr_b16 v[218:219], v234 offset:6656\n"
    "v_mfma_f32_32x32x16_bf16 v[144:159], v[196:199], v[32:35], v[144:159]\n"
    "s_waitcnt lgkmcnt(8)\n"
    "ds_read_b64_tr_b16 v[220:221], v235 offset:6144\n"
    "ds_read_b64_tr_b16 v[222:223], v235 offset:6656\n"
    "v_mfma_f32_32x32x16_bf16 v[128:143], v[200:203], v[36:39], v[128:143]\n"
    "s_waitcnt lgkmcnt(8)\n"
    "v_mfma_f32_32x32x16_bf16 v[144:159], v[204:207], v[36:39], v[144:159]\n"
    "s_waitcnt lgkmcnt(6)\n"
    "v_dot2c_f32_bf16 v252, v32, v172\n"
    "v_dot2c_f32_bf16 v253, v32, v32\n"
    "v_dot2c_f32_bf16 v252, v33, v172\n"
    "v_dot2c_f32_bf16 v253, v33, v33\n"
    "v_dot2c_f32_bf16 v252, v34, v172\n"
    "v_dot2c_f32_bf16 v253, v34, v34\n"
    "v_dot2c_f32_bf16 v252, v35, v172\n"
    "v_dot2c_f32_bf16 v253, v35, v35\n"
    "v_dot2c_f32_bf16 v252, v36, v172\n"
    "v_dot2c_f32_bf16 v253, v36, v36\n"
    "v_dot2c_f32_bf16 v252, v37, v172\n"
    "v_dot2c_f32_bf16 v253, v37, v37\n"
    "v_dot2c_f32_bf16 v252, v38, v172\n"
    "v_dot2c_f32_bf16 v253, v38, v38\n"
    "v_dot2c_f32_bf16 v252, v39, v172\n"
    "v_dot2c_f32_bf16 v253, v39, v39\n"
    "v_dot2c_f32_bf16 v252, v40, v172\n"
    "v_dot2c_f32_bf16 v253, v40, v40\n"
    "v_dot2c_f32_bf16 v252, v41, v172\n"
    "v_dot2c_f32_bf16 v253, v41, v41\n"
    "v_dot2c_f32_bf16 v252, v42, v172\n"
    "v_dot2c_f32_bf16 v253, v42, v42\n"
    "v_dot2c_f32_bf16 v252, v43, v172\n"
    "v_dot2c_f32_bf16 v253, v43, v43\n"
    "v_dot2c_f32_bf16 v252, v44, v172\n"
    "v_dot2c_f32_bf16 v253, v44, v44\n"
    "v_dot2c_f32_bf16 v252, v45, v172\n"
    "v_dot2c_f32_bf16 v253, v45, v45\n"
    "v_dot2c_f32_bf16 v252, v46, v172\n"
    "v_dot2c_f32_bf16 v253, v46, v46\n"
    "v_dot2c_f32_bf16 v252, v47, v172\n"
    "v_dot2c_f32_bf16 v253, v47, v47\n"
    "v_mfma_f32_32x32x16_bf16 v[128:143], v[208:211], v[40:43], v[128:143]\n"
    "s_waitcnt lgkmcnt(0)\n"
    "s_barrier\n"
    "v_mfma_f32_32x32x16_bf16 v[144:159], v[212:215], v[40:43], v[144:159]\n"
    "ds_read_b128 v[48:51], v164 offset:16384\n"
    "ds_read_b128 v[52:55], v165 offset:16384\n"
    "ds_read_b128 v[56:59], v166 offset:16384\n"
    "ds_read_b128 v[60:63], v167 offset:16384\n"
    "ds_read_b64_tr_b16 v[192:193], v234 offset:8192\n"
    "ds_read_b64_tr_b16 v[194:195], v234 offset:8704\n"
    "ds_read_b64_tr_b16 v[196:197], v235 offset:8192\n"
    "ds_read_b64_tr_b16 v[198:199], v235 offset:8704\n"
    "v_mfma_f32_32x32x16_bf16 v[128:143], v[216:219], v[44:47], v[128:143]\n"
    "ds_read_b64_tr_b16 v[200:201], v234 offset:10240\n"
    "ds_read_b64_tr_b16 v[202:203], v234 offset:10752\n"
    "ds_read_b64_tr_b16 v[204:205], v235 offset:10240\n"
    "ds_read_b64_tr_b16 v[206:207], v235 offset:10752\n"
    "v_mfma_f32_32x32x16_bf16 v[144:159], v[220:223], v[44:47], v[144:159]\n"
    "s_waitcnt lgkmcnt(6)\n"
    "ds_read_b64_tr_b16 v[208:209], v234 offset:12288\n"
    "ds_read_b64_tr_b16 v[210:211], v234 offset:12800\n"
    "ds_read_b64_tr_b16 v[212:213], v235 offset:12288\n"
    "ds_read_b64_tr_b16 v[214:215], v235 offset:12800\n"
    "v_mfma_f32_32x32x16_bf16 v[128:143], v[192:195], v[48:51], v[128:143]\n"
    "s_waitcnt lgkmcnt(8)\n"
    "ds_read_b64_tr_b16 v[216:217], v234 offset:14336\n"
    "ds_read_b64_tr_b16 v[218:219], v234 offset:14848\n"
    "v_mfma_f32_32x32x16_bf16 v[144:159], v[196:199], v[48:51], v[144:159]\n"
    "s_waitcnt lgkmcnt(8)\n"
    "ds_read_b64_tr_b16 v[220:221], v235 offset:14336\n"
    "ds_read_b64_tr_b16 v[222:223], v235 offset:14848\n"
    "v_mfma_f32_32x32x16_bf16 v[128:143], v[200:203], v[52:55], v[128:143]\n"
    "s_waitcnt lgkmcnt(8)\n"
    "v_mfma_f32_32x32x16_bf16 v[144:159], v[204:207], v[52:55], v[144:159]\n"
    "s_waitcnt lgkmcnt(6)\n"
    "v_dot2c_f32_bf16 v252, v48, v172\n"
    "v_dot2c_f32_bf16 v253, v48, v48\n"
    "v_dot2c_f32_bf16 v252, v49, v172\n"
    "v_dot2c_f32_bf16 v253, v49, v49\n"
    "v_dot2c_f32_bf16 v252, v50, v172\n"
    "v_dot2c_f32_bf16 v253, v50, v50\n"
    "v_dot2c_f32_bf16 v252, v51, v172\n"
    "v_dot2c_f32_bf16 v253, v51, v51\n"
    "v_dot2c_f32_bf16 v252, v52, v172\n"
    "v_dot2c_f32_bf16 v253, v52, v52\n"
    "v_dot2c_f32_bf16 v252, v53, v172\n"
    "v_dot2c_f32_bf16 v253, v53, v53\n"
    "v_dot2c_f32_bf16 v252, v54, v172\n"
    "v_dot2c_f32_bf16 v253, v54, v54\n"
    "v_dot2c_f32_bf16 v252, v55, v172\n"
    "v_dot2c_f32_bf16 v253, v55, v55\n"
    "v_dot2c_f32_bf16 v252, v56, v172\n"
    "v_dot2c_f32_bf16 v253, v56, v56\n"
    "v_dot2c_f32_bf16 v252, v57, v172\n"
    "v_dot2c_f32_bf16 v253, v57, v57\n"
    "v_dot2c_f32_bf16 v252, v58, v172\n"
    "v_dot2c_f32_bf16 v253, v58, v58\n"
    "v_dot2c_f32_bf16 v252, v59, v172\n"
    "v_dot2c_f32_bf16 v253, v59, v59\n"
    "v_dot2c_f32_bf16 v252, v60, v172\n"
    "v_dot2c_f32_bf16 v253, v60, v60\n"
    "v_dot2c_f32_bf16 v252, v61, v172\n"
    "v_dot2c_f32_bf16 v253, v61, v61\n"
    "v_dot2c_f32_bf16 v252, v62, v172\n"
    "v_dot2c_f32_bf16 v253, v62, v62\n"
    "v_dot2c_f32_bf16 v252, v63, v172\n"
    "v_dot2c_f32_bf16 v253, v63, v63\n"
    "v_mfma_f32_32x32x16_bf16 v[128:143], v[208:211], v[56:59], v[128:143]\n"
    "s_waitcnt lgkmcnt(0)\n"
    "s_barrier\n"
    "v_mfma_f32_32x32x16_bf16 v[144:159], v[212:215], v[56:59], v[144:159]\n"
    "ds_read_b128 v[64:67], v164 offset:32768\n"
    "ds_read_b128 v[68:71], v165 offset:32768\n"
    "ds_read_b128 v[72:75], v166 offset:32768\n"
    "ds_read_b128 v[76:79], v167 offset:32768\n"
    "ds_read_b64_tr_b16 v[192:193], v234 offset:49152\n"
    "ds_read_b64_tr_b16 v[194:195], v234 offset:49664\n"
    "ds_read_b64_tr_b16 v[196:197], v235 offset:49152\n"
    "ds_read_b64_tr_b16 v[198:199], v235 offset:49664\n"
    "v_mfma_f32_32x32x16_bf16 v[128:143], v[216:219], v[60:63], v[128:143]\n"
    "ds_read_b64_tr_b16 v[200:201], v234 offset:51200\n"
    "ds_read_b64_tr_b16 v[202:203], v234 offset:51712\n"
    "ds_read_b64_tr_b16 v[204:205], v235 offset:51200\n"
    "ds_read_b64_tr_b16 v[206:207], v235 offset:51712\n"
    "v_mfma_f32_32x32x16_bf16 v[144:159], v[220:223], v[60:63], v[144:159]\n"
    "s_waitcnt lgkmcnt(6)\n"
    "ds_read_b64_tr_b16 v[208:209], v234 offset:53248\n"
    "ds_read_b64_tr_b16 v[210:211], v234 offset:53760\n"
    "ds_read_b64_tr_b16 v[212:213], v235 offset:53248\n"
    "ds_read_b64_tr_b16 v[214:215], v235 offset:53760\n"
    "v_mfma_f32_32x32x16_bf16 v[128:143], v[192:195], v[64:67], v[128:143]\n"
    "s_waitcnt lgkmcnt(8)\n"
    "ds_read_b64_tr_b16 v[216:217], v234 offset:55296\n"
    "ds_read_b64_tr_b16 v[218:219], v234 offset:55808\n"
    "v_mfma_f32_32x32x16_bf16 v[144:159], v[196:199], v[64:67], v[144:159]\n"
    "s_waitcnt lgkmcnt(8)\n"
    "ds_read_b64_tr_b16 v[220:221], v235 offset:55296\n"
    "ds_read_b64_tr_b16 v[222:223], v235 offset:55808\n"
    "v_mfma_f32_32x32x16_bf16 v[128:143], v[200:203], v[68:71], v[128:143]\n"
    "s_waitcnt lgkmcnt(8)\n"
    "v_mfma_f32_32x32x16_bf16 v[144:159], v[204:207], v[68:71], v[144:159]\n"
    "s_waitcnt lgkmcnt(6)\n"
    "v_dot2c_f32_bf16 v252, v64, v172\n"
    "v_dot2c_f32_bf16 v253, v64, v64\n"
    "v_dot2c_f32_bf16 v252, v65, v172\n"
    "v_dot2c_f32_bf16 v253, v65, v65\n"
    "v_dot2c_f32_bf16 v252, v66, v172\n"
    "v_dot2c_f32_bf16 v253, v66, v66\n"
    "v_dot2c_f32_bf16 v252, v67, v172\n"
    "v_dot2c_f32_bf16 v253, v67, v67\n"
    "v_dot2c_f32_bf16 v252, v68, v172\n"
    "v_dot2c_f32_bf16 v253, v68, v68\n"
    "v_dot2c_f32_bf16 v252, v69, v172\n"
    "v_dot2c_f32_bf16 v253, v69, v69\n"
    "v_dot2c_f32_bf16 v252, v70, v172\n"
    "v_dot2c_f32_bf16 v253, v70, v70\n"
    "v_dot2c_f32_bf16 v252, v71, v172\n"
    "v_dot2c_f32_bf16 v253, v71, v71\n"
    "v_dot2c_f32_bf16 v252, v72, v172\n"
    "v_dot2c_f32_bf16 v253, v72, v72\n"
    "v_dot2c_f32_bf16 v252, v73, v172\n"
    "v_dot2c_f32_bf16 v253, v73, v73\n"
    "v_dot2c_f32_bf16 v252, v74, v172\n"
    "v_dot2c_f32_bf16 v253, v74, v74\n"
    "v_dot2c_f32_bf16 v252, v75, v172\n"
    "v_dot2c_f32_bf16 v253, v75, v75\n"
    "v_dot2c_f32_bf16 v252, v76, v172\n"
    "v_dot2c_f32_bf16 v253, v76, v76\n"
    "v_dot2c_f32_bf16 v252, v77, v172\n"
    "v_dot2c_f32_bf16 v253, v77, v77\n"
    "v_dot2c_f32_bf16 v252, v78, v172\n"
    "v_dot2c_f32_bf16 v253, v78, v78\n"
    "v_dot2c_f32_bf16 v252, v79, v172\n"
    "v_dot2c_f32_bf16 v253, v79, v79\n"
    "v_mfma_f32_32x32x16_bf16 v[128:143], v[208:211], v[72:75], v[128:143]\n"
    "s_waitcnt lgkmcnt(0)\n"
    "s_barrier\n"
    "v_mfma_f32_32x32x16_bf16 v[144:159], v[212:215], v[72:75], v[144:159]\n"
    "ds_read_b128 v[80:83], v160 offset:0\n"
    "ds_read_b128 v[84:87], v161 offset:0\n"
    "ds_read_b128 v[88:91], v162 offset:0\n"
    "ds_read_b128 v[92:95], v163 offset:0\n"
    "ds_read_b64_tr_b16 v[192:193], v234 offset:57344\n"
    "ds_read_b64_tr_b16 v[194:195], v234 offset:57856\n"
    "ds_read_b64_tr_b16 v[196:197], v235 offset:57344\n"
    "ds_read_b64_tr_b16 v[198:199], v235 offset:57856\n"
    "v_mfma_f32_32x32x16_bf16 v[128:143], v[216:219], v[76:79], v[128:143]\n"
    "ds_read_b64_tr_b16 v[200:201], v234 offset:59392\n"
    "ds_read_b64_tr_b16 v[202:203], v234 offset:59904\n"
    "ds_read_b64_tr_b16 v[204:205], v235 offset:59392\n"
    "ds_read_b64_tr_b16 v[206:207], v235 offset:59904\n"
    "v_mfma_f32_32x32x16_bf16 v[144:159], v[220:223], v[76:79], v[144:159]\n"
    "s_waitcnt lgkmcnt(6)\n"
    "ds_read_b64_tr_b16 v[208:209], v234 offset:61440\n"
    "ds_read_b64_tr_b16 v[210:211], v234 offset:61952\n"
    "ds_read_b64_tr_b16 v[212:213], v235 offset:61440\n"
    "ds_read_b64_tr_b16 v[214:215], v235 offset:61952\n"
    "v_mfma_f32_32x32x16_bf16 v[128:143], v[192:195], v[80:83], v[128:143]\n"
    "s_waitcnt lgkmcnt(8)\n"
    "ds_read_b64_tr_b16 v[216:217], v234 offset:63488\n"
    "ds_read_b64_tr_b16 v[218:219], v234 offset:64000\n"
    "v_mfma_f32_32x32x16_bf16 v[144:159], v[196:199], v[80:83], v[144:159]\n"
    "s_waitcnt lgkmcnt(8)\n"
    "ds_read_b64_tr_b16 v[220:221], v235 offset:63488\n"
    "ds_read_b64_tr_b16 v[222:223], v235 offset:64000\n"
    "v_mfma_f32_32x32x16_bf16 v[128:143], v[200:203], v[84:87], v[128:143]\n"
    "s_waitcnt lgkmcnt(8)\n"
    "v_mfma_f32_32x32x16_bf16 v[144:159], v[204:207], v[84:87], v[144:159]\n"
    "s_waitcnt lgkmcnt(6)\n"
    "v_dot2c_f32_bf16 v252, v80, v172\n"
    "v_dot2c_f32_bf16 v253, v80, v80\n"
    "v_dot2c_f32_bf16 v252, v81, v172\n"
    "v_dot2c_f32_bf16 v253, v81, v81\n"
    "v_dot2c_f32_bf16 v252, v82, v172\n"
    "v_dot2c_f32_bf16 v253, v82, v82\n"
    "v_dot2c_f32_bf16 v252, v83, v172\n"
    "v_dot2c_f32_bf16 v253, v83, v83\n"
    "v_dot2c_f32_bf16 v252, v84, v172\n"
    "v_dot2c_f32_bf16 v253, v84, v84\n"
    "v_dot2c_f32_bf16 v252, v85, v172\n"
    "v_dot2c_f32_bf16 v253, v85, v85\n"
    "v_dot2c_f32_bf16 v252, v86, v172\n"
    "v_dot2c_f32_bf16 v253, v86, v86\n"
    "v_dot2c_f32_bf16 v252, v87, v172\n"
    "v_dot2c_f32_bf16 v253, v87, v87\n"
    "v_dot2c_f32_bf16 v252, v88, v172\n"
    "v_dot2c_f32_bf16 v253, v88, v88\n"
    "v_dot2c_f32_bf16 v252, v89, v172\n"
    "v_dot2c_f32_bf16 v253, v89, v89\n"
    "v_dot2c_f32_bf16 v252, v90, v172\n"
    "v_dot2c_f32_bf16 v253, v90, v90\n"
    "v_dot2c_f32_bf16 v252, v91, v172\n"
    "v_dot2c_f32_bf16 v253, v91, v91\n"
    "v_dot2c_f32_bf16 v252, v92, v172\n"
    "v_dot2c_f32_bf16 v253, v92, v92\n"
    "v_dot2c_f32_bf16 v252, v93, v172\n"
    "v_dot2c_f32_bf16 v253, v93, v93\n"
    "v_dot2c_f32_bf16 v252, v94, v172\n"
    "v_dot2c_f32_bf16 v253, v94, v94\n"
    "v_dot2c_f32_bf16 v252, v95, v172\n"
    "v_dot2c_f32_bf16 v253, v95, v95\n"
    "v_mfma_f32_32x32x16_bf16 v[128:143], v[208:211], v[88:91], v[128:143]\n"
    "s_waitcnt lgkmcnt(0)\n"
    "s_barrier\n"
    "v_mfma_f32_32x32x16_bf16 v[144:159], v[212:215], v[88:91], v[144:159]\n"
    "ds_read_b128 v[96:99], v160 offset:16384\n"
    "ds_read_b128 v[100:103], v161 offset:16384\n"
    "ds_read_b128 v[104:107], v162 offset:16384\n"
    "ds_read_b128 v[108:111], v163 offset:16384\n"
    "ds_read_b64_tr_b16 v[192:193], v232 offset:32768\n"
    "ds_read_b64_tr_b16 v[194:195], v232 offset:33280\n"
    "ds_read_b64_tr_b16 v[196:197], v233 offset:32768\n"
    "ds_read_b64_tr_b16 v[198:199], v233 offset:33280\n"
    "v_mfma_f32_32x32x16_bf16 v[128:143], v[216:219], v[92:95], v[128:143]\n"
    "ds_read_b64_tr_b16 v[200:201], v232 offset:34816\n"
    "ds_read_b64_tr_b16 v[202:203], v232 offset:35328\n"
    "ds_read_b64_tr_b16 v[204:205], v233 offset:34816\n"
    "ds_read_b64_tr_b16 v[206:207], v233 offset:35328\n"
    "v_mfma_f32_32x32x16_bf16 v[144:159], v[220:223], v[92:95], v[144:159]\n"
    "s_waitcnt lgkmcnt(6)\n"
    "ds_read_b64_tr_b16 v[208:209], v232 offset:36864\n"
    "ds_read_b64_tr_b16 v[210:211], v232 offset:37376\n"
    "ds_read_b64_tr_b16 v[212:213], v233 offset:36864\n"
    "ds_read_b64_tr_b16 v[214:215], v233 offset:37376\n"
    "v_mfma_f32_32x32x16_bf16 v[128:143], v[192:195], v[96:99], v[128:143]\n"
    "s_waitcnt lgkmcnt(8)\n"
    "ds_read_b64_tr_b16 v[216:217], v232 offset:38912\n"
    "ds_read_b64_tr_b16 v[218:219], v232 offset:39424\n"
    "v_mfma_f32_32x32x16_bf16 v[144:159], v[196:199], v[96:99], v[144:159]\n"
    "s_waitcnt lgkmcnt(8)\n"
    "ds_read_b64_tr_b16 v[220:221], v233 offset:38912\n"
    "ds_read_b64_tr_b16 v[222:223], v233 offset:39424\n"
    "v_mfma_f32_32x32x16_bf16 v[128:143], v[200:203], v[100:103], v[128:143]\n"
    "s_waitcnt lgkmcnt(8)\n"
    "v_mfma_f32_32x32x16_bf16 v[144:159], v[204:207], v[100:103], v[144:159]\n"
    "s_waitcnt lgkmcnt(6)\n"
    "v_dot2c_f32_bf16 v252, v96, v172\n"
    "v_dot2c_f32_bf16 v253, v96, v96\n"
    "v_dot2c_f32_bf16 v252, v97, v172\n"
    "v_dot2c_f32_bf16 v253, v97, v97\n"
    "v_dot2c_f32_bf16 v252, v98, v172\n"
    "v_dot2c_f32_bf16 v253, v98, v98\n"
    "v_dot2c_f32_bf16 v252, v99, v172\n"
    "v_dot2c_f32_bf16 v253, v99, v99\n"
    "v_dot2c_f32_bf16 v252, v100, v172\n"
    "v_dot2c_f32_bf16 v253, v100, v100\n"
    "v_dot2c_f32_bf16 v252, v101, v172\n"
    "v_dot2c_f32_bf16 v253, v101, v101\n"
    "v_dot2c_f32_bf16 v252, v102, v172\n"
    "v_dot2c_f32_bf16 v253, v102, v102\n"
    "v_dot2c_f32_bf16 v252, v103, v172\n"
    "v_dot2c_f32_bf16 v253, v103, v103\n"
    "v_dot2c_f32_bf16 v252, v104, v172\n"
    "v_dot2c_f32_bf16 v253, v104, v104\n"
    "v_dot2c_f32_bf16 v252, v105, v172\n"
    "v_dot2c_f32_bf16 v253, v105, v105\n"
    "v_dot2c_f32_bf16 v252, v106, v172\n"
    "v_dot2c_f32_bf16 v253, v106, v106\n"
    "v_dot2c_f32_bf16 v252, v107, v172\n"
    "v_dot2c_f32_bf16 v253, v107, v107\n"
    "v_dot2c_f32_bf16 v252, v108, v172\n"
    "v_dot2c_f32_bf16 v253, v108, v108\n"
    "v_dot2c_f32_bf16 v252, v109, v172\n"
    "v_dot2c_f32_bf16 v253, v109, v109\n"
    "v_dot2c_f32_bf16 v252, v110, v172\n"
    "v_dot2c_f32_bf16 v253, v110, v110\n"
    "v_dot2c_f32_bf16 v252, v111, v172\n"
    "v_dot2c_f32_bf16 v253, v111, v111\n"
    "v_mfma_f32_32x32x16_bf16 v[128:143], v[208:211], v[104:107], v[128:143]\n"
    "s_waitcnt lgkmcnt(0)\n"
    "s_barrier\n"
    "v_mfma_f32_32x32x16_bf16 v[144:159], v[212:215], v[104:107], v[144:159]\n"
    "ds_read_b128 v[112:115], v160 offset:49152\n"
    "ds_read_b128 v[116:119], v161 offset:49152\n"
    "ds_read_b128 v[120:123], v162 offset:49152\n"
    "ds_read_b128 v[124:127], v163 offset:49152\n"
    "ds_read_b64_tr_b16 v[192:193], v232 offset:40960\n"
    "ds_read_b64_tr_b16 v[194:195], v232 offset:41472\n"
    "ds_read_b64_tr_b16 v[196:197], v233 offset:40960\n"
    "ds_read_b64_tr_b16 v[198:199], v233 offset:41472\n"
    "v_mfma_f32_32x32x16_bf16 v[128:143], v[216:219], v[108:111], v[128:143]\n"
    "ds_read_b64_tr_b16 v[200:201], v232 offset:43008\n"
    "ds_read_b64_tr_b16 v[202:203], v232 offset:43520\n"
    "ds_read_b64_tr_b16 v[204:205], v233 offset:43008\n"
    "ds_read_b64_tr_b16 v[206:207], v233 offset:43520\n"
    "v_mfma_f32_32x32x16_bf16 v[144:159], v[220:223], v[108:111], v[144:159]\n"
    "s_waitcnt lgkmcnt(6)\n"
    "ds_read_b64_tr_b16 v[208:209], v232 offset:45056\n"
    "ds_read_b64_tr_b16 v[210:211], v232 offset:45568\n"
    "ds_read_b64_tr_b16 v[212:213], v233 offset:45056\n"
    "ds_read_b64_tr_b16 v[214:215], v233 offset:45568\n"
    "v_mfma_f32_32x32x16_bf16 v[128:143], v[192:195], v[112:115], v[128:143]\n"
    "s_waitcnt lgkmcnt(8)\n"
    "ds_read_b64_tr_b16 v[216:217], v232 offset:47104\n"
    "ds_read_b64_tr_b16 v[218:219], v232 offset:47616\n"
    "v_mfma_f32_32x32x16_bf16 v[144:159], v[196:199], v[112:115], v[144:159]\n"
    "s_waitcnt lgkmcnt(8)\n"
    "ds_read_b64_tr_b16 v[220:221], v233 offset:47104\n"
    "ds_read_b64_tr_b16 v[222:223], v233 offset:47616\n"
    "v_mfma_f32_32x32x16_bf16 v[128:143], v[200:203], v[116:119], v[128:143]\n"
    "s_waitcnt lgkmcnt(8)\n"
    "v_mfma_f32_32x32x16_bf16 v[144:159], v[204:207], v[116:119], v[144:159]\n"
    "s_waitcnt lgkmcnt(6)\n"
    "v_dot2c_f32_bf16 v252, v112, v172\n"
    "v_dot2c_f32_bf16 v253, v112, v112\n"
    "v_dot2c_f32_bf16 v252, v113, v172\n"
    "v_dot2c_f32_bf16 v253, v113, v113\n"
    "v_dot2c_f32_bf16 v252, v114, v172\n"
    "v_dot2c_f32_bf16 v253, v114, v114\n"
    "v_dot2c_f32_bf16 v252, v115, v172\n"
    "v_dot2c_f32_bf16 v253, v115, v115\n"
    "v_dot2c_f32_bf16 v252, v116, v172\n"
    "v_dot2c_f32_bf16 v253, v116, v116\n"
    "v_dot2c_f32_bf16 v252, v117, v172\n"
    "v_dot2c_f32_bf16 v253, v117, v117\n"
    "v_dot2c_f32_bf16 v252, v118, v172\n"
    "v_dot2c_f32_bf16 v253, v118, v118\n"
    "v_dot2c_f32_bf16 v252, v119, v172\n"
    "v_dot2c_f32_bf16 v253, v119, v119\n"
    "v_dot2c_f32_bf16 v252, v120, v172\n"
    "v_dot2c_f32_bf16 v253, v120, v120\n"
    "v_dot2c_f32_bf16 v252, v121, v172\n"
    "v_dot2c_f32_bf16 v253, v121, v121\n"
    "v_dot2c_f32_bf16 v252, v122, v172\n"
    "v_dot2c_f32_bf16 v253, v122, v122\n"
    "v_dot2c_f32_bf16 v252, v123, v172\n"
    "v_dot2c_f32_bf16 v253, v123, v123\n"
    "v_dot2c_f32_bf16 v252, v124, v172\n"
    "v_dot2c_f32_bf16 v253, v124, v124\n"
    "v_dot2c_f32_bf16 v252, v125, v172\n"
    "v_dot2c_f32_bf16 v253, v125, v125\n"
    "v_dot2c_f32_bf16 v252, v126, v172\n"
    "v_dot2c_f32_bf16 v253, v126, v126\n"
    "v_dot2c_f32_bf16 v252, v127, v172\n"
    "v_dot2c_f32_bf16 v253, v127, v127\n"
    "v_mfma_f32_32x32x16_bf16 v[128:143], v[208:211], v[120:123], v[128:143]\n"
    "s_waitcnt lgkmcnt(0)\n"
    "s_barrier\n"
    "v_mfma_f32_32x32x16_bf16 v[144:159], v[212:215], v[120:123], v[144:159]\n"
    "ds_read_b64 v[240:241], v248 offset:0\n"
    "ds_read_b64 v[242:243], v248 offset:256\n"
    "ds_read_b64_tr_b16 v[192:193], v234 offset:0\n"
    "ds_read_b64_tr_b16 v[194:195], v234 offset:512\n"
    "ds_read_b64_tr_b16 v[196:197], v235 offset:0\n"
    "ds_read_b64_tr_b16 v[198:199], v235 offset:512\n"
    "v_mfma_f32_32x32x16_bf16 v[128:143], v[216:219], v[124:127], v[128:143]\n"
    "ds_read_b64_tr_b16 v[200:201], v234 offset:2048\n"
    "ds_read_b64_tr_b16 v[202:203], v234 offset:2560\n"
    "ds_read_b64_tr_b16 v[204:205], v235 offset:2048\n"
    "ds_read_b64_tr_b16 v[206:207], v235 offset:2560\n"
    "v_mfma_f32_32x32x16_bf16 v[144:159], v[220:223], v[124:127], v[144:159]\n"
    "s_waitcnt lgkmcnt(6)\n"
    "ds_read_b64_tr_b16 v[208:209], v234 offset:4096\n"
    "ds_read_b64_tr_b16 v[210:211], v234 offset:4608\n"
    "ds_read_b64_tr_b16 v[212:213], v235 offset:4096\n"
    "ds_read_b64_tr_b16 v[214:215], v235 offset:4608\n"
    "v_mfma_f32_32x32x16_bf16 v[160:175], v[192:195], v[0:3], 0\n"
    "s_waitcnt lgkmcnt(8)\n"
    "ds_read_b64_tr_b16 v[216:217], v234 offset:6144\n"
    "ds_read_b64_tr_b16 v[218:219], v234 offset:6656\n"
    "v_permlane32_swap_b32 v252, v253\n"
    "v_add_f32 v252, v252, v253\n"
    "v_mov_b32 v253, v252\n"
    "v_mfma_f32_32x32x16_bf16 v[176:191], v[196:199], v[0:3], 0\n"
    "s_waitcnt lgkmcnt(8)\n"
    "ds_read_b64_tr_b16 v[220:221], v235 offset:6144\n"
    "ds_read_b64_tr_b16 v[222:223], v235 offset:6656\n"
    "v_permlane32_swap_b32 v252, v253\n"
    "v_mul_f32 v252, 0x3b000000, v252\n"
    "v_mfma_f32_32x32x16_bf16 v[160:175], v[200:203], v[4:7], v[160:175]\n"
    "s_waitcnt lgkmcnt(8)\n"
    "ds_read_b64_tr_b16 v[192:193], v234 offset:8192\n"
    "ds_read_b64_tr_b16 v[194:195], v234 offset:8704\n"
    "v_mul_f32 v224, v252, v252\n"
    "v_mul_f32 v253, 0x3b000000, v253\n"
    "v_mfma_f32_32x32x16_bf16 v[176:191], v[204:207], v[4:7], v[176:191]\n"
    "s_waitcnt lgkmcnt(8)\n"
    "ds_read_b64_tr_b16 v[196:197], v235 offset:8192\n"
    "ds_read_b64_tr_b16 v[198:199], v235 offset:8704\n"
    "v_sub_f32 v253, v253, v224\n"
    "v_max_f32 v253, 0, v253\n"
    "v_mfma_f32_32x32x16_bf16 v[160:175], v[208:211], v[8:11], v[160:175]\n"
    "s_waitcnt lgkmcnt(8)\n"
    "ds_read_b64_tr_b16 v[200:201], v234 offset:10240\n"
    "ds_read_b64_tr_b16 v[202:203], v234 offset:10752\n"
    "v_cvt_pk_bf16_f32 v224, v253, v253\n"
    "v_lshlrev_b32 v224, 16, v224\n"
    "v_mfma_f32_32x32x16_bf16 v[176:191], v[212:215], v[8:11], v[176:191]\n"
    "s_waitcnt lgkmcnt(8)\n"
    "ds_read_b64_tr_b16 v[204:205], v235 offset:10240\n"
    "ds_read_b64_tr_b16 v[206:207], v235 offset:10752\n"
    "v_add_f32 v224, 0x3727c5ac, v224\n"
    "v_cvt_pk_bf16_f32 v224, v224, v224\n"
    "v_mfma_f32_32x32x16_bf16 v[160:175], v[216:219], v[12:15], v[160:175]\n"
    "s_waitcnt lgkmcnt(8)\n"
    "ds_read_b64_tr_b16 v[208:209], v234 offset:12288\n"
    "ds_read_b64_tr_b16 v[210:211], v234 offset:12800\n"
    "v_lshlrev_b32 v224, 16, v224\n"
    "v_sqrt_f32 v224, v224\n"
    "v_mfma_f32_32x32x16_bf16 v[176:191], v[220:223], v[12:15], v[176:191]\n"
    "s_waitcnt lgkmcnt(8)\n"
    "ds_read_b64_tr_b16 v[212:213], v235 offset:12288\n"
    "ds_read_b64_tr_b16 v[214:215], v235 offset:12800\n"
    "v_cvt_pk_bf16_f32 v244, -v252, -v252\n"
    "v_cvt_pk_bf16_f32 v245, v224, -v252\n"
    "v_mfma_f32_32x32x16_bf16 v[160:175], v[192:195], v[16:19], v[160:175]\n"
    "s_waitcnt lgkmcnt(8)\n"
    "ds_read_b64_tr_b16 v[216:217], v234 offset:14336\n"
    "ds_read_b64_tr_b16 v[218:219], v234 offset:14848\n"
    "v_lshlrev_b32 v224, 16, v245\n"
    "v_rcp_f32 v239, v224\n"
    "v_mfma_f32_32x32x16_bf16 v[176:191], v[196:199], v[16:19], v[176:191]\n"
    "s_waitcnt lgkmcnt(8)\n"
    "ds_read_b64_tr_b16 v[220:221], v235 offset:14336\n"
    "ds_read_b64_tr_b16 v[222:223], v235 offset:14848\n"
    "v_and_b32 v244, v244, v250\n"
    "v_and_b32 v245, v245, v250\n"
    "v_mfma_f32_32x32x16_bf16 v[160:175], v[200:203], v[20:23], v[160:175]\n"
    "s_waitcnt lgkmcnt(8)\n"
    "v_mul_f32 v252, 0x3eb8aa3b, v239\n"
    "v_mfma_f32_32x32x16_bf16 v[128:143], v[240:243], v[244:247], v[128:143]\n"
    "v_mfma_f32_32x32x16_bf16 v[176:191], v[204:207], v[20:23], v[176:191]\n"
    "s_waitcnt lgkmcnt(6)\n"
    "v_mfma_f32_32x32x16_bf16 v[144:159], v[242:245], v[244:247], v[144:159]\n"
    "v_mfma_f32_32x32x16_bf16 v[160:175], v[208:211], v[24:27], v[160:175]\n"
    "s_waitcnt lgkmcnt(0)\n"
    "s_barrier\n"
    "v_mfma_f32_32x32x16_bf16 v[176:191], v[212:215], v[24:27], v[176:191]\n"
    "ds_read_b64_tr_b16 v[192:193], v234 offset:16384\n"
    "ds_read_b64_tr_b16 v[194:195], v234 offset:16896\n"
    "ds_read_b64_tr_b16 v[196:197], v235 offset:16384\n"
    "ds_read_b64_tr_b16 v[198:199], v235 offset:16896\n"
    "v_mfma_f32_32x32x16_bf16 v[160:175], v[216:219], v[28:31], v[160:175]\n"
    "ds_read_b64_tr_b16 v[200:201], v234 offset:18432\n"
    "ds_read_b64_tr_b16 v[202:203], v234 offset:18944\n"
    "ds_read_b64_tr_b16 v[204:205], v235 offset:18432\n"
    "ds_read_b64_tr_b16 v[206:207], v235 offset:18944\n"
    "v_mfma_f32_32x32x16_bf16 v[176:191], v[220:223], v[28:31], v[176:191]\n"
    "s_waitcnt lgkmcnt(6)\n"
    "ds_read_b64_tr_b16 v[208:209], v234 offset:20480\n"
    "ds_read_b64_tr_b16 v[210:211], v234 offset:20992\n"
    "ds_read_b64_tr_b16 v[212:213], v235 offset:20480\n"
    "ds_read_b64_tr_b16 v[214:215], v235 offset:20992\n"
    "v_mfma_f32_32x32x16_bf16 v[160:175], v[192:195], v[32:35], v[160:175]\n"
    "s_waitcnt lgkmcnt(8)\n"
    "ds_read_b64_tr_b16 v[216:217], v234 offset:22528\n"
    "ds_read_b64_tr_b16 v[218:219], v234 offset:23040\n"
    "v_mul_f32 v128, v239, v128\n"
    "v_mul_f32 v129, v239, v129\n"
    "v_mfma_f32_32x32x16_bf16 v[176:191], v[196:199], v[32:35], v[176:191]\n"
    "s_waitcnt lgkmcnt(8)\n"
    "ds_read_b64_tr_b16 v[220:221], v235 offset:22528\n"
    "ds_read_b64_tr_b16 v[222:223], v235 offset:23040\n"
    "v_mul_f32 v130, v239, v130\n"
    "v_mul_f32 v131, v239, v131\n"
    "v_mfma_f32_32x32x16_bf16 v[160:175], v[200:203], v[36:39], v[160:175]\n"
    "s_waitcnt lgkmcnt(8)\n"
    "ds_read_b64_tr_b16 v[192:193], v234 offset:24576\n"
    "ds_read_b64_tr_b16 v[194:195], v234 offset:25088\n"
    "v_mul_f32 v132, v239, v132\n"
    "v_mul_f32 v133, v239, v133\n"
    "v_mfma_f32_32x32x16_bf16 v[176:191], v[204:207], v[36:39], v[176:191]\n"
    "s_waitcnt lgkmcnt(8)\n"
    "ds_read_b64_tr_b16 v[196:197], v235 offset:24576\n"
    "ds_read_b64_tr_b16 v[198:199], v235 offset:25088\n"
    "v_mul_f32 v134, v239, v134\n"
    "v_mul_f32 v135, v239, v135\n"
    "v_mfma_f32_32x32x16_bf16 v[160:175], v[208:211], v[40:43], v[160:175]\n"
    "s_waitcnt lgkmcnt(8)\n"
    "ds_read_b64_tr_b16 v[200:201], v234 offset:26624\n"
    "ds_read_b64_tr_b16 v[202:203], v234 offset:27136\n"
    "v_mul_f32 v136, v239, v136\n"
    "v_mul_f32 v137, v239, v137\n"
    "v_mfma_f32_32x32x16_bf16 v[176:191], v[212:215], v[40:43], v[176:191]\n"
    "s_waitcnt lgkmcnt(8)\n"
    "ds_read_b64_tr_b16 v[204:205], v235 offset:26624\n"
    "ds_read_b64_tr_b16 v[206:207], v235 offset:27136\n"
    "v_mul_f32 v138, v239, v138\n"
    "v_mul_f32 v139, v239, v139\n"
    "v_mfma_f32_32x32x16_bf16 v[160:175], v[216:219], v[44:47], v[160:175]\n"
    "s_waitcnt lgkmcnt(8)\n"
    "ds_read_b64_tr_b16 v[208:209], v234 offset:28672\n"
    "ds_read_b64_tr_b16 v[210:211], v234 offset:29184\n"
    "v_mul_f32 v140, v239, v140\n"
    "v_mul_f32 v141, v239, v141\n"
    "v_mfma_f32_32x32x16_bf16 v[176:191], v[220:223], v[44:47], v[176:191]\n"
    "s_waitcnt lgkmcnt(8)\n"
    "ds_read_b64_tr_b16 v[212:213], v235 offset:28672\n"
    "ds_read_b64_tr_b16 v[214:215], v235 offset:29184\n"
    "v_mul_f32 v142, v239, v142\n"
    "v_mul_f32 v143, v239, v143\n"
    "v_mfma_f32_32x32x16_bf16 v[160:175], v[192:195], v[48:51], v[160:175]\n"
    "s_waitcnt lgkmcnt(8)\n"
    "ds_read_b64_tr_b16 v[216:217], v234 offset:30720\n"
    "ds_read_b64_tr_b16 v[218:219], v234 offset:31232\n"
    "v_cvt_pk_bf16_f32 v128, v128, v129\n"
    "v_cvt_pk_bf16_f32 v129, v130, v131\n"
    "v_mfma_f32_32x32x16_bf16 v[176:191], v[196:199], v[48:51], v[176:191]\n"
    "s_waitcnt lgkmcnt(8)\n"
    "ds_read_b64_tr_b16 v[220:221], v235 offset:30720\n"
    "ds_read_b64_tr_b16 v[222:223], v235 offset:31232\n"
    "v_cvt_pk_bf16_f32 v130, v132, v133\n"
    "v_cvt_pk_bf16_f32 v131, v134, v135\n"
    "v_mfma_f32_32x32x16_bf16 v[160:175], v[200:203], v[52:55], v[160:175]\n"
    "s_waitcnt lgkmcnt(8)\n"
    "v_cvt_pk_bf16_f32 v132, v136, v137\n"
    "v_cvt_pk_bf16_f32 v133, v138, v139\n"
    "v_mfma_f32_32x32x16_bf16 v[176:191], v[204:207], v[52:55], v[176:191]\n"
    "s_waitcnt lgkmcnt(6)\n"
    "v_cvt_pk_bf16_f32 v134, v140, v141\n"
    "v_cvt_pk_bf16_f32 v135, v142, v143\n"
    "v_mfma_f32_32x32x16_bf16 v[160:175], v[208:211], v[56:59], v[160:175]\n"
    "s_waitcnt lgkmcnt(0)\n"
    "s_barrier\n"
    "v_mfma_f32_32x32x16_bf16 v[176:191], v[212:215], v[56:59], v[176:191]\n"
    "ds_read_b64_tr_b16 v[192:193], v234 offset:32768\n"
    "ds_read_b64_tr_b16 v[194:195], v234 offset:33280\n"
    "ds_read_b64_tr_b16 v[196:197], v235 offset:32768\n"
    "ds_read_b64_tr_b16 v[198:199], v235 offset:33280\n"
    "v_mfma_f32_32x32x16_bf16 v[160:175], v[216:219], v[60:63], v[160:175]\n"
    "ds_read_b64_tr_b16 v[200:201], v234 offset:34816\n"
    "ds_read_b64_tr_b16 v[202:203], v234 offset:35328\n"
    "ds_read_b64_tr_b16 v[204:205], v235 offset:34816\n"
    "ds_read_b64_tr_b16 v[206:207], v235 offset:35328\n"
    "v_mfma_f32_32x32x16_bf16 v[176:191], v[220:223], v[60:63], v[176:191]\n"
    "s_waitcnt lgkmcnt(6)\n"
    "ds_read_b64_tr_b16 v[208:209], v234 offset:36864\n"
    "ds_read_b64_tr_b16 v[210:211], v234 offset:37376\n"
    "ds_read_b64_tr_b16 v[212:213], v235 offset:36864\n"
    "ds_read_b64_tr_b16 v[214:215], v235 offset:37376\n"
    "v_mfma_f32_32x32x16_bf16 v[160:175], v[192:195], v[64:67], v[160:175]\n"
    "s_waitcnt lgkmcnt(8)\n"
    "ds_read_b64_tr_b16 v[216:217], v234 offset:38912\n"
    "ds_read_b64_tr_b16 v[218:219], v234 offset:39424\n"
    "v_mul_f32 v144, v239, v144\n"
    "v_mul_f32 v145, v239, v145\n"
    "v_mfma_f32_32x32x16_bf16 v[176:191], v[196:199], v[64:67], v[176:191]\n"
    "s_waitcnt lgkmcnt(8)\n"
    "ds_read_b64_tr_b16 v[220:221], v235 offset:38912\n"
    "ds_read_b64_tr_b16 v[222:223], v235 offset:39424\n"
    "v_mul_f32 v146, v239, v146\n"
    "v_mul_f32 v147, v239, v147\n"
    "v_mfma_f32_32x32x16_bf16 v[160:175], v[200:203], v[68:71], v[160:175]\n"
    "s_waitcnt lgkmcnt(8)\n"
    "ds_read_b64_tr_b16 v[192:193], v234 offset:40960\n"
    "ds_read_b64_tr_b16 v[194:195], v234 offset:41472\n"
    "v_mul_f32 v148, v239, v148\n"
    "v_mul_f32 v149, v239, v149\n"
    "v_mfma_f32_32x32x16_bf16 v[176:191], v[204:207], v[68:71], v[176:191]\n"
    "s_waitcnt lgkmcnt(8)\n"
    "ds_read_b64_tr_b16 v[196:197], v235 offset:40960\n"
    "ds_read_b64_tr_b16 v[198:199], v235 offset:41472\n"
    "v_mul_f32 v150, v239, v150\n"
    "v_mul_f32 v151, v239, v151\n"
    "v_mfma_f32_32x32x16_bf16 v[160:175], v[208:211], v[72:75], v[160:175]\n"
    "s_waitcnt lgkmcnt(8)\n"
    "ds_read_b64_tr_b16 v[200:201], v234 offset:43008\n"
    "ds_read_b64_tr_b16 v[202:203], v234 offset:43520\n"
    "v_mul_f32 v152, v239, v152\n"
    "v_mul_f32 v153, v239, v153\n"
    "v_mfma_f32_32x32x16_bf16 v[176:191], v[212:215], v[72:75], v[176:191]\n"
    "s_waitcnt lgkmcnt(8)\n"
    "ds_read_b64_tr_b16 v[204:205], v235 offset:43008\n"
    "ds_read_b64_tr_b16 v[206:207], v235 offset:43520\n"
    "v_mul_f32 v154, v239, v154\n"
    "v_mul_f32 v155, v239, v155\n"
    "v_mfma_f32_32x32x16_bf16 v[160:175], v[216:219], v[76:79], v[160:175]\n"
    "s_waitcnt lgkmcnt(8)\n"
    "ds_read_b64_tr_b16 v[208:209], v234 offset:45056\n"
    "ds_read_b64_tr_b16 v[210:211], v234 offset:45568\n"
    "v_mul_f32 v156, v239, v156\n"
    "v_mul_f32 v157, v239, v157\n"
    "v_mfma_f32_32x32x16_bf16 v[176:191], v[220:223], v[76:79], v[176:191]\n"
    "s_waitcnt lgkmcnt(8)\n"
    "ds_read_b64_tr_b16 v[212:213], v235 offset:45056\n"
    "ds_read_b64_tr_b16 v[214:215], v235 offset:45568\n"
    "v_mul_f32 v158, v239, v158\n"
    "v_mul_f32 v159, v239, v159\n"
    "v_mfma_f32_32x32x16_bf16 v[160:175], v[192:195], v[80:83], v[160:175]\n"
    "s_waitcnt lgkmcnt(8)\n"
    "ds_read_b64_tr_b16 v[216:217], v234 offset:47104\n"
    "ds_read_b64_tr_b16 v[218:219], v234 offset:47616\n"
    "v_cvt_pk_bf16_f32 v144, v144, v145\n"
    "v_cvt_pk_bf16_f32 v145, v146, v147\n"
    "v_mfma_f32_32x32x16_bf16 v[176:191], v[196:199], v[80:83], v[176:191]\n"
    "s_waitcnt lgkmcnt(8)\n"
    "ds_read_b64_tr_b16 v[220:221], v235 offset:47104\n"
    "ds_read_b64_tr_b16 v[222:223], v235 offset:47616\n"
    "v_cvt_pk_bf16_f32 v146, v148, v149\n"
    "v_cvt_pk_bf16_f32 v147, v150, v151\n"
    "v_mfma_f32_32x32x16_bf16 v[160:175], v[200:203], v[84:87], v[160:175]\n"
    "s_waitcnt lgkmcnt(8)\n"
    "v_cvt_pk_bf16_f32 v148, v152, v153\n"
    "v_cvt_pk_bf16_f32 v149, v154, v155\n"
    "v_mfma_f32_32x32x16_bf16 v[176:191], v[204:207], v[84:87], v[176:191]\n"
    "s_waitcnt lgkmcnt(6)\n"
    "v_cvt_pk_bf16_f32 v150, v156, v157\n"
    "v_cvt_pk_bf16_f32 v151, v158, v159\n"
    "v_mfma_f32_32x32x16_bf16 v[160:175], v[208:211], v[88:91], v[160:175]\n"
    "s_waitcnt lgkmcnt(0)\n"
    "s_barrier\n"
    "v_mfma_f32_32x32x16_bf16 v[176:191], v[212:215], v[88:91], v[176:191]\n"
    "ds_read_b64_tr_b16 v[192:193], v232 offset:0\n"
    "ds_read_b64_tr_b16 v[194:195], v232 offset:512\n"
    "ds_read_b64_tr_b16 v[196:197], v233 offset:0\n"
    "ds_read_b64_tr_b16 v[198:199], v233 offset:512\n"
    "v_mfma_f32_32x32x16_bf16 v[160:175], v[216:219], v[92:95], v[160:175]\n"
    "ds_read_b64_tr_b16 v[200:201], v232 offset:2048\n"
    "ds_read_b64_tr_b16 v[202:203], v232 offset:2560\n"
    "ds_read_b64_tr_b16 v[204:205], v233 offset:2048\n"
    "ds_read_b64_tr_b16 v[206:207], v233 offset:2560\n"
    "v_mfma_f32_32x32x16_bf16 v[176:191], v[220:223], v[92:95], v[176:191]\n"
    "s_waitcnt lgkmcnt(6)\n"
    "ds_read_b64_tr_b16 v[208:209], v232 offset:4096\n"
    "ds_read_b64_tr_b16 v[210:211], v232 offset:4608\n"
    "ds_read_b64_tr_b16 v[212:213], v233 offset:4096\n"
    "ds_read_b64_tr_b16 v[214:215], v233 offset:4608\n"
    "v_mfma_f32_32x32x16_bf16 v[160:175], v[192:195], v[96:99], v[160:175]\n"
    "s_waitcnt lgkmcnt(8)\n"
    "ds_read_b64_tr_b16 v[216:217], v232 offset:6144\n"
    "ds_read_b64_tr_b16 v[218:219], v232 offset:6656\n"
    "v_mfma_f32_32x32x16_bf16 v[176:191], v[196:199], v[96:99], v[176:191]\n"
    "s_waitcnt lgkmcnt(8)\n"
    "ds_read_b64_tr_b16 v[220:221], v233 offset:6144\n"
    "ds_read_b64_tr_b16 v[222:223], v233 offset:6656\n"
    "v_mfma_f32_32x32x16_bf16 v[160:175], v[200:203], v[100:103], v[160:175]\n"
    "s_waitcnt lgkmcnt(8)\n"
    "ds_read_b64_tr_b16 v[192:193], v232 offset:8192\n"
    "ds_read_b64_tr_b16 v[194:195], v232 offset:8704\n"
    "v_mfma_f32_32x32x16_bf16 v[176:191], v[204:207], v[100:103], v[176:191]\n"
    "s_waitcnt lgkmcnt(8)\n"
    "ds_read_b64_tr_b16 v[196:197], v233 offset:8192\n"
    "ds_read_b64_tr_b16 v[198:199], v233 offset:8704\n"
    "ds_write_b64 v254, v[128:129] offset:0\n"
    "v_mfma_f32_32x32x16_bf16 v[160:175], v[208:211], v[104:107], v[160:175]\n"
    "s_waitcnt lgkmcnt(9)\n"
    "ds_read_b64_tr_b16 v[200:201], v232 offset:10240\n"
    "ds_read_b64_tr_b16 v[202:203], v232 offset:10752\n"
    "ds_write_b64 v254, v[130:131] offset:16\n"
    "v_mfma_f32_32x32x16_bf16 v[176:191], v[212:215], v[104:107], v[176:191]\n"
    "s_waitcnt lgkmcnt(10)\n"
    "ds_read_b64_tr_b16 v[204:205], v233 offset:10240\n"
    "ds_read_b64_tr_b16 v[206:207], v233 offset:10752\n"
    "ds_write_b64 v254, v[132:133] offset:32\n"
    "v_mfma_f32_32x32x16_bf16 v[160:175], v[216:219], v[108:111], v[160:175]\n"
    "s_waitcnt lgkmcnt(11)\n"
    "ds_read_b64_tr_b16 v[208:209], v232 offset:12288\n"
    "ds_read_b64_tr_b16 v[210:211], v232 offset:12800\n"
    "ds_write_b64 v254, v[134:135] offset:48\n"
    "v_mfma_f32_32x32x16_bf16 v[176:191], v[220:223], v[108:111], v[176:191]\n"
    "s_waitcnt lgkmcnt(12)\n"
    "ds_read_b64_tr_b16 v[212:213], v233 offset:12288\n"
    "ds_read_b64_tr_b16 v[214:215], v233 offset:12800\n"
    "ds_write_b64 v254, v[144:145] offset:64\n"
    "v_mfma_f32_32x32x16_bf16 v[160:175], v[192:195], v[112:115], v[160:175]\n"
    "s_waitcnt lgkmcnt(13)\n"
    "ds_read_b64_tr_b16 v[216:217], v232 offset:14336\n"
    "ds_read_b64_tr_b16 v[218:219], v232 offset:14848\n"
    "ds_write_b64 v254, v[146:147] offset:80\n"
    "v_mfma_f32_32x32x16_bf16 v[176:191], v[196:199], v[112:115], v[176:191]\n"
    "s_waitcnt lgkmcnt(13)\n"
    "ds_read_b64_tr_b16 v[220:221], v233 offset:14336\n"
    "ds_read_b64_tr_b16 v[222:223], v233 offset:14848\n"
    "ds_write_b64 v254, v[148:149] offset:96\n"
    "v_mfma_f32_32x32x16_bf16 v[160:175], v[200:203], v[116:119], v[160:175]\n"
    "s_waitcnt lgkmcnt(13)\n"
    "ds_write_b64 v254, v[150:151] offset:112\n"
    "v_mfma_f32_32x32x16_bf16 v[176:191], v[204:207], v[116:119], v[176:191]\n"
    "s_waitcnt lgkmcnt(11)\n"
    "v_mfma_f32_32x32x16_bf16 v[160:175], v[208:211], v[120:123], v[160:175]\n"
    "s_waitcnt lgkmcnt(0)\n"
    "s_barrier\n"
    "v_mfma_f32_32x32x16_bf16 v[176:191], v[212:215], v[120:123], v[176:191]\n"
    "ds_read_b64 v[240:241], v248 offset:512\n"
    "ds_read_b64 v[242:243], v248 offset:768\n"
    "ds_read_b64_tr_b16 v[192:193], v234 offset:49152\n"
    "ds_read_b64_tr_b16 v[194:195], v234 offset:49664\n"
    "ds_read_b64_tr_b16 v[196:197], v235 offset:49152\n"
    "ds_read_b64_tr_b16 v[198:199], v235 offset:49664\n"
    "v_mfma_f32_32x32x16_bf16 v[160:175], v[216:219], v[124:127], v[160:175]\n"
    "ds_read_b64_tr_b16 v[200:201], v234 offset:51200\n"
    "ds_read_b64_tr_b16 v[202:203], v234 offset:51712\n"
    "ds_read_b64_tr_b16 v[204:205], v235 offset:51200\n"
    "ds_read_b64_tr_b16 v[206:207], v235 offset:51712\n"
    "v_mfma_f32_32x32x16_bf16 v[176:191], v[220:223], v[124:127], v[176:191]\n"
    "s_waitcnt lgkmcnt(6)\n"
    "ds_read_b64_tr_b16 v[208:209], v234 offset:53248\n"
    "ds_read_b64_tr_b16 v[210:211], v234 offset:53760\n"
    "ds_read_b64_tr_b16 v[212:213], v235 offset:53248\n"
    "ds_read_b64_tr_b16 v[214:215], v235 offset:53760\n"
    "v_mfma_f32_32x32x16_bf16 v[160:175], v[240:243], v[244:247], v[160:175]\n"
    "v_mfma_f32_32x32x16_bf16 v[176:191], v[242:245], v[244:247], v[176:191]\n"
    "v_mfma_f32_32x32x16_bf16 v[128:143], v[192:195], v[0:3], 0\n"
    "s_waitcnt lgkmcnt(8)\n"
    "ds_read_b64_tr_b16 v[216:217], v234 offset:55296\n"
    "ds_read_b64_tr_b16 v[218:219], v234 offset:55808\n"
    "v_mfma_f32_32x32x16_bf16 v[144:159], v[196:199], v[0:3], 0\n"
    "s_waitcnt lgkmcnt(8)\n"
    "ds_read_b64_tr_b16 v[220:221], v235 offset:55296\n"
    "ds_read_b64_tr_b16 v[222:223], v235 offset:55808\n"
    "v_mfma_f32_32x32x16_bf16 v[128:143], v[200:203], v[4:7], v[128:143]\n"
    "s_waitcnt lgkmcnt(8)\n"
    "ds_read_b64_tr_b16 v[192:193], v234 offset:57344\n"
    "ds_read_b64_tr_b16 v[194:195], v234 offset:57856\n"
    "v_mfma_f32_32x32x16_bf16 v[144:159], v[204:207], v[4:7], v[144:159]\n"
    "s_waitcnt lgkmcnt(8)\n"
    "ds_read_b64_tr_b16 v[196:197], v235 offset:57344\n"
    "ds_read_b64_tr_b16 v[198:199], v235 offset:57856\n"
    "v_mul_f32 v160, v252, v160\n"
    "v_mul_f32 v161, v252, v161\n"
    "v_mul_f32 v162, v252, v162\n"
    "v_mul_f32 v163, v252, v163\n"
    "v_mul_f32 v164, v252, v164\n"
    "v_mul_f32 v165, v252, v165\n"
    "v_mfma_f32_32x32x16_bf16 v[128:143], v[208:211], v[8:11], v[128:143]\n"
    "s_waitcnt lgkmcnt(8)\n"
    "ds_read_b64_tr_b16 v[200:201], v234 offset:59392\n"
    "ds_read_b64_tr_b16 v[202:203], v234 offset:59904\n"
    "v_mul_f32 v166, v252, v166\n"
    "v_mul_f32 v167, v252, v167\n"
    "v_mul_f32 v168, v252, v168\n"
    "v_mul_f32 v169, v252, v169\n"
    "v_mul_f32 v170, v252, v170\n"
    "v_mul_f32 v171, v252, v171\n"
    "v_mfma_f32_32x32x16_bf16 v[144:159], v[212:215], v[8:11], v[144:159]\n"
    "s_waitcnt lgkmcnt(8)\n"
    "ds_read_b64_tr_b16 v[204:205], v235 offset:59392\n"
    "ds_read_b64_tr_b16 v[206:207], v235 offset:59904\n"
    "v_mul_f32 v172, v252, v172\n"
    "v_mul_f32 v173, v252, v173\n"
    "v_mul_f32 v174, v252, v174\n"
    "v_mul_f32 v175, v252, v175\n"
    "v_cvt_pk_bf16_f32 v160, v160, v161\n"
    "v_cvt_pk_bf16_f32 v161, v162, v163\n"
    "v_mfma_f32_32x32x16_bf16 v[128:143], v[216:219], v[12:15], v[128:143]\n"
    "s_waitcnt lgkmcnt(8)\n"
    "ds_read_b64_tr_b16 v[208:209], v234 offset:61440\n"
    "ds_read_b64_tr_b16 v[210:211], v234 offset:61952\n"
    "v_cvt_pk_bf16_f32 v162, v164, v165\n"
    "v_cvt_pk_bf16_f32 v163, v166, v167\n"
    "v_cvt_pk_bf16_f32 v164, v168, v169\n"
    "v_cvt_pk_bf16_f32 v165, v170, v171\n"
    "v_cvt_pk_bf16_f32 v166, v172, v173\n"
    "v_mfma_f32_32x32x16_bf16 v[144:159], v[220:223], v[12:15], v[144:159]\n"
    "s_waitcnt lgkmcnt(8)\n"
    "ds_read_b64_tr_b16 v[212:213], v235 offset:61440\n"
    "ds_read_b64_tr_b16 v[214:215], v235 offset:61952\n"
    "v_cvt_pk_bf16_f32 v167, v174, v175\n"
    "v_mul_f32 v176, v252, v176\n"
    "v_mul_f32 v177, v252, v177\n"
    "v_mul_f32 v178, v252, v178\n"
    "v_mul_f32 v179, v252, v179\n"
    "v_mfma_f32_32x32x16_bf16 v[128:143], v[192:195], v[16:19], v[128:143]\n"
    "s_waitcnt lgkmcnt(8)\n"
    "ds_read_b64_tr_b16 v[216:217], v234 offset:63488\n"
    "ds_read_b64_tr_b16 v[218:219], v234 offset:64000\n"
    "v_mul_f32 v180, v252, v180\n"
    "v_mul_f32 v181, v252, v181\n"
    "v_mul_f32 v182, v252, v182\n"
    "v_mul_f32 v183, v252, v183\n"
    "v_mul_f32 v184, v252, v184\n"
    "v_mfma_f32_32x32x16_bf16 v[144:159], v[196:199], v[16:19], v[144:159]\n"
    "s_waitcnt lgkmcnt(8)\n"
    "ds_read_b64_tr_b16 v[220:221], v235 offset:63488\n"
    "ds_read_b64_tr_b16 v[222:223], v235 offset:64000\n"
    "v_mul_f32 v185, v252, v185\n"
    "v_mul_f32 v186, v252, v186\n"
    "v_mul_f32 v187, v252, v187\n"
    "v_mul_f32 v188, v252, v188\n"
    "v_mul_f32 v189, v252, v189\n"
    "v_mfma_f32_32x32x16_bf16 v[128:143], v[200:203], v[20:23], v[128:143]\n"
    "s_waitcnt lgkmcnt(8)\n"
    "v_mul_f32 v190, v252, v190\n"
    "v_mul_f32 v191, v252, v191\n"
    "v_cvt_pk_bf16_f32 v176, v176, v177\n"
    "v_cvt_pk_bf16_f32 v177, v178, v179\n"
    "v_cvt_pk_bf16_f32 v178, v180, v181\n"
    "v_mfma_f32_32x32x16_bf16 v[144:159], v[204:207], v[20:23], v[144:159]\n"
    "s_waitcnt lgkmcnt(6)\n"
    "v_cvt_pk_bf16_f32 v179, v182, v183\n"
    "v_cvt_pk_bf16_f32 v180, v184, v185\n"
    "v_cvt_pk_bf16_f32 v181, v186, v187\n"
    "v_cvt_pk_bf16_f32 v182, v188, v189\n"
    "v_cvt_pk_bf16_f32 v183, v190, v191\n"
    "v_mfma_f32_32x32x16_bf16 v[128:143], v[208:211], v[24:27], v[128:143]\n"
    "s_waitcnt lgkmcnt(0)\n"
    "s_barrier\n"
    "v_mfma_f32_32x32x16_bf16 v[144:159], v[212:215], v[24:27], v[144:159]\n"
    "ds_read_b64_tr_b16 v[192:193], v232 offset:16384\n"
    "ds_read_b64_tr_b16 v[194:195], v232 offset:16896\n"
    "ds_read_b64_tr_b16 v[196:197], v233 offset:16384\n"
    "ds_read_b64_tr_b16 v[198:199], v233 offset:16896\n"
    "v_mfma_f32_32x32x16_bf16 v[128:143], v[216:219], v[28:31], v[128:143]\n"
    "ds_read_b64_tr_b16 v[200:201], v232 offset:18432\n"
    "ds_read_b64_tr_b16 v[202:203], v232 offset:18944\n"
    "ds_read_b64_tr_b16 v[204:205], v233 offset:18432\n"
    "ds_read_b64_tr_b16 v[206:207], v233 offset:18944\n"
    "v_mfma_f32_32x32x16_bf16 v[144:159], v[220:223], v[28:31], v[144:159]\n"
    "s_waitcnt lgkmcnt(6)\n"
    "ds_read_b64_tr_b16 v[208:209], v232 offset:20480\n"
    "ds_read_b64_tr_b16 v[210:211], v232 offset:20992\n"
    "ds_read_b64_tr_b16 v[212:213], v233 offset:20480\n"
    "ds_read_b64_tr_b16 v[214:215], v233 offset:20992\n"
    "v_mfma_f32_32x32x16_bf16 v[128:143], v[192:195], v[32:35], v[128:143]\n"
    "s_waitcnt lgkmcnt(8)\n"
    "ds_read_b64_tr_b16 v[216:217], v232 offset:22528\n"
    "ds_read_b64_tr_b16 v[218:219], v232 offset:23040\n"
    "v_mfma_f32_32x32x16_bf16 v[144:159], v[196:199], v[32:35], v[144:159]\n"
    "s_waitcnt lgkmcnt(8)\n"
    "ds_read_b64_tr_b16 v[220:221], v233 offset:22528\n"
    "ds_read_b64_tr_b16 v[222:223], v233 offset:23040\n"
    "v_mfma_f32_32x32x16_bf16 v[128:143], v[200:203], v[36:39], v[128:143]\n"
    "s_waitcnt lgkmcnt(8)\n"
    "ds_read_b64_tr_b16 v[192:193], v232 offset:24576\n"
    "ds_read_b64_tr_b16 v[194:195], v232 offset:25088\n"
    "v_mfma_f32_32x32x16_bf16 v[144:159], v[204:207], v[36:39], v[144:159]\n"
    "s_waitcnt lgkmcnt(8)\n"
    "ds_read_b64_tr_b16 v[196:197], v233 offset:24576\n"
    "ds_read_b64_tr_b16 v[198:199], v233 offset:25088\n"
    "ds_write_b64 v254, v[160:161] offset:0\n"
    "v_mfma_f32_32x32x16_bf16 v[128:143], v[208:211], v[40:43], v[128:143]\n"
    "s_waitcnt lgkmcnt(9)\n"
    "ds_read_b64_tr_b16 v[200:201], v232 offset:26624\n"
    "ds_read_b64_tr_b16 v[202:203], v232 offset:27136\n"
    "ds_write_b64 v254, v[162:163] offset:16\n"
    "v_mfma_f32_32x32x16_bf16 v[144:159], v[212:215], v[40:43], v[144:159]\n"
    "s_waitcnt lgkmcnt(10)\n"
    "ds_read_b64_tr_b16 v[204:205], v233 offset:26624\n"
    "ds_read_b64_tr_b16 v[206:207], v233 offset:27136\n"
    "ds_write_b64 v254, v[164:165] offset:32\n"
    "v_mfma_f32_32x32x16_bf16 v[128:143], v[216:219], v[44:47], v[128:143]\n"
    "s_waitcnt lgkmcnt(11)\n"
    "ds_read_b64_tr_b16 v[208:209], v232 offset:28672\n"
    "ds_read_b64_tr_b16 v[210:211], v232 offset:29184\n"
    "ds_write_b64 v254, v[166:167] offset:48\n"
    "v_mfma_f32_32x32x16_bf16 v[144:159], v[220:223], v[44:47], v[144:159]\n"
    "s_waitcnt lgkmcnt(12)\n"
    "ds_read_b64_tr_b16 v[212:213], v233 offset:28672\n"
    "ds_read_b64_tr_b16 v[214:215], v233 offset:29184\n"
    "ds_write_b64 v254, v[176:177] offset:64\n"
    "v_mfma_f32_32x32x16_bf16 v[128:143], v[192:195], v[48:51], v[128:143]\n"
    "s_waitcnt lgkmcnt(13)\n"
    "ds_read_b64_tr_b16 v[216:217], v232 offset:30720\n"
    "ds_read_b64_tr_b16 v[218:219], v232 offset:31232\n"
    "ds_write_b64 v254, v[178:179] offset:80\n"
    "v_mfma_f32_32x32x16_bf16 v[144:159], v[196:199], v[48:51], v[144:159]\n"
    "s_waitcnt lgkmcnt(13)\n"
    "ds_read_b64_tr_b16 v[220:221], v233 offset:30720\n"
    "ds_read_b64_tr_b16 v[222:223], v233 offset:31232\n"
    "ds_write_b64 v254, v[180:181] offset:96\n"
    "v_mfma_f32_32x32x16_bf16 v[128:143], v[200:203], v[52:55], v[128:143]\n"
    "s_waitcnt lgkmcnt(13)\n"
    "ds_write_b64 v254, v[182:183] offset:112\n"
    "v_mfma_f32_32x32x16_bf16 v[144:159], v[204:207], v[52:55], v[144:159]\n"
    "s_waitcnt lgkmcnt(11)\n"
    "v_mfma_f32_32x32x16_bf16 v[128:143], v[208:211], v[56:59], v[128:143]\n"
    "s_waitcnt lgkmcnt(0)\n"
    "s_barrier\n"
    "v_mfma_f32_32x32x16_bf16 v[144:159], v[212:215], v[56:59], v[144:159]\n"
    "ds_read_b64_tr_b16 v[192:193], v232 offset:32768\n"
    "ds_read_b64_tr_b16 v[194:195], v232 offset:33280\n"
    "ds_read_b64_tr_b16 v[196:197], v233 offset:32768\n"
    "ds_read_b64_tr_b16 v[198:199], v233 offset:33280\n"
    "v_mfma_f32_32x32x16_bf16 v[128:143], v[216:219], v[60:63], v[128:143]\n"
    "ds_read_b64_tr_b16 v[200:201], v232 offset:34816\n"
    "ds_read_b64_tr_b16 v[202:203], v232 offset:35328\n"
    "ds_read_b64_tr_b16 v[204:205], v233 offset:34816\n"
    "ds_read_b64_tr_b16 v[206:207], v233 offset:35328\n"
    "v_mfma_f32_32x32x16_bf16 v[144:159], v[220:223], v[60:63], v[144:159]\n"
    "s_waitcnt lgkmcnt(6)\n"
    "ds_read_b64_tr_b16 v[208:209], v232 offset:36864\n"
    "ds_read_b64_tr_b16 v[210:211], v232 offset:37376\n"
    "ds_read_b64_tr_b16 v[212:213], v233 offset:36864\n"
    "ds_read_b64_tr_b16 v[214:215], v233 offset:37376\n"
    "v_mfma_f32_32x32x16_bf16 v[128:143], v[192:195], v[64:67], v[128:143]\n"
    "s_waitcnt lgkmcnt(8)\n"
    "ds_read_b64_tr_b16 v[216:217], v232 offset:38912\n"
    "ds_read_b64_tr_b16 v[218:219], v232 offset:39424\n"
    "v_mfma_f32_32x32x16_bf16 v[144:159], v[196:199], v[64:67], v[144:159]\n"
    "s_waitcnt lgkmcnt(8)\n"
    "ds_read_b64_tr_b16 v[220:221], v233 offset:38912\n"
    "ds_read_b64_tr_b16 v[222:223], v233 offset:39424\n"
    "v_mfma_f32_32x32x16_bf16 v[128:143], v[200:203], v[68:71], v[128:143]\n"
    "s_waitcnt lgkmcnt(8)\n"
    "ds_read_b64_tr_b16 v[192:193], v232 offset:40960\n"
    "ds_read_b64_tr_b16 v[194:195], v232 offset:41472\n"
    "v_mfma_f32_32x32x16_bf16 v[144:159], v[204:207], v[68:71], v[144:159]\n"
    "s_waitcnt lgkmcnt(8)\n"
    "ds_read_b64_tr_b16 v[196:197], v233 offset:40960\n"
    "ds_read_b64_tr_b16 v[198:199], v233 offset:41472\n"
    "v_mfma_f32_32x32x16_bf16 v[128:143], v[208:211], v[72:75], v[128:143]\n"
    "s_waitcnt lgkmcnt(8)\n"
    "ds_read_b64_tr_b16 v[200:201], v232 offset:43008\n"
    "ds_read_b64_tr_b16 v[202:203], v232 offset:43520\n"
    "v_mfma_f32_32x32x16_bf16 v[144:159], v[212:215], v[72:75], v[144:159]\n"
    "s_waitcnt lgkmcnt(8)\n"
    "ds_read_b64_tr_b16 v[204:205], v233 offset:43008\n"
    "ds_read_b64_tr_b16 v[206:207], v233 offset:43520\n"
    "v_mfma_f32_32x32x16_bf16 v[128:143], v[216:219], v[76:79], v[128:143]\n"
    "s_waitcnt lgkmcnt(8)\n"
    "ds_read_b64_tr_b16 v[208:209], v232 offset:45056\n"
    "ds_read_b64_tr_b16 v[210:211], v232 offset:45568\n"
    "v_mfma_f32_32x32x16_bf16 v[144:159], v[220:223], v[76:79], v[144:159]\n"
    "s_waitcnt lgkmcnt(8)\n"
    "ds_read_b64_tr_b16 v[212:213], v233 offset:45056\n"
    "ds_read_b64_tr_b16 v[214:215], v233 offset:45568\n"
    "v_mfma_f32_32x32x16_bf16 v[128:143], v[192:195], v[80:83], v[128:143]\n"
    "s_waitcnt lgkmcnt(8)\n"
    "ds_read_b64_tr_b16 v[216:217], v232 offset:47104\n"
    "ds_read_b64_tr_b16 v[218:219], v232 offset:47616\n"
    "v_mfma_f32_32x32x16_bf16 v[144:159], v[196:199], v[80:83], v[144:159]\n"
    "s_waitcnt lgkmcnt(8)\n"
    "ds_read_b64_tr_b16 v[220:221], v233 offset:47104\n"
    "ds_read_b64_tr_b16 v[222:223], v233 offset:47616\n"
    "v_mfma_f32_32x32x16_bf16 v[128:143], v[200:203], v[84:87], v[128:143]\n"
    "s_waitcnt lgkmcnt(8)\n"
    "v_mfma_f32_32x32x16_bf16 v[144:159], v[204:207], v[84:87], v[144:159]\n"
    "s_waitcnt lgkmcnt(6)\n"
    "v_mfma_f32_32x32x16_bf16 v[128:143], v[208:211], v[88:91], v[128:143]\n"
    "s_waitcnt lgkmcnt(0)\n"
    "s_barrier\n"
    "v_mfma_f32_32x32x16_bf16 v[144:159], v[212:215], v[88:91], v[144:159]\n"
    "ds_read_b64_tr_b16 v[192:193], v232 offset:49152\n"
    "ds_read_b64_tr_b16 v[194:195], v232 offset:49664\n"
    "ds_read_b64_tr_b16 v[196:197], v233 offset:49152\n"
    "ds_read_b64_tr_b16 v[198:199], v233 offset:49664\n"
    "s_add_u32 s85, s87, 0\n"
    "buffer_load_dwordx4 v[0:3], v238, s[44:47], s85 offen offset:0 sc1\n"
    "v_mfma_f32_32x32x16_bf16 v[128:143], v[216:219], v[92:95], v[128:143]\n"
    "ds_read_b64_tr_b16 v[200:201], v232 offset:51200\n"
    "ds_read_b64_tr_b16 v[202:203], v232 offset:51712\n"
    "ds_read_b64_tr_b16 v[204:205], v233 offset:51200\n"
    "ds_read_b64_tr_b16 v[206:207], v233 offset:51712\n"
    "buffer_load_dwordx4 v[4:7], v238, s[44:47], s85 offen offset:1024 sc1\n"
    "v_mfma_f32_32x32x16_bf16 v[144:159], v[220:223], v[92:95], v[144:159]\n"
    "s_waitcnt lgkmcnt(6)\n"
    "ds_read_b64_tr_b16 v[208:209], v232 offset:53248\n"
    "ds_read_b64_tr_b16 v[210:211], v232 offset:53760\n"
    "ds_read_b64_tr_b16 v[212:213], v233 offset:53248\n"
    "ds_read_b64_tr_b16 v[214:215], v233 offset:53760\n"
    "buffer_load_dwordx4 v[8:11], v238, s[44:47], s85 offen offset:2048 sc1\n"
    "v_mfma_f32_32x32x16_bf16 v[128:143], v[192:195], v[96:99], v[128:143]\n"
    "s_waitcnt lgkmcnt(8)\n"
    "ds_read_b64_tr_b16 v[216:217], v232 offset:55296\n"
    "ds_read_b64_tr_b16 v[218:219], v232 offset:55808\n"
    "buffer_load_dwordx4 v[12:15], v238, s[44:47], s85 offen offset:3072 sc1\n"
    "s_waitcnt vmcnt(4)\n"
    "v_mov_b32 v224, v230\n"
    "v_mfma_f32_32x32x16_bf16 v[144:159], v[196:199], v[96:99], v[144:159]\n"
    "s_waitcnt lgkmcnt(8)\n"
    "ds_read_b64_tr_b16 v[220:221], v233 offset:55296\n"
    "ds_read_b64_tr_b16 v[222:223], v233 offset:55808\n"
    "s_add_u32 s85, s87, 4096\n"
    "buffer_load_dwordx4 v[16:19], v238, s[44:47], s85 offen offset:0 sc1\n"
    "v_mov_b32 v225, 0\n"
    "v_mfma_f32_32x32x16_bf16 v[128:143], v[200:203], v[100:103], v[128:143]\n"
    "s_waitcnt lgkmcnt(8)\n"
    "ds_read_b64_tr_b16 v[192:193], v232 offset:57344\n"
    "ds_read_b64_tr_b16 v[194:195], v232 offset:57856\n"
    "buffer_load_dwordx4 v[20:23], v238, s[44:47], s85 offen offset:1024 sc1\n"
    "v_mov_b32 v226, 0\n"
    "v_mfma_f32_32x32x16_bf16 v[144:159], v[204:207], v[100:103], v[144:159]\n"
    "s_waitcnt lgkmcnt(8)\n"
    "ds_read_b64_tr_b16 v[196:197], v233 offset:57344\n"
    "ds_read_b64_tr_b16 v[198:199], v233 offset:57856\n"
    "buffer_load_dwordx4 v[24:27], v238, s[44:47], s85 offen offset:2048 sc1\n"
    "v_mov_b32 v227, 0\n"
    "v_mfma_f32_32x32x16_bf16 v[128:143], v[208:211], v[104:107], v[128:143]\n"
    "s_waitcnt lgkmcnt(8)\n"
    "ds_read_b64_tr_b16 v[200:201], v232 offset:59392\n"
    "ds_read_b64_tr_b16 v[202:203], v232 offset:59904\n"
    "buffer_load_dwordx4 v[28:31], v238, s[44:47], s85 offen offset:3072 sc1\n"
    "v_mfma_f32_32x32x16_bf16 v[144:159], v[212:215], v[104:107], v[144:159]\n"
    "s_waitcnt lgkmcnt(8)\n"
    "ds_read_b64_tr_b16 v[204:205], v233 offset:59392\n"
    "ds_read_b64_tr_b16 v[206:207], v233 offset:59904\n"
    "s_add_u32 s85, s87, 8192\n"
    "buffer_load_dwordx4 v[32:35], v238, s[44:47], s85 offen offset:0 sc1\n"
    "v_mfma_f32_32x32x16_bf16 v[128:143], v[216:219], v[108:111], v[128:143]\n"
    "s_waitcnt lgkmcnt(8)\n"
    "ds_read_b64_tr_b16 v[208:209], v232 offset:61440\n"
    "ds_read_b64_tr_b16 v[210:211], v232 offset:61952\n"
    "buffer_load_dwordx4 v[36:39], v238, s[44:47], s85 offen offset:1024 sc1\n"
    "v_mfma_f32_32x32x16_bf16 v[144:159], v[220:223], v[108:111], v[144:159]\n"
    "s_waitcnt lgkmcnt(8)\n"
    "ds_read_b64_tr_b16 v[212:213], v233 offset:61440\n"
    "ds_read_b64_tr_b16 v[214:215], v233 offset:61952\n"
    "buffer_load_dwordx4 v[40:43], v238, s[44:47], s85 offen offset:2048 sc1\n"
    "v_mfma_f32_32x32x16_bf16 v[128:143], v[192:195], v[112:115], v[128:143]\n"
    "s_waitcnt lgkmcnt(8)\n"
    "ds_read_b64_tr_b16 v[216:217], v232 offset:63488\n"
    "ds_read_b64_tr_b16 v[218:219], v232 offset:64000\n"
    "buffer_load_dwordx4 v[44:47], v238, s[44:47], s85 offen offset:3072 sc1\n"
    "v_mfma_f32_32x32x16_bf16 v[144:159], v[196:199], v[112:115], v[144:159]\n"
    "s_waitcnt lgkmcnt(8)\n"
    "ds_read_b64_tr_b16 v[220:221], v233 offset:63488\n"
    "ds_read_b64_tr_b16 v[222:223], v233 offset:64000\n"
    "v_mfma_f32_32x32x16_bf16 v[128:143], v[200:203], v[116:119], v[128:143]\n"
    "s_waitcnt lgkmcnt(8)\n"
    "v_mfma_f32_32x32x16_bf16 v[144:159], v[204:207], v[116:119], v[144:159]\n"
    "s_waitcnt lgkmcnt(6)\n"
    "v_mfma_f32_32x32x16_bf16 v[128:143], v[208:211], v[120:123], v[128:143]\n"
    "s_waitcnt lgkmcnt(0)\n"
    "s_barrier\n"
    "v_mfma_f32_32x32x16_bf16 v[144:159], v[212:215], v[120:123], v[144:159]\n"
    "v_mov_b32 v232, v228\n"
    "v_add_u32 v234, 0x10000, v228\n"
    "v_add_u32 v236, 0x20000, v228\n"
    "v_xor_b32 v233, 64, v232\n"
    "v_xor_b32 v235, 64, v234\n"
    "v_xor_b32 v237, 64, v236\n"
    "ds_read_b64 v[240:241], v248 offset:1024\n"
    "ds_read_b64 v[242:243], v248 offset:1280\n"
    "s_setprio 3\n"
    "v_mov_b32 v228, 0x3f80\n"
    "v_cmp_gt_u32 vcc, 32, v255\n"
    "v_mov_b32 v229, 0\n"
    "v_mov_b32 v230, 0\n"
    "v_mov_b32 v231, 0\n"
    "v_cndmask_b32 v228, 0, v228, vcc\n"
    "s_nop 1\n"
    "v_mfma_f32_32x32x16_bf16 v[64:79], v[224:227], v[228:231], 0\n"
    "ds_read_b64_tr_b16 v[192:193], v234 offset:0\n"
    "ds_read_b64_tr_b16 v[194:195], v234 offset:1024\n"
    "ds_read_b64_tr_b16 v[196:197], v235 offset:0\n"
    "ds_read_b64_tr_b16 v[198:199], v235 offset:1024\n"
    "v_mfma_f32_32x32x16_bf16 v[128:143], v[216:219], v[124:127], v[128:143]\n"
    "ds_read_b64_tr_b16 v[200:201], v234 offset:2048\n"
    "ds_read_b64_tr_b16 v[202:203], v234 offset:3072\n"
    "ds_read_b64_tr_b16 v[204:205], v235 offset:2048\n"
    "ds_read_b64_tr_b16 v[206:207], v235 offset:3072\n"
    "v_mfma_f32_32x32x16_bf16 v[144:159], v[220:223], v[124:127], v[144:159]\n"
    "s_waitcnt vmcnt(11) lgkmcnt(6)\n"
    "ds_read_b64_tr_b16 v[208:209], v234 offset:4096\n"
    "ds_read_b64_tr_b16 v[210:211], v234 offset:5120\n"
    "ds_read_b64_tr_b16 v[212:213], v235 offset:4096\n"
    "ds_read_b64_tr_b16 v[214:215], v235 offset:5120\n"
    "v_mfma_f32_32x32x16_bf16 v[128:143], v[240:243], v[244:247], v[128:143]\n"
    "v_mfma_f32_32x32x16_bf16 v[144:159], v[242:245], v[244:247], v[144:159]\n"
    "v_mfma_f32_32x32x16_bf16 v[64:79], v[192:195], v[0:3], v[64:79]\n"
    "s_waitcnt lgkmcnt(8)\n"
    "ds_read_b64_tr_b16 v[216:217], v234 offset:6144\n"
    "ds_read_b64_tr_b16 v[218:219], v234 offset:7168\n"
    "v_mfma_f32_32x32x16_bf16 v[80:95], v[196:199], v[0:3], 0\n"
    "s_waitcnt vmcnt(10) lgkmcnt(8)\n"
    "ds_read_b64_tr_b16 v[220:221], v235 offset:6144\n"
    "ds_read_b64_tr_b16 v[222:223], v235 offset:7168\n"
    "v_mfma_f32_32x32x16_bf16 v[64:79], v[200:203], v[4:7], v[64:79]\n"
    "s_waitcnt lgkmcnt(8)\n"
    "s_nop 0\n"
    "v_mul_f32 v128, v239, v128\n"
    "v_mul_f32 v129, v239, v129\n"
    "v_mul_f32 v130, v239, v130\n"
    "v_mul_f32 v131, v239, v131\n"
    "v_mul_f32 v132, v239, v132\n"
    "v_mul_f32 v133, v239, v133\n"
    "v_mul_f32 v134, v239, v134\n"
    "v_mul_f32 v135, v239, v135\n"
    "v_mul_f32 v136, v239, v136\n"
    "v_mul_f32 v137, v239, v137\n"
    "v_mul_f32 v138, v239, v138\n"
    "v_mul_f32 v139, v239, v139\n"
    "v_mul_f32 v140, v239, v140\n"
    "v_mul_f32 v141, v239, v141\n"
    "v_mul_f32 v142, v239, v142\n"
    "v_mul_f32 v143, v239, v143\n"
    "v_cvt_pk_bf16_f32 v128, v128, v129\n"
    "v_cvt_pk_bf16_f32 v129, v130, v131\n"
    "v_cvt_pk_bf16_f32 v130, v132, v133\n"
    "v_cvt_pk_bf16_f32 v131, v134, v135\n"
    "v_cvt_pk_bf16_f32 v132, v136, v137\n"
    "v_cvt_pk_bf16_f32 v133, v138, v139\n"
    "v_cvt_pk_bf16_f32 v134, v140, v141\n"
    "v_cvt_pk_bf16_f32 v135, v142, v143\n"
    "v_mfma_f32_32x32x16_bf16 v[80:95], v[204:207], v[4:7], v[80:95]\n"
    "s_waitcnt vmcnt(9) lgkmcnt(6)\n"
    "v_mul_f32 v144, v239, v144\n"
    "v_mul_f32 v145, v239, v145\n"
    "v_mul_f32 v146, v239, v146\n"
    "v_mul_f32 v147, v239, v147\n"
    "v_mul_f32 v148, v239, v148\n"
    "v_mul_f32 v149, v239, v149\n"
    "v_mul_f32 v150, v239, v150\n"
    "v_mul_f32 v151, v239, v151\n"
    "v_mul_f32 v152, v239, v152\n"
    "v_mul_f32 v153, v239, v153\n"
    "v_mul_f32 v154, v239, v154\n"
    "v_mul_f32 v155, v239, v155\n"
    "v_mul_f32 v156, v239, v156\n"
    "v_mul_f32 v157, v239, v157\n"
    "v_mul_f32 v158, v239, v158\n"
    "v_mul_f32 v159, v239, v159\n"
    "v_cvt_pk_bf16_f32 v144, v144, v145\n"
    "v_cvt_pk_bf16_f32 v145, v146, v147\n"
    "v_cvt_pk_bf16_f32 v146, v148, v149\n"
    "v_cvt_pk_bf16_f32 v147, v150, v151\n"
    "v_cvt_pk_bf16_f32 v148, v152, v153\n"
    "v_cvt_pk_bf16_f32 v149, v154, v155\n"
    "v_cvt_pk_bf16_f32 v150, v156, v157\n"
    "v_cvt_pk_bf16_f32 v151, v158, v159\n"
    "v_mfma_f32_32x32x16_bf16 v[64:79], v[208:211], v[8:11], v[64:79]\n"
    "s_waitcnt lgkmcnt(0)\n"
    "s_barrier\n"
    "v_mfma_f32_32x32x16_bf16 v[80:95], v[212:215], v[8:11], v[80:95]\n"
    "s_waitcnt vmcnt(8)\n"
    "ds_read_b64_tr_b16 v[192:193], v234 offset:16384\n"
    "ds_read_b64_tr_b16 v[194:195], v234 offset:17408\n"
    "ds_read_b64_tr_b16 v[196:197], v235 offset:16384\n"
    "ds_read_b64_tr_b16 v[198:199], v235 offset:17408\n"
    "ds_write_b64 v254, v[128:129] offset:0\n"
    "s_add_u32 s85, s87, 12288\n"
    "buffer_load_dwordx4 v[48:51], v238, s[44:47], s85 offen offset:0 sc1\n"
    "v_mfma_f32_32x32x16_bf16 v[64:79], v[216:219], v[12:15], v[64:79]\n"
    "ds_read_b64_tr_b16 v[200:201], v234 offset:18432\n"
    "ds_read_b64_tr_b16 v[202:203], v234 offset:19456\n"
    "ds_read_b64_tr_b16 v[204:205], v235 offset:18432\n"
    "ds_read_b64_tr_b16 v[206:207], v235 offset:19456\n"
    "ds_write_b64 v254, v[130:131] offset:16\n"
    "buffer_load_dwordx4 v[52:55], v238, s[44:47], s85 offen offset:1024 sc1\n"
    "v_mfma_f32_32x32x16_bf16 v[80:95], v[220:223], v[12:15], v[80:95]\n"
    "s_waitcnt vmcnt(9) lgkmcnt(8)\n"
    "ds_read_b64_tr_b16 v[208:209], v234 offset:20480\n"
    "ds_read_b64_tr_b16 v[210:211], v234 offset:21504\n"
    "ds_read_b64_tr_b16 v[212:213], v235 offset:20480\n"
    "ds_read_b64_tr_b16 v[214:215], v235 offset:21504\n"
    "ds_write_b64 v254, v[132:133] offset:32\n"
    "buffer_load_dwordx4 v[56:59], v238, s[44:47], s85 offen offset:2048 sc1\n"
    "v_mfma_f32_32x32x16_bf16 v[64:79], v[192:195], v[16:19], v[64:79]\n"
    "s_waitcnt lgkmcnt(11)\n"
    "ds_read_b64_tr_b16 v[216:217], v234 offset:22528\n"
    "ds_read_b64_tr_b16 v[218:219], v234 offset:23552\n"
    "ds_write_b64 v254, v[134:135] offset:48\n"
    "buffer_load_dwordx4 v[60:63], v238, s[44:47], s85 offen offset:3072 sc1\n"
    "v_mfma_f32_32x32x16_bf16 v[80:95], v[196:199], v[16:19], v[80:95]\n"
    "s_waitcnt vmcnt(10) lgkmcnt(11)\n"
    "ds_read_b64_tr_b16 v[220:221], v235 offset:22528\n"
    "ds_read_b64_tr_b16 v[222:223], v235 offset:23552\n"
    "ds_write_b64 v254, v[144:145] offset:64\n"
    "v_mfma_f32_32x32x16_bf16 v[64:79], v[200:203], v[20:23], v[64:79]\n"
    "s_waitcnt lgkmcnt(12)\n"
    "ds_write_b64 v254, v[146:147] offset:80\n"
    "v_mfma_f32_32x32x16_bf16 v[80:95], v[204:207], v[20:23], v[80:95]\n"
    "s_waitcnt vmcnt(9) lgkmcnt(10)\n"
    "ds_write_b64 v254, v[148:149] offset:96\n"
    "v_mfma_f32_32x32x16_bf16 v[64:79], v[208:211], v[24:27], v[64:79]\n"
    "ds_write_b64 v254, v[150:151] offset:112\n"
    "s_waitcnt lgkmcnt(0)\n"
    "s_barrier\n"
    "v_mfma_f32_32x32x16_bf16 v[80:95], v[212:215], v[24:27], v[80:95]\n"
    "s_waitcnt vmcnt(8)\n"
    "ds_read_b64_tr_b16 v[192:193], v234 offset:32768\n"
    "ds_read_b64_tr_b16 v[194:195], v234 offset:33792\n"
    "ds_read_b64_tr_b16 v[196:197], v235 offset:32768\n"
    "ds_read_b64_tr_b16 v[198:199], v235 offset:33792\n"
    "s_add_u32 s85, s87, 16384\n"
    "buffer_load_dwordx4 v[0:3], v238, s[44:47], s85 offen offset:0 sc1\n"
    "v_mfma_f32_32x32x16_bf16 v[64:79], v[216:219], v[28:31], v[64:79]\n"
    "ds_read_b64_tr_b16 v[200:201], v234 offset:34816\n"
    "ds_read_b64_tr_b16 v[202:203], v234 offset:35840\n"
    "ds_read_b64_tr_b16 v[204:205], v235 offset:34816\n"
    "ds_read_b64_tr_b16 v[206:207], v235 offset:35840\n"
    "buffer_load_dwordx4 v[4:7], v238, s[44:47], s85 offen offset:1024 sc1\n"
    "v_mfma_f32_32x32x16_bf16 v[80:95], v[220:223], v[28:31], v[80:95]\n"
    "s_waitcnt vmcnt(9) lgkmcnt(6)\n"
    "ds_read_b64_tr_b16 v[208:209], v234 offset:36864\n"
    "ds_read_b64_tr_b16 v[210:211], v234 offset:37888\n"
    "ds_read_b64_tr_b16 v[212:213], v235 offset:36864\n"
    "ds_read_b64_tr_b16 v[214:215], v235 offset:37888\n"
    "buffer_load_dwordx4 v[8:11], v238, s[44:47], s85 offen offset:2048 sc1\n"
    "v_mfma_f32_32x32x16_bf16 v[64:79], v[192:195], v[32:35], v[64:79]\n"
    "s_waitcnt lgkmcnt(8)\n"
    "ds_read_b64_tr_b16 v[216:217], v234 offset:38912\n"
    "ds_read_b64_tr_b16 v[218:219], v234 offset:39936\n"
    "buffer_load_dwordx4 v[12:15], v238, s[44:47], s85 offen offset:3072 sc1\n"
    "v_mfma_f32_32x32x16_bf16 v[80:95], v[196:199], v[32:35], v[80:95]\n"
    "s_waitcnt vmcnt(10) lgkmcnt(8)\n"
    "ds_read_b64_tr_b16 v[220:221], v235 offset:38912\n"
    "ds_read_b64_tr_b16 v[222:223], v235 offset:39936\n"
    "v_mfma_f32_32x32x16_bf16 v[64:79], v[200:203], v[36:39], v[64:79]\n"
    "s_waitcnt lgkmcnt(8)\n"
    "v_mfma_f32_32x32x16_bf16 v[80:95], v[204:207], v[36:39], v[80:95]\n"
    "s_waitcnt vmcnt(9) lgkmcnt(6)\n"
    "v_mfma_f32_32x32x16_bf16 v[64:79], v[208:211], v[40:43], v[64:79]\n"
    "s_waitcnt lgkmcnt(0)\n"
    "s_barrier\n"
    "v_mfma_f32_32x32x16_bf16 v[80:95], v[212:215], v[40:43], v[80:95]\n"
    "s_waitcnt vmcnt(8)\n"
    "ds_read_b64_tr_b16 v[192:193], v232 offset:0\n"
    "ds_read_b64_tr_b16 v[194:195], v232 offset:1024\n"
    "ds_read_b64_tr_b16 v[196:197], v233 offset:0\n"
    "ds_read_b64_tr_b16 v[198:199], v233 offset:1024\n"
    "s_add_u32 s85, s87, 20480\n"
    "buffer_load_dwordx4 v[16:19], v238, s[44:47], s85 offen offset:0 sc1\n"
    "v_mfma_f32_32x32x16_bf16 v[64:79], v[216:219], v[44:47], v[64:79]\n"
    "ds_read_b64_tr_b16 v[200:201], v232 offset:2048\n"
    "ds_read_b64_tr_b16 v[202:203], v232 offset:3072\n"
    "ds_read_b64_tr_b16 v[204:205], v233 offset:2048\n"
    "ds_read_b64_tr_b16 v[206:207], v233 offset:3072\n"
    "buffer_load_dwordx4 v[20:23], v238, s[44:47], s85 offen offset:1024 sc1\n"
    "v_mfma_f32_32x32x16_bf16 v[80:95], v[220:223], v[44:47], v[80:95]\n"
    "s_waitcnt vmcnt(9) lgkmcnt(6)\n"
    "ds_read_b64_tr_b16 v[208:209], v232 offset:4096\n"
    "ds_read_b64_tr_b16 v[210:211], v232 offset:5120\n"
    "ds_read_b64_tr_b16 v[212:213], v233 offset:4096\n"
    "ds_read_b64_tr_b16 v[214:215], v233 offset:5120\n"
    "buffer_load_dwordx4 v[24:27], v238, s[44:47], s85 offen offset:2048 sc1\n"
    "v_mfma_f32_32x32x16_bf16 v[64:79], v[192:195], v[48:51], v[64:79]\n"
    "s_waitcnt lgkmcnt(8)\n"
    "ds_read_b64_tr_b16 v[216:217], v232 offset:6144\n"
    "ds_read_b64_tr_b16 v[218:219], v232 offset:7168\n"
    "buffer_load_dwordx4 v[28:31], v238, s[44:47], s85 offen offset:3072 sc1\n"
    "v_mfma_f32_32x32x16_bf16 v[80:95], v[196:199], v[48:51], v[80:95]\n"
    "s_waitcnt vmcnt(10) lgkmcnt(8)\n"
    "ds_read_b64_tr_b16 v[220:221], v233 offset:6144\n"
    "ds_read_b64_tr_b16 v[222:223], v233 offset:7168\n"
    "v_mfma_f32_32x32x16_bf16 v[64:79], v[200:203], v[52:55], v[64:79]\n"
    "s_waitcnt lgkmcnt(8)\n"
    "v_mfma_f32_32x32x16_bf16 v[80:95], v[204:207], v[52:55], v[80:95]\n"
    "s_waitcnt vmcnt(9) lgkmcnt(6)\n"
    "v_mfma_f32_32x32x16_bf16 v[64:79], v[208:211], v[56:59], v[64:79]\n"
    "s_waitcnt lgkmcnt(0)\n"
    "s_barrier\n"
    "v_mfma_f32_32x32x16_bf16 v[80:95], v[212:215], v[56:59], v[80:95]\n"
    "s_waitcnt vmcnt(8)\n"
    "ds_read_b64_tr_b16 v[192:193], v234 offset:49152\n"
    "ds_read_b64_tr_b16 v[194:195], v234 offset:50176\n"
    "ds_read_b64_tr_b16 v[196:197], v235 offset:49152\n"
    "ds_read_b64_tr_b16 v[198:199], v235 offset:50176\n"
    "s_add_u32 s85, s87, 24576\n"
    "buffer_load_dwordx4 v[32:35], v238, s[44:47], s85 offen offset:0 sc1\n"
    "v_mfma_f32_32x32x16_bf16 v[64:79], v[216:219], v[60:63], v[64:79]\n"
    "ds_read_b64_tr_b16 v[200:201], v234 offset:51200\n"
    "ds_read_b64_tr_b16 v[202:203], v234 offset:52224\n"
    "ds_read_b64_tr_b16 v[204:205], v235 offset:51200\n"
    "ds_read_b64_tr_b16 v[206:207], v235 offset:52224\n"
    "buffer_load_dwordx4 v[36:39], v238, s[44:47], s85 offen offset:1024 sc1\n"
    "v_mfma_f32_32x32x16_bf16 v[80:95], v[220:223], v[60:63], v[80:95]\n"
    "s_waitcnt vmcnt(9) lgkmcnt(6)\n"
    "ds_read_b64_tr_b16 v[208:209], v234 offset:53248\n"
    "ds_read_b64_tr_b16 v[210:211], v234 offset:54272\n"
    "ds_read_b64_tr_b16 v[212:213], v235 offset:53248\n"
    "ds_read_b64_tr_b16 v[214:215], v235 offset:54272\n"
    "buffer_load_dwordx4 v[40:43], v238, s[44:47], s85 offen offset:2048 sc1\n"
    "v_mfma_f32_32x32x16_bf16 v[64:79], v[192:195], v[0:3], v[64:79]\n"
    "s_waitcnt lgkmcnt(8)\n"
    "ds_read_b64_tr_b16 v[216:217], v234 offset:55296\n"
    "ds_read_b64_tr_b16 v[218:219], v234 offset:56320\n"
    "buffer_load_dwordx4 v[44:47], v238, s[44:47], s85 offen offset:3072 sc1\n"
    "v_mfma_f32_32x32x16_bf16 v[80:95], v[196:199], v[0:3], v[80:95]\n"
    "s_waitcnt vmcnt(10) lgkmcnt(8)\n"
    "ds_read_b64_tr_b16 v[220:221], v235 offset:55296\n"
    "ds_read_b64_tr_b16 v[222:223], v235 offset:56320\n"
    "v_mfma_f32_32x32x16_bf16 v[64:79], v[200:203], v[4:7], v[64:79]\n"
    "s_waitcnt lgkmcnt(8)\n"
    "v_mfma_f32_32x32x16_bf16 v[80:95], v[204:207], v[4:7], v[80:95]\n"
    "s_waitcnt vmcnt(9) lgkmcnt(6)\n"
    "v_mfma_f32_32x32x16_bf16 v[64:79], v[208:211], v[8:11], v[64:79]\n"
    "s_waitcnt lgkmcnt(0)\n"
    "s_barrier\n"
    "v_mfma_f32_32x32x16_bf16 v[80:95], v[212:215], v[8:11], v[80:95]\n"
    "s_waitcnt vmcnt(8)\n"
    "ds_read_b64_tr_b16 v[192:193], v232 offset:16384\n"
    "ds_read_b64_tr_b16 v[194:195], v232 offset:17408\n"
    "ds_read_b64_tr_b16 v[196:197], v233 offset:16384\n"
    "ds_read_b64_tr_b16 v[198:199], v233 offset:17408\n"
    "s_add_u32 s85, s87, 28672\n"
    "buffer_load_dwordx4 v[48:51], v238, s[44:47], s85 offen offset:0 sc1\n"
    "v_mfma_f32_32x32x16_bf16 v[64:79], v[216:219], v[12:15], v[64:79]\n"
    "ds_read_b64_tr_b16 v[200:201], v232 offset:18432\n"
    "ds_read_b64_tr_b16 v[202:203], v232 offset:19456\n"
    "ds_read_b64_tr_b16 v[204:205], v233 offset:18432\n"
    "ds_read_b64_tr_b16 v[206:207], v233 offset:19456\n"
    "buffer_load_dwordx4 v[52:55], v238, s[44:47], s85 offen offset:1024 sc1\n"
    "v_mfma_f32_32x32x16_bf16 v[80:95], v[220:223], v[12:15], v[80:95]\n"
    "s_waitcnt vmcnt(9) lgkmcnt(6)\n"
    "ds_read_b64_tr_b16 v[208:209], v232 offset:20480\n"
    "ds_read_b64_tr_b16 v[210:211], v232 offset:21504\n"
    "ds_read_b64_tr_b16 v[212:213], v233 offset:20480\n"
    "ds_read_b64_tr_b16 v[214:215], v233 offset:21504\n"
    "buffer_load_dwordx4 v[56:59], v238, s[44:47], s85 offen offset:2048 sc1\n"
    "v_mfma_f32_32x32x16_bf16 v[64:79], v[192:195], v[16:19], v[64:79]\n"
    "s_waitcnt lgkmcnt(8)\n"
    "ds_read_b64_tr_b16 v[216:217], v232 offset:22528\n"
    "ds_read_b64_tr_b16 v[218:219], v232 offset:23552\n"
    "buffer_load_dwordx4 v[60:63], v238, s[44:47], s85 offen offset:3072 sc1\n"
    "v_mfma_f32_32x32x16_bf16 v[80:95], v[196:199], v[16:19], v[80:95]\n"
    "s_waitcnt vmcnt(10) lgkmcnt(8)\n"
    "ds_read_b64_tr_b16 v[220:221], v233 offset:22528\n"
    "ds_read_b64_tr_b16 v[222:223], v233 offset:23552\n"
    "v_mfma_f32_32x32x16_bf16 v[64:79], v[200:203], v[20:23], v[64:79]\n"
    "s_waitcnt lgkmcnt(8)\n"
    "v_mfma_f32_32x32x16_bf16 v[80:95], v[204:207], v[20:23], v[80:95]\n"
    "s_waitcnt vmcnt(9) lgkmcnt(6)\n"
    "v_mfma_f32_32x32x16_bf16 v[64:79], v[208:211], v[24:27], v[64:79]\n"
    "s_waitcnt lgkmcnt(0)\n"
    "s_barrier\n"
    "v_mfma_f32_32x32x16_bf16 v[80:95], v[212:215], v[24:27], v[80:95]\n"
    "s_waitcnt vmcnt(8)\n"
    "ds_read_b64_tr_b16 v[192:193], v232 offset:32768\n"
    "ds_read_b64_tr_b16 v[194:195], v232 offset:33792\n"
    "ds_read_b64_tr_b16 v[196:197], v233 offset:32768\n"
    "ds_read_b64_tr_b16 v[198:199], v233 offset:33792\n"
    "s_add_u32 s85, s87, 32768\n"
    "buffer_load_dwordx4 v[0:3], v238, s[44:47], s85 offen offset:0 sc1\n"
    "v_mfma_f32_32x32x16_bf16 v[64:79], v[216:219], v[28:31], v[64:79]\n"
    "ds_read_b64_tr_b16 v[200:201], v232 offset:34816\n"
    "ds_read_b64_tr_b16 v[202:203], v232 offset:35840\n"
    "ds_read_b64_tr_b16 v[204:205], v233 offset:34816\n"
    "ds_read_b64_tr_b16 v[206:207], v233 offset:35840\n"
    "buffer_load_dwordx4 v[4:7], v238, s[44:47], s85 offen offset:1024 sc1\n"
    "v_mfma_f32_32x32x16_bf16 v[80:95], v[220:223], v[28:31], v[80:95]\n"
    "s_waitcnt vmcnt(9) lgkmcnt(6)\n"
    "ds_read_b64_tr_b16 v[208:209], v232 offset:36864\n"
    "ds_read_b64_tr_b16 v[210:211], v232 offset:37888\n"
    "ds_read_b64_tr_b16 v[212:213], v233 offset:36864\n"
    "ds_read_b64_tr_b16 v[214:215], v233 offset:37888\n"
    "buffer_load_dwordx4 v[8:11], v238, s[44:47], s85 offen offset:2048 sc1\n"
    "v_mfma_f32_32x32x16_bf16 v[64:79], v[192:195], v[32:35], v[64:79]\n"
    "s_waitcnt lgkmcnt(8)\n"
    "ds_read_b64_tr_b16 v[216:217], v232 offset:38912\n"
    "ds_read_b64_tr_b16 v[218:219], v232 offset:39936\n"
    "buffer_load_dwordx4 v[12:15], v238, s[44:47], s85 offen offset:3072 sc1\n"
    "v_mfma_f32_32x32x16_bf16 v[80:95], v[196:199], v[32:35], v[80:95]\n"
    "s_waitcnt vmcnt(10) lgkmcnt(8)\n"
    "ds_read_b64_tr_b16 v[220:221], v233 offset:38912\n"
    "ds_read_b64_tr_b16 v[222:223], v233 offset:39936\n"
    "v_mfma_f32_32x32x16_bf16 v[64:79], v[200:203], v[36:39], v[64:79]\n"
    "s_waitcnt lgkmcnt(8)\n"
    "v_mfma_f32_32x32x16_bf16 v[80:95], v[204:207], v[36:39], v[80:95]\n"
    "s_waitcnt vmcnt(9) lgkmcnt(6)\n"
    "v_mfma_f32_32x32x16_bf16 v[64:79], v[208:211], v[40:43], v[64:79]\n"
    "s_waitcnt lgkmcnt(0)\n"
    "s_barrier\n"
    "v_mfma_f32_32x32x16_bf16 v[80:95], v[212:215], v[40:43], v[80:95]\n"
    "s_waitcnt vmcnt(8)\n"
    "ds_read_b64_tr_b16 v[192:193], v232 offset:49152\n"
    "ds_read_b64_tr_b16 v[194:195], v232 offset:50176\n"
    "ds_read_b64_tr_b16 v[196:197], v233 offset:49152\n"
    "ds_read_b64_tr_b16 v[198:199], v233 offset:50176\n"
    "s_add_u32 s85, s87, 36864\n"
    "buffer_load_dwordx4 v[16:19], v238, s[44:47], s85 offen offset:0 sc1\n"
    "v_mfma_f32_32x32x16_bf16 v[64:79], v[216:219], v[44:47], v[64:79]\n"
    "ds_read_b64_tr_b16 v[200:201], v232 offset:51200\n"
    "ds_read_b64_tr_b16 v[202:203], v232 offset:52224\n"
    "ds_read_b64_tr_b16 v[204:205], v233 offset:51200\n"
    "ds_read_b64_tr_b16 v[206:207], v233 offset:52224\n"
    "buffer_load_dwordx4 v[20:23], v238, s[44:47], s85 offen offset:1024 sc1\n"
    "v_mfma_f32_32x32x16_bf16 v[80:95], v[220:223], v[44:47], v[80:95]\n"
    "s_waitcnt vmcnt(9) lgkmcnt(6)\n"
    "ds_read_b64_tr_b16 v[208:209], v232 offset:53248\n"
    "ds_read_b64_tr_b16 v[210:211], v232 offset:54272\n"
    "ds_read_b64_tr_b16 v[212:213], v233 offset:53248\n"
    "ds_read_b64_tr_b16 v[214:215], v233 offset:54272\n"
    "buffer_load_dwordx4 v[24:27], v238, s[44:47], s85 offen offset:2048 sc1\n"
    "v_mfma_f32_32x32x16_bf16 v[64:79], v[192:195], v[48:51], v[64:79]\n"
    "s_waitcnt lgkmcnt(8)\n"
    "ds_read_b64_tr_b16 v[216:217], v232 offset:55296\n"
    "ds_read_b64_tr_b16 v[218:219], v232 offset:56320\n"
    "buffer_load_dwordx4 v[28:31], v238, s[44:47], s85 offen offset:3072 sc1\n"
    "v_mfma_f32_32x32x16_bf16 v[80:95], v[196:199], v[48:51], v[80:95]\n"
    "s_waitcnt vmcnt(10) lgkmcnt(8)\n"
    "ds_read_b64_tr_b16 v[220:221], v233 offset:55296\n"
    "ds_read_b64_tr_b16 v[222:223], v233 offset:56320\n"
    "v_mfma_f32_32x32x16_bf16 v[64:79], v[200:203], v[52:55], v[64:79]\n"
    "s_waitcnt lgkmcnt(8)\n"
    "v_mfma_f32_32x32x16_bf16 v[80:95], v[204:207], v[52:55], v[80:95]\n"
    "s_waitcnt vmcnt(9) lgkmcnt(6)\n"
    "v_mfma_f32_32x32x16_bf16 v[64:79], v[208:211], v[56:59], v[64:79]\n"
    "s_waitcnt lgkmcnt(0)\n"
    "s_barrier\n"
    "v_mfma_f32_32x32x16_bf16 v[80:95], v[212:215], v[56:59], v[80:95]\n"
    "s_waitcnt vmcnt(8)\n"
    "ds_read_b64_tr_b16 v[192:193], v234 offset:0\n"
    "ds_read_b64_tr_b16 v[194:195], v234 offset:1024\n"
    "ds_read_b64_tr_b16 v[196:197], v235 offset:0\n"
    "ds_read_b64_tr_b16 v[198:199], v235 offset:1024\n"
    "s_add_u32 s85, s87, 40960\n"
    "buffer_load_dwordx4 v[32:35], v238, s[44:47], s85 offen offset:0 sc1\n"
    "v_mfma_f32_32x32x16_bf16 v[64:79], v[216:219], v[60:63], v[64:79]\n"
    "ds_read_b64_tr_b16 v[200:201], v234 offset:2048\n"
    "ds_read_b64_tr_b16 v[202:203], v234 offset:3072\n"
    "ds_read_b64_tr_b16 v[204:205], v235 offset:2048\n"
    "ds_read_b64_tr_b16 v[206:207], v235 offset:3072\n"
    "buffer_load_dwordx4 v[36:39], v238, s[44:47], s85 offen offset:1024 sc1\n"
    "v_mfma_f32_32x32x16_bf16 v[80:95], v[220:223], v[60:63], v[80:95]\n"
    "s_waitcnt vmcnt(9) lgkmcnt(6)\n"
    "ds_read_b64_tr_b16 v[208:209], v234 offset:4096\n"
    "ds_read_b64_tr_b16 v[210:211], v234 offset:5120\n"
    "ds_read_b64_tr_b16 v[212:213], v235 offset:4096\n"
    "ds_read_b64_tr_b16 v[214:215], v235 offset:5120\n"
    "buffer_load_dwordx4 v[40:43], v238, s[44:47], s85 offen offset:2048 sc1\n"
    "v_mfma_f32_32x32x16_bf16 v[64:79], v[192:195], v[0:3], v[64:79]\n"
    "s_waitcnt lgkmcnt(8)\n"
    "ds_read_b64_tr_b16 v[216:217], v234 offset:6144\n"
    "ds_read_b64_tr_b16 v[218:219], v234 offset:7168\n"
    "buffer_load_dwordx4 v[44:47], v238, s[44:47], s85 offen offset:3072 sc1\n"
    "v_mfma_f32_32x32x16_bf16 v[80:95], v[196:199], v[0:3], v[80:95]\n"
    "s_waitcnt vmcnt(10) lgkmcnt(8)\n"
    "ds_read_b64_tr_b16 v[220:221], v235 offset:6144\n"
    "ds_read_b64_tr_b16 v[222:223], v235 offset:7168\n"
    "v_mfma_f32_32x32x16_bf16 v[64:79], v[200:203], v[4:7], v[64:79]\n"
    "s_waitcnt lgkmcnt(8)\n"
    "v_mfma_f32_32x32x16_bf16 v[80:95], v[204:207], v[4:7], v[80:95]\n"
    "s_waitcnt vmcnt(9) lgkmcnt(6)\n"
    "v_mfma_f32_32x32x16_bf16 v[64:79], v[208:211], v[8:11], v[64:79]\n"
    "s_waitcnt lgkmcnt(0)\n"
    "s_barrier\n"
    "v_mfma_f32_32x32x16_bf16 v[80:95], v[212:215], v[8:11], v[80:95]\n"
    "s_waitcnt vmcnt(8)\n"
    "ds_read_b64_tr_b16 v[192:193], v234 offset:16384\n"
    "ds_read_b64_tr_b16 v[194:195], v234 offset:17408\n"
    "ds_read_b64_tr_b16 v[196:197], v235 offset:16384\n"
    "ds_read_b64_tr_b16 v[198:199], v235 offset:17408\n"
    "s_add_u32 s85, s87, 45056\n"
    "buffer_load_dwordx4 v[48:51], v238, s[44:47], s85 offen offset:0 sc1\n"
    "v_and_b32 v96, 31, v255\n"
    "v_bfe_u32 v97, v255, 1, 3\n"
    "v_mfma_f32_32x32x16_bf16 v[64:79], v[216:219], v[12:15], v[64:79]\n"
    "ds_read_b64_tr_b16 v[200:201], v234 offset:18432\n"
    "ds_read_b64_tr_b16 v[202:203], v234 offset:19456\n"
    "ds_read_b64_tr_b16 v[204:205], v235 offset:18432\n"
    "ds_read_b64_tr_b16 v[206:207], v235 offset:19456\n"
    "buffer_load_dwordx4 v[52:55], v238, s[44:47], s85 offen offset:1024 sc1\n"
    "s_lshr_b32 s72, s70, 1\n"
    "s_lshl_b32 s72, s72, 2\n"
    "v_mfma_f32_32x32x16_bf16 v[80:95], v[220:223], v[12:15], v[80:95]\n"
    "s_waitcnt vmcnt(9) lgkmcnt(6)\n"
    "ds_read_b64_tr_b16 v[208:209], v234 offset:20480\n"
    "ds_read_b64_tr_b16 v[210:211], v234 offset:21504\n"
    "ds_read_b64_tr_b16 v[212:213], v235 offset:20480\n"
    "ds_read_b64_tr_b16 v[214:215], v235 offset:21504\n"
    "buffer_load_dwordx4 v[56:59], v238, s[44:47], s85 offen offset:2048 sc1\n"
    "v_xor_b32 v97, s72, v97\n"
    "v_lshlrev_b32 v98, 7, v96\n"
    "v_mfma_f32_32x32x16_bf16 v[64:79], v[192:195], v[16:19], v[64:79]\n"
    "s_waitcnt lgkmcnt(8)\n"
    "ds_read_b64_tr_b16 v[216:217], v234 offset:22528\n"
    "ds_read_b64_tr_b16 v[218:219], v234 offset:23552\n"
    "buffer_load_dwordx4 v[60:63], v238, s[44:47], s85 offen offset:3072 sc1\n"
    "v_lshl_add_u32 v98, v97, 4, v98\n"
    "v_lshrrev_b32 v99, 5, v255\n"
    "v_lshl_add_u32 v98, v99, 3, v98\n"
    "s_and_b32 s72, s70, 1\n"
    "v_mfma_f32_32x32x16_bf16 v[80:95], v[196:199], v[16:19], v[80:95]\n"
    "s_waitcnt vmcnt(10) lgkmcnt(8)\n"
    "ds_read_b64_tr_b16 v[220:221], v235 offset:22528\n"
    "ds_read_b64_tr_b16 v[222:223], v235 offset:23552\n"
    "s_lshl_b32 s72, s72, 12\n"
    "s_add_u32 s72, s72, 0x24000\n"
    "v_add_u32 v100, s72, v98\n"
    "v_xor_b32 v101, 16, v100\n"
    "v_mfma_f32_32x32x16_bf16 v[64:79], v[200:203], v[20:23], v[64:79]\n"
    "s_waitcnt lgkmcnt(8)\n"
    "v_xor_b32 v102, 32, v100\n"
    "v_xor_b32 v103, 48, v100\n"
    "ds_read_b64 v[104:105], v100\n"
    "ds_read_b64 v[106:107], v101\n"
    "v_mfma_f32_32x32x16_bf16 v[80:95], v[204:207], v[20:23], v[80:95]\n"
    "s_waitcnt vmcnt(9) lgkmcnt(8)\n"
    "ds_read_b64 v[108:109], v102\n"
    "ds_read_b64 v[110:111], v103\n"
    "s_waitcnt lgkmcnt(3)\n"
    "v_lshlrev_b32 v176, 16, v104\n"
    "v_and_b32 v177, 0xffff0000, v104\n"
    "v_mfma_f32_32x32x16_bf16 v[64:79], v[208:211], v[24:27], v[64:79]\n"
    "s_waitcnt lgkmcnt(0)\n"
    "s_barrier\n"
    "v_mfma_f32_32x32x16_bf16 v[80:95], v[212:215], v[24:27], v[80:95]\n"
    "s_waitcnt vmcnt(8)\n"
    "ds_read_b64_tr_b16 v[192:193], v234 offset:32768\n"
    "ds_read_b64_tr_b16 v[194:195], v234 offset:33792\n"
    "ds_read_b64_tr_b16 v[196:197], v235 offset:32768\n"
    "ds_read_b64_tr_b16 v[198:199], v235 offset:33792\n"
    "s_add_u32 s85, s87, 49152\n"
    "buffer_load_dwordx4 v[0:3], v238, s[44:47], s85 offen offset:0 sc1\n"
    "v_lshlrev_b32 v178, 16, v105\n"
    "v_and_b32 v179, 0xffff0000, v105\n"
    "v_mfma_f32_32x32x16_bf16 v[64:79], v[216:219], v[28:31], v[64:79]\n"
    "ds_read_b64_tr_b16 v[200:201], v234 offset:34816\n"
    "ds_read_b64_tr_b16 v[202:203], v234 offset:35840\n"
    "ds_read_b64_tr_b16 v[204:205], v235 offset:34816\n"
    "ds_read_b64_tr_b16 v[206:207], v235 offset:35840\n"
    "buffer_load_dwordx4 v[4:7], v238, s[44:47], s85 offen offset:1024 sc1\n"
    "v_lshlrev_b32 v180, 16, v106\n"
    "v_and_b32 v181, 0xffff0000, v106\n"
    "v_mfma_f32_32x32x16_bf16 v[80:95], v[220:223], v[28:31], v[80:95]\n"
    "s_waitcnt vmcnt(9) lgkmcnt(6)\n"
    "ds_read_b64_tr_b16 v[208:209], v234 offset:36864\n"
    "ds_read_b64_tr_b16 v[210:211], v234 offset:37888\n"
    "ds_read_b64_tr_b16 v[212:213], v235 offset:36864\n"
    "ds_read_b64_tr_b16 v[214:215], v235 offset:37888\n"
    "buffer_load_dwordx4 v[8:11], v238, s[44:47], s85 offen offset:2048 sc1\n"
    "v_lshlrev_b32 v182, 16, v107\n"
    "v_and_b32 v183, 0xffff0000, v107\n"
    "v_lshlrev_b32 v184, 16, v108\n"
    "v_and_b32 v185, 0xffff0000, v108\n"
    "v_lshlrev_b32 v186, 16, v109\n"
    "v_mfma_f32_32x32x16_bf16 v[64:79], v[192:195], v[32:35], v[64:79]\n"
    "s_waitcnt lgkmcnt(8)\n"
    "ds_read_b64_tr_b16 v[216:217], v234 offset:38912\n"
    "ds_read_b64_tr_b16 v[218:219], v234 offset:39936\n"
    "buffer_load_dwordx4 v[12:15], v238, s[44:47], s85 offen offset:3072 sc1\n"
    "v_and_b32 v187, 0xffff0000, v109\n"
    "v_lshlrev_b32 v188, 16, v110\n"
    "v_and_b32 v189, 0xffff0000, v110\n"
    "v_lshlrev_b32 v190, 16, v111\n"
    "v_mfma_f32_32x32x16_bf16 v[80:95], v[196:199], v[32:35], v[80:95]\n"
    "s_waitcnt vmcnt(10) lgkmcnt(8)\n"
    "ds_read_b64_tr_b16 v[220:221], v235 offset:38912\n"
    "ds_read_b64_tr_b16 v[222:223], v235 offset:39936\n"
    "v_and_b32 v191, 0xffff0000, v111\n"
    "v_lshlrev_b32 v96, 11, v96\n"
    "v_lshl_add_u32 v96, v99, 4, v96\n"
    "s_lshl_b32 s72, s68, 16\n"
    "v_mfma_f32_32x32x16_bf16 v[64:79], v[200:203], v[36:39], v[64:79]\n"
    "s_waitcnt lgkmcnt(8)\n"
    "s_and_b32 s73, s70, 1\n"
    "s_lshl_b32 s73, s73, 10\n"
    "s_add_u32 s72, s72, s73\n"
    "s_lshl_b32 s73, s69, 7\n"
    "v_mfma_f32_32x32x16_bf16 v[80:95], v[204:207], v[36:39], v[80:95]\n"
    "s_waitcnt vmcnt(9) lgkmcnt(6)\n"
    "s_add_u32 s72, s72, s73\n"
    "s_lshr_b32 s73, s70, 1\n"
    "s_lshl_b32 s73, s73, 6\n"
    "s_add_u32 s84, s72, s73\n"
    "v_mfma_f32_32x32x16_bf16 v[64:79], v[208:211], v[40:43], v[64:79]\n"
    "s_waitcnt lgkmcnt(0)\n"
    "s_barrier\n"
    "v_mfma_f32_32x32x16_bf16 v[80:95], v[212:215], v[40:43], v[80:95]\n"
    "s_waitcnt vmcnt(8)\n"
    "ds_read_b64_tr_b16 v[192:193], v236 offset:0\n"
    "ds_read_b64_tr_b16 v[194:195], v236 offset:1024\n"
    "ds_read_b64_tr_b16 v[196:197], v237 offset:0\n"
    "ds_read_b64_tr_b16 v[198:199], v237 offset:1024\n"
    "s_add_u32 s85, s87, 53248\n"
    "buffer_load_dwordx4 v[16:19], v238, s[44:47], s85 offen offset:0 sc1\n"
    "v_mfma_f32_32x32x16_bf16 v[64:79], v[216:219], v[44:47], v[64:79]\n"
    "ds_read_b64_tr_b16 v[200:201], v236 offset:2048\n"
    "ds_read_b64_tr_b16 v[202:203], v236 offset:3072\n"
    "ds_read_b64_tr_b16 v[204:205], v237 offset:2048\n"
    "ds_read_b64_tr_b16 v[206:207], v237 offset:3072\n"
    "buffer_load_dwordx4 v[20:23], v238, s[44:47], s85 offen offset:1024 sc1\n"
    "v_mfma_f32_32x32x16_bf16 v[80:95], v[220:223], v[44:47], v[80:95]\n"
    "s_waitcnt vmcnt(9) lgkmcnt(6)\n"
    "ds_read_b64_tr_b16 v[208:209], v236 offset:4096\n"
    "ds_read_b64_tr_b16 v[210:211], v236 offset:5120\n"
    "ds_read_b64_tr_b16 v[212:213], v237 offset:4096\n"
    "ds_read_b64_tr_b16 v[214:215], v237 offset:5120\n"
    "buffer_load_dwordx4 v[24:27], v238, s[44:47], s85 offen offset:2048 sc1\n"
    "v_mfma_f32_32x32x16_bf16 v[64:79], v[192:195], v[48:51], v[64:79]\n"
    "s_waitcnt lgkmcnt(8)\n"
    "ds_read_b64_tr_b16 v[216:217], v236 offset:6144\n"
    "ds_read_b64_tr_b16 v[218:219], v236 offset:7168\n"
    "buffer_load_dwordx4 v[28:31], v238, s[44:47], s85 offen offset:3072 sc1\n"
    "s_lshl_b32 s72, s70, 12\n"
    "v_mfma_f32_32x32x16_bf16 v[80:95], v[196:199], v[48:51], v[80:95]\n"
    "s_waitcnt vmcnt(10) lgkmcnt(8)\n"
    "ds_read_b64_tr_b16 v[220:221], v237 offset:6144\n"
    "ds_read_b64_tr_b16 v[222:223], v237 offset:7168\n"
    "s_xor_b32 s73, s72, 0x2000\n"
    "v_mfma_f32_32x32x16_bf16 v[64:79], v[200:203], v[52:55], v[64:79]\n"
    "s_waitcnt lgkmcnt(8)\n"
    "v_add_u32 v248, s72, v238\n"
    "v_mfma_f32_32x32x16_bf16 v[80:95], v[204:207], v[52:55], v[80:95]\n"
    "s_waitcnt vmcnt(9) lgkmcnt(6)\n"
    "v_add_u32 v249, s73, v238\n"
    "v_mfma_f32_32x32x16_bf16 v[64:79], v[208:211], v[56:59], v[64:79]\n"
    "s_waitcnt lgkmcnt(0)\n"
    "s_barrier\n"
    "v_mfma_f32_32x32x16_bf16 v[80:95], v[212:215], v[56:59], v[80:95]\n"
    "s_waitcnt vmcnt(8)\n"
    "ds_read_b64_tr_b16 v[192:193], v232 offset:0\n"
    "ds_read_b64_tr_b16 v[194:195], v232 offset:1024\n"
    "ds_read_b64_tr_b16 v[196:197], v233 offset:0\n"
    "ds_read_b64_tr_b16 v[198:199], v233 offset:1024\n"
    "s_add_u32 s85, s87, 57344\n"
    "buffer_load_dwordx4 v[32:35], v238, s[44:47], s85 offen offset:0 sc1\n"
    "v_mfma_f32_32x32x16_bf16 v[64:79], v[216:219], v[60:63], v[64:79]\n"
    "ds_read_b64_tr_b16 v[200:201], v232 offset:2048\n"
    "ds_read_b64_tr_b16 v[202:203], v232 offset:3072\n"
    "ds_read_b64_tr_b16 v[204:205], v233 offset:2048\n"
    "ds_read_b64_tr_b16 v[206:207], v233 offset:3072\n"
    "buffer_load_dwordx4 v[36:39], v238, s[44:47], s85 offen offset:1024 sc1\n"
    "v_mfma_f32_32x32x16_bf16 v[80:95], v[220:223], v[60:63], v[80:95]\n"
    "s_waitcnt vmcnt(9) lgkmcnt(6)\n"
    "ds_read_b64_tr_b16 v[208:209], v232 offset:4096\n"
    "ds_read_b64_tr_b16 v[210:211], v232 offset:5120\n"
    "ds_read_b64_tr_b16 v[212:213], v233 offset:4096\n"
    "ds_read_b64_tr_b16 v[214:215], v233 offset:5120\n"
    "buffer_load_dwordx4 v[40:43], v238, s[44:47], s85 offen offset:2048 sc1\n"
    "v_mfma_f32_32x32x16_bf16 v[64:79], v[192:195], v[0:3], v[64:79]\n"
    "s_waitcnt lgkmcnt(8)\n"
    "ds_read_b64_tr_b16 v[216:217], v232 offset:6144\n"
    "ds_read_b64_tr_b16 v[218:219], v232 offset:7168\n"
    "buffer_load_dwordx4 v[44:47], v238, s[44:47], s85 offen offset:3072 sc1\n"
    "v_add_u32 v252, 0x4000, v248\n"
    "v_mfma_f32_32x32x16_bf16 v[80:95], v[196:199], v[0:3], v[80:95]\n"
    "s_waitcnt vmcnt(10) lgkmcnt(8)\n"
    "ds_read_b64_tr_b16 v[220:221], v233 offset:6144\n"
    "ds_read_b64_tr_b16 v[222:223], v233 offset:7168\n"
    "v_add_u32 v253, 0x4000, v249\n"
    "v_mfma_f32_32x32x16_bf16 v[64:79], v[200:203], v[4:7], v[64:79]\n"
    "s_waitcnt lgkmcnt(8)\n"
    "v_add_u32 v248, 0x0, v248\n"
    "v_mfma_f32_32x32x16_bf16 v[80:95], v[204:207], v[4:7], v[80:95]\n"
    "s_waitcnt vmcnt(9) lgkmcnt(6)\n"
    "v_add_u32 v249, 0x0, v249\n"
    "v_mfma_f32_32x32x16_bf16 v[64:79], v[208:211], v[8:11], v[64:79]\n"
    "s_waitcnt lgkmcnt(0)\n"
    "s_barrier\n"
    "v_mfma_f32_32x32x16_bf16 v[80:95], v[212:215], v[8:11], v[80:95]\n"
    "s_waitcnt vmcnt(8)\n"
    "ds_read_b64_tr_b16 v[192:193], v234 offset:49152\n"
    "ds_read_b64_tr_b16 v[194:195], v234 offset:50176\n"
    "ds_read_b64_tr_b16 v[196:197], v235 offset:49152\n"
    "ds_read_b64_tr_b16 v[198:199], v235 offset:50176\n"
    "s_add_u32 s85, s87, 61440\n"
    "buffer_load_dwordx4 v[48:51], v238, s[44:47], s85 offen offset:0 sc1\n"
    "v_mfma_f32_32x32x16_bf16 v[64:79], v[216:219], v[12:15], v[64:79]\n"
    "ds_read_b64_tr_b16 v[200:201], v234 offset:51200\n"
    "ds_read_b64_tr_b16 v[202:203], v234 offset:52224\n"
    "ds_read_b64_tr_b16 v[204:205], v235 offset:51200\n"
    "ds_read_b64_tr_b16 v[206:207], v235 offset:52224\n"
    "buffer_load_dwordx4 v[52:55], v238, s[44:47], s85 offen offset:1024 sc1\n"
    "v_mfma_f32_32x32x16_bf16 v[80:95], v[220:223], v[12:15], v[80:95]\n"
    "s_waitcnt vmcnt(9) lgkmcnt(6)\n"
    "ds_read_b64_tr_b16 v[208:209], v234 offset:53248\n"
    "ds_read_b64_tr_b16 v[210:211], v234 offset:54272\n"
    "ds_read_b64_tr_b16 v[212:213], v235 offset:53248\n"
    "ds_read_b64_tr_b16 v[214:215], v235 offset:54272\n"
    "buffer_load_dwordx4 v[56:59], v238, s[44:47], s85 offen offset:2048 sc1\n"
    "v_mfma_f32_32x32x16_bf16 v[64:79], v[192:195], v[16:19], v[64:79]\n"
    "s_waitcnt lgkmcnt(8)\n"
    "ds_read_b64_tr_b16 v[216:217], v234 offset:55296\n"
    "ds_read_b64_tr_b16 v[218:219], v234 offset:56320\n"
    "buffer_load_dwordx4 v[60:63], v238, s[44:47], s85 offen offset:3072 sc1\n"
    "v_mfma_f32_32x32x16_bf16 v[80:95], v[196:199], v[16:19], v[80:95]\n"
    "s_waitcnt vmcnt(10) lgkmcnt(8)\n"
    "ds_read_b64_tr_b16 v[220:221], v235 offset:55296\n"
    "ds_read_b64_tr_b16 v[222:223], v235 offset:56320\n"
    "v_mfma_f32_32x32x16_bf16 v[64:79], v[200:203], v[20:23], v[64:79]\n"
    "s_waitcnt lgkmcnt(8)\n"
    "v_mfma_f32_32x32x16_bf16 v[80:95], v[204:207], v[20:23], v[80:95]\n"
    "s_waitcnt vmcnt(9) lgkmcnt(6)\n"
    "v_mfma_f32_32x32x16_bf16 v[64:79], v[208:211], v[24:27], v[64:79]\n"
    "s_waitcnt lgkmcnt(0)\n"
    "s_barrier\n"
    "v_mfma_f32_32x32x16_bf16 v[80:95], v[212:215], v[24:27], v[80:95]\n"
    "s_waitcnt vmcnt(8)\n"
    "ds_read_b64_tr_b16 v[192:193], v232 offset:16384\n"
    "ds_read_b64_tr_b16 v[194:195], v232 offset:20480\n"
    "ds_read_b64_tr_b16 v[196:197], v233 offset:16384\n"
    "ds_read_b64_tr_b16 v[198:199], v233 offset:20480\n"
    "s_add_u32 s85, s87, 65536\n"
    "buffer_load_dwordx4 v[0:3], v238, s[44:47], s85 offen offset:0 sc1\n"
    "v_mfma_f32_32x32x16_bf16 v[64:79], v[216:219], v[28:31], v[64:79]\n"
    "ds_read_b64_tr_b16 v[200:201], v232 offset:17408\n"
    "ds_read_b64_tr_b16 v[202:203], v232 offset:21504\n"
    "ds_read_b64_tr_b16 v[204:205], v233 offset:17408\n"
    "ds_read_b64_tr_b16 v[206:207], v233 offset:21504\n"
    "buffer_load_dwordx4 v[4:7], v238, s[44:47], s85 offen offset:1024 sc1\n"
    "v_mfma_f32_32x32x16_bf16 v[80:95], v[220:223], v[28:31], v[80:95]\n"
    "s_waitcnt vmcnt(9) lgkmcnt(6)\n"
    "ds_read_b64_tr_b16 v[208:209], v232 offset:18432\n"
    "ds_read_b64_tr_b16 v[210:211], v232 offset:22528\n"
    "ds_read_b64_tr_b16 v[212:213], v233 offset:18432\n"
    "ds_read_b64_tr_b16 v[214:215], v233 offset:22528\n"
    "buffer_load_dwordx4 v[8:11], v238, s[44:47], s85 offen offset:2048 sc1\n"
    "v_mfma_f32_32x32x16_bf16 v[128:143], v[192:195], v[32:35], 0\n"
    "s_waitcnt lgkmcnt(8)\n"
    "ds_read_b64_tr_b16 v[216:217], v232 offset:19456\n"
    "ds_read_b64_tr_b16 v[218:219], v232 offset:23552\n"
    "buffer_load_dwordx4 v[12:15], v238, s[44:47], s85 offen offset:3072 sc1\n"
    "v_mfma_f32_32x32x16_bf16 v[144:159], v[196:199], v[32:35], 0\n"
    "s_waitcnt vmcnt(10) lgkmcnt(8)\n"
    "ds_read_b64_tr_b16 v[220:221], v233 offset:19456\n"
    "ds_read_b64_tr_b16 v[222:223], v233 offset:23552\n"
    "ds_write_b128 v248, v[80:83] offset:0\n"
    "v_mfma_f32_32x32x16_bf16 v[128:143], v[200:203], v[36:39], v[128:143]\n"
    "s_waitcnt lgkmcnt(9)\n"
    "ds_write_b128 v248, v[84:87] offset:1024\n"
    "v_mfma_f32_32x32x16_bf16 v[144:159], v[204:207], v[36:39], v[144:159]\n"
    "s_waitcnt vmcnt(9) lgkmcnt(8)\n"
    "ds_write_b128 v248, v[88:91] offset:2048\n"
    "v_mfma_f32_32x32x16_bf16 v[128:143], v[208:211], v[40:43], v[128:143]\n"
    "ds_write_b128 v248, v[92:95] offset:3072\n"
    "s_waitcnt lgkmcnt(0)\n"
    "s_barrier\n"
    "v_mfma_f32_32x32x16_bf16 v[144:159], v[212:215], v[40:43], v[144:159]\n"
    "s_waitcnt vmcnt(8)\n"
    "ds_read_b128 v[160:163], v249 offset:0\n"
    "ds_read_b128 v[164:167], v249 offset:1024\n"
    "ds_read_b128 v[168:171], v249 offset:2048\n"
    "ds_read_b128 v[172:175], v249 offset:3072\n"
    "ds_read_b64_tr_b16 v[192:193], v232 offset:32768\n"
    "ds_read_b64_tr_b16 v[194:195], v232 offset:36864\n"
    "ds_read_b64_tr_b16 v[196:197], v233 offset:32768\n"
    "ds_read_b64_tr_b16 v[198:199], v233 offset:36864\n"
    "s_add_u32 s85, s87, 69632\n"
    "buffer_load_dwordx4 v[16:19], v238, s[44:47], s85 offen offset:0 sc1\n"
    "s_waitcnt lgkmcnt(7)\n"
    "v_add_f32 v64, v64, v160\n"
    "v_add_f32 v65, v65, v161\n"
    "v_add_f32 v66, v66, v162\n"
    "v_add_f32 v67, v67, v163\n"
    "s_waitcnt lgkmcnt(6)\n"
    "v_add_f32 v68, v68, v164\n"
    "v_mfma_f32_32x32x16_bf16 v[128:143], v[216:219], v[44:47], v[128:143]\n"
    "ds_read_b64_tr_b16 v[200:201], v232 offset:33792\n"
    "ds_read_b64_tr_b16 v[202:203], v232 offset:37888\n"
    "ds_read_b64_tr_b16 v[204:205], v233 offset:33792\n"
    "ds_read_b64_tr_b16 v[206:207], v233 offset:37888\n"
    "buffer_load_dwordx4 v[20:23], v238, s[44:47], s85 offen offset:1024 sc1\n"
    "v_add_f32 v69, v69, v165\n"
    "v_add_f32 v70, v70, v166\n"
    "v_add_f32 v71, v71, v167\n"
    "s_waitcnt lgkmcnt(9)\n"
    "v_add_f32 v72, v72, v168\n"
    "v_add_f32 v73, v73, v169\n"
    "v_mfma_f32_32x32x16_bf16 v[144:159], v[220:223], v[44:47], v[144:159]\n"
    "s_waitcnt vmcnt(9) lgkmcnt(6)\n"
    "ds_read_b64_tr_b16 v[208:209], v232 offset:34816\n"
    "ds_read_b64_tr_b16 v[210:211], v232 offset:38912\n"
    "ds_read_b64_tr_b16 v[212:213], v233 offset:34816\n"
    "ds_read_b64_tr_b16 v[214:215], v233 offset:38912\n"
    "buffer_load_dwordx4 v[24:27], v238, s[44:47], s85 offen offset:2048 sc1\n"
    "v_add_f32 v74, v74, v170\n"
    "v_add_f32 v75, v75, v171\n"
    "v_add_f32 v76, v76, v172\n"
    "v_add_f32 v77, v77, v173\n"
    "v_add_f32 v78, v78, v174\n"
    "v_mfma_f32_32x32x16_bf16 v[128:143], v[192:195], v[48:51], v[128:143]\n"
    "s_waitcnt lgkmcnt(8)\n"
    "ds_read_b64_tr_b16 v[216:217], v232 offset:35840\n"
    "ds_read_b64_tr_b16 v[218:219], v232 offset:39936\n"
    "buffer_load_dwordx4 v[28:31], v238, s[44:47], s85 offen offset:3072 sc1\n"
    "v_add_f32 v79, v79, v175\n"
    "v_mul_f32 v160, 0xbfb8aa3b, v64\n"
    "v_mul_f32 v161, 0xbfb8aa3b, v65\n"
    "v_mul_f32 v162, 0xbfb8aa3b, v66\n"
    "v_mul_f32 v163, 0xbfb8aa3b, v67\n"
    "v_mfma_f32_32x32x16_bf16 v[144:159], v[196:199], v[48:51], v[144:159]\n"
    "s_waitcnt vmcnt(10) lgkmcnt(8)\n"
    "ds_read_b64_tr_b16 v[220:221], v233 offset:35840\n"
    "ds_read_b64_tr_b16 v[222:223], v233 offset:39936\n"
    "v_mul_f32 v164, 0xbfb8aa3b, v68\n"
    "v_mul_f32 v165, 0xbfb8aa3b, v69\n"
    "v_mul_f32 v166, 0xbfb8aa3b, v70\n"
    "v_mul_f32 v167, 0xbfb8aa3b, v71\n"
    "v_mfma_f32_32x32x16_bf16 v[128:143], v[200:203], v[52:55], v[128:143]\n"
    "s_waitcnt lgkmcnt(8)\n"
    "v_mul_f32 v168, 0xbfb8aa3b, v72\n"
    "v_mul_f32 v169, 0xbfb8aa3b, v73\n"
    "v_mul_f32 v170, 0xbfb8aa3b, v74\n"
    "v_mul_f32 v171, 0xbfb8aa3b, v75\n"
    "v_mfma_f32_32x32x16_bf16 v[144:159], v[204:207], v[52:55], v[144:159]\n"
    "s_waitcnt vmcnt(9) lgkmcnt(6)\n"
    "v_mul_f32 v172, 0xbfb8aa3b, v76\n"
    "v_mul_f32 v173, 0xbfb8aa3b, v77\n"
    "v_mul_f32 v174, 0xbfb8aa3b, v78\n"
    "v_mul_f32 v175, 0xbfb8aa3b, v79\n"
    "v_mfma_f32_32x32x16_bf16 v[128:143], v[208:211], v[56:59], v[128:143]\n"
    "s_waitcnt lgkmcnt(0)\n"
    "s_barrier\n"
    "v_mfma_f32_32x32x16_bf16 v[144:159], v[212:215], v[56:59], v[144:159]\n"
    "s_waitcnt vmcnt(8)\n"
    "ds_read_b64_tr_b16 v[192:193], v232 offset:49152\n"
    "ds_read_b64_tr_b16 v[194:195], v232 offset:53248\n"
    "ds_read_b64_tr_b16 v[196:197], v233 offset:49152\n"
    "ds_read_b64_tr_b16 v[198:199], v233 offset:53248\n"
    "v_exp_f32 v160, v160\n"
    "v_exp_f32 v161, v161\n"
    "v_exp_f32 v162, v162\n"
    "v_exp_f32 v163, v163\n"
    "v_exp_f32 v164, v164\n"
    "v_mfma_f32_32x32x16_bf16 v[128:143], v[216:219], v[60:63], v[128:143]\n"
    "ds_read_b64_tr_b16 v[200:201], v232 offset:50176\n"
    "ds_read_b64_tr_b16 v[202:203], v232 offset:54272\n"
    "ds_read_b64_tr_b16 v[204:205], v233 offset:50176\n"
    "ds_read_b64_tr_b16 v[206:207], v233 offset:54272\n"
    "v_exp_f32 v165, v165\n"
    "v_exp_f32 v166, v166\n"
    "v_exp_f32 v167, v167\n"
    "v_exp_f32 v168, v168\n"
    "v_exp_f32 v169, v169\n"
    "v_mfma_f32_32x32x16_bf16 v[144:159], v[220:223], v[60:63], v[144:159]\n"
    "s_waitcnt vmcnt(7) lgkmcnt(6)\n"
    "ds_read_b64_tr_b16 v[208:209], v232 offset:51200\n"
    "ds_read_b64_tr_b16 v[210:211], v232 offset:55296\n"
    "ds_read_b64_tr_b16 v[212:213], v233 offset:51200\n"
    "ds_read_b64_tr_b16 v[214:215], v233 offset:55296\n"
    "v_exp_f32 v170, v170\n"
    "v_exp_f32 v171, v171\n"
    "v_exp_f32 v172, v172\n"
    "v_exp_f32 v173, v173\n"
    "v_exp_f32 v174, v174\n"
    "v_mfma_f32_32x32x16_bf16 v[128:143], v[192:195], v[0:3], v[128:143]\n"
    "s_waitcnt lgkmcnt(8)\n"
    "ds_read_b64_tr_b16 v[216:217], v232 offset:52224\n"
    "ds_read_b64_tr_b16 v[218:219], v232 offset:56320\n"
    "v_exp_f32 v175, v175\n"
    "v_add_f32 v160, 1.0, v160\n"
    "v_add_f32 v161, 1.0, v161\n"
    "v_add_f32 v162, 1.0, v162\n"
    "v_add_f32 v163, 1.0, v163\n"
    "v_mfma_f32_32x32x16_bf16 v[144:159], v[196:199], v[0:3], v[144:159]\n"
    "s_waitcnt vmcnt(6) lgkmcnt(8)\n"
    "ds_read_b64_tr_b16 v[220:221], v233 offset:52224\n"
    "ds_read_b64_tr_b16 v[222:223], v233 offset:56320\n"
    "v_add_f32 v164, 1.0, v164\n"
    "v_add_f32 v165, 1.0, v165\n"
    "v_add_f32 v166, 1.0, v166\n"
    "v_add_f32 v167, 1.0, v167\n"
    "v_mfma_f32_32x32x16_bf16 v[128:143], v[200:203], v[4:7], v[128:143]\n"
    "s_waitcnt lgkmcnt(8)\n"
    "v_add_f32 v168, 1.0, v168\n"
    "v_add_f32 v169, 1.0, v169\n"
    "v_add_f32 v170, 1.0, v170\n"
    "v_add_f32 v171, 1.0, v171\n"
    "v_mfma_f32_32x32x16_bf16 v[144:159], v[204:207], v[4:7], v[144:159]\n"
    "s_waitcnt vmcnt(5) lgkmcnt(6)\n"
    "v_add_f32 v172, 1.0, v172\n"
    "v_add_f32 v173, 1.0, v173\n"
    "v_add_f32 v174, 1.0, v174\n"
    "v_add_f32 v175, 1.0, v175\n"
    "v_mfma_f32_32x32x16_bf16 v[128:143], v[208:211], v[8:11], v[128:143]\n"
    "s_waitcnt lgkmcnt(0)\n"
    "s_barrier\n"
    "v_mfma_f32_32x32x16_bf16 v[144:159], v[212:215], v[8:11], v[144:159]\n"
    "s_waitcnt vmcnt(4)\n"
    "ds_read_b64_tr_b16 v[192:193], v235 offset:0\n"
    "ds_read_b64_tr_b16 v[194:195], v235 offset:4096\n"
    "ds_read_b64_tr_b16 v[196:197], v235 offset:1024\n"
    "ds_read_b64_tr_b16 v[198:199], v235 offset:5120\n"
    "v_rcp_f32 v160, v160\n"
    "v_rcp_f32 v161, v161\n"
    "v_rcp_f32 v162, v162\n"
    "v_rcp_f32 v163, v163\n"
    "v_rcp_f32 v164, v164\n"
    "v_mfma_f32_32x32x16_bf16 v[128:143], v[216:219], v[12:15], v[128:143]\n"
    "ds_read_b64_tr_b16 v[200:201], v235 offset:2048\n"
    "ds_read_b64_tr_b16 v[202:203], v235 offset:6144\n"
    "ds_read_b64_tr_b16 v[204:205], v235 offset:3072\n"
    "ds_read_b64_tr_b16 v[206:207], v235 offset:7168\n"
    "v_rcp_f32 v165, v165\n"
    "v_rcp_f32 v166, v166\n"
    "v_rcp_f32 v167, v167\n"
    "v_rcp_f32 v168, v168\n"
    "v_rcp_f32 v169, v169\n"
    "v_mfma_f32_32x32x16_bf16 v[144:159], v[220:223], v[12:15], v[144:159]\n"
    "s_waitcnt vmcnt(3) lgkmcnt(6)\n"
    "ds_read_b64_tr_b16 v[208:209], v234 offset:0\n"
    "ds_read_b64_tr_b16 v[210:211], v234 offset:4096\n"
    "ds_read_b64_tr_b16 v[212:213], v234 offset:1024\n"
    "ds_read_b64_tr_b16 v[214:215], v234 offset:5120\n"
    "v_rcp_f32 v170, v170\n"
    "v_rcp_f32 v171, v171\n"
    "v_rcp_f32 v172, v172\n"
    "v_rcp_f32 v173, v173\n"
    "v_rcp_f32 v174, v174\n"
    "v_mfma_f32_32x32x16_bf16 v[144:159], v[192:195], v[16:19], v[144:159]\n"
    "s_waitcnt vmcnt(2) lgkmcnt(8)\n"
    "ds_read_b64_tr_b16 v[216:217], v234 offset:2048\n"
    "ds_read_b64_tr_b16 v[218:219], v234 offset:6144\n"
    "v_rcp_f32 v175, v175\n"
    "v_mul_f32 v64, v64, v160\n"
    "v_mul_f32 v65, v65, v161\n"
    "v_mul_f32 v66, v66, v162\n"
    "v_mul_f32 v67, v67, v163\n"
    "v_mfma_f32_32x32x16_bf16 v[144:159], v[196:199], v[20:23], v[144:159]\n"
    "s_waitcnt vmcnt(1) lgkmcnt(8)\n"
    "ds_read_b64_tr_b16 v[220:221], v234 offset:3072\n"
    "ds_read_b64_tr_b16 v[222:223], v234 offset:7168\n"
    "v_mul_f32 v68, v68, v164\n"
    "v_mul_f32 v69, v69, v165\n"
    "v_mul_f32 v70, v70, v166\n"
    "v_mul_f32 v71, v71, v167\n"
    "v_mfma_f32_32x32x16_bf16 v[144:159], v[200:203], v[24:27], v[144:159]\n"
    "s_waitcnt vmcnt(0) lgkmcnt(8)\n"
    "v_mul_f32 v72, v72, v168\n"
    "v_mul_f32 v73, v73, v169\n"
    "v_mul_f32 v74, v74, v170\n"
    "v_mul_f32 v75, v75, v171\n"
    "v_mfma_f32_32x32x16_bf16 v[144:159], v[204:207], v[28:31], v[144:159]\n"
    "s_waitcnt lgkmcnt(6)\n"
    "v_mul_f32 v76, v76, v172\n"
    "v_mul_f32 v77, v77, v173\n"
    "v_mul_f32 v78, v78, v174\n"
    "v_mul_f32 v79, v79, v175\n"
    "v_mfma_f32_32x32x16_bf16 v[128:143], v[208:211], v[16:19], v[128:143]\n"
    "s_waitcnt lgkmcnt(4)\n"
    "v_mfma_f32_32x32x16_bf16 v[128:143], v[212:215], v[20:23], v[128:143]\n"
    "s_waitcnt lgkmcnt(2)\n"
    "s_nop 2\n"
    "ds_write_b128 v252, v[144:147] offset:0\n"
    "ds_write_b128 v252, v[148:151] offset:1024\n"
    "v_mfma_f32_32x32x16_bf16 v[128:143], v[216:219], v[24:27], v[128:143]\n"
    "s_waitcnt lgkmcnt(2)\n"
    "ds_write_b128 v252, v[152:155] offset:2048\n"
    "ds_write_b128 v252, v[156:159] offset:3072\n"
    "v_mfma_f32_32x32x16_bf16 v[128:143], v[220:223], v[28:31], v[128:143]\n"
    "s_waitcnt lgkmcnt(0)\n"
    "s_barrier\n"
    "ds_read_b128 v[160:163], v253 offset:0\n"
    "ds_read_b128 v[164:167], v253 offset:1024\n"
    "ds_read_b128 v[168:171], v253 offset:2048\n"
    "ds_read_b128 v[172:175], v253 offset:3072\n"
    "s_nop 5\n"
    "v_fma_f32 v176, v128, v64, v176\n"
    "v_fma_f32 v177, v129, v65, v177\n"
    "v_fma_f32 v178, v130, v66, v178\n"
    "v_fma_f32 v179, v131, v67, v179\n"
    "v_fma_f32 v180, v132, v68, v180\n"
    "v_fma_f32 v181, v133, v69, v181\n"
    "v_fma_f32 v182, v134, v70, v182\n"
    "v_fma_f32 v183, v135, v71, v183\n"
    "v_fma_f32 v184, v136, v72, v184\n"
    "v_fma_f32 v185, v137, v73, v185\n"
    "v_fma_f32 v186, v138, v74, v186\n"
    "v_fma_f32 v187, v139, v75, v187\n"
    "v_fma_f32 v188, v140, v76, v188\n"
    "v_fma_f32 v189, v141, v77, v189\n"
    "v_fma_f32 v190, v142, v78, v190\n"
    "v_fma_f32 v191, v143, v79, v191\n"
    "s_waitcnt lgkmcnt(3)\n"
    "v_fma_f32 v176, v160, v64, v176\n"
    "v_fma_f32 v177, v161, v65, v177\n"
    "v_fma_f32 v178, v162, v66, v178\n"
    "v_fma_f32 v179, v163, v67, v179\n"
    "s_waitcnt lgkmcnt(2)\n"
    "v_fma_f32 v180, v164, v68, v180\n"
    "v_fma_f32 v181, v165, v69, v181\n"
    "v_fma_f32 v182, v166, v70, v182\n"
    "v_fma_f32 v183, v167, v71, v183\n"
    "s_waitcnt lgkmcnt(1)\n"
    "v_fma_f32 v184, v168, v72, v184\n"
    "v_fma_f32 v185, v169, v73, v185\n"
    "v_fma_f32 v186, v170, v74, v186\n"
    "v_fma_f32 v187, v171, v75, v187\n"
    "s_waitcnt lgkmcnt(0)\n"
    "v_fma_f32 v188, v172, v76, v188\n"
    "v_fma_f32 v189, v173, v77, v189\n"
    "v_fma_f32 v190, v174, v78, v190\n"
    "v_fma_f32 v191, v175, v79, v191\n"
    "v_cvt_pk_bf16_f32 v160, v176, v177\n"
    "v_cvt_pk_bf16_f32 v161, v178, v179\n"
    "v_cvt_pk_bf16_f32 v162, v180, v181\n"
    "v_cvt_pk_bf16_f32 v163, v182, v183\n"
    "v_cvt_pk_bf16_f32 v164, v184, v185\n"
    "v_cvt_pk_bf16_f32 v165, v186, v187\n"
    "v_cvt_pk_bf16_f32 v166, v188, v189\n"
    "v_cvt_pk_bf16_f32 v167, v190, v191\n"
    "v_permlane32_swap_b32 v160, v162\n"
    "v_permlane32_swap_b32 v161, v163\n"
    "v_permlane32_swap_b32 v164, v166\n"
    "v_permlane32_swap_b32 v165, v167\n"
    "buffer_store_dwordx4 v[160:163], v96, s[52:55], s84 offen\n"
    "buffer_store_dwordx4 v[164:167], v96, s[52:55], s84 offen offset:32\n"
    "s_endpgm\n"
    "KEND:\n"
    : "+v"(tid)
    : "s"(__builtin_amdgcn_kernarg_segment_ptr()), "s"(blockIdx.x), "s"((unsigned)(size_t)(__attribute__((address_space(3))) char*)smem)
    : "v1", "v2", "v3", "v4", "v5", "v6", "v7", "v8", "v9", "v10", "v11", "v12", "v13", "v14", "v15", "v16", "v17", "v18", "v19", "v20", "v21", "v22", "v23", "v24", "v25", "v26", "v27", "v28", "v29", "v30", "v31", "v32", "v33", "v34", "v35", "v36", "v37", "v38", "v39", "v40", "v41", "v42", "v43", "v44", "v45", "v46", "v47", "v48", "v49", "v50", "v51", "v52", "v53", "v54", "v55", "v56", "v57", "v58", "v59", "v60", "v61", "v62", "v63", "v64", "v65", "v66", "v67", "v68", "v69", "v70", "v71", "v72", "v73", "v74", "v75", "v76", "v77", "v78", "v79", "v80", "v81", "v82", "v83", "v84", "v85", "v86", "v87", "v88", "v89", "v90", "v91", "v92", "v93", "v94", "v95", "v96", "v97", "v98", "v99", "v100", "v101", "v102", "v103", "v104", "v105", "v106", "v107", "v108", "v109", "v110", "v111", "v112", "v113", "v114", "v115", "v116", "v117", "v118", "v119", "v120", "v121", "v122", "v123", "v124", "v125", "v126", "v127", "v128", "v129", "v130", "v131", "v132", "v133", "v134", "v135", "v136", "v137", "v138", "v139", "v140", "v141", "v142", "v143", "v144", "v145", "v146", "v147", "v148", "v149", "v150", "v151", "v152", "v153", "v154", "v155", "v156", "v157", "v158", "v159", "v160", "v161", "v162", "v163", "v164", "v165", "v166", "v167", "v168", "v169", "v170", "v171", "v172", "v173", "v174", "v175", "v176", "v177", "v178", "v179", "v180", "v181", "v182", "v183", "v184", "v185", "v186", "v187", "v188", "v189", "v190", "v191", "v192", "v193", "v194", "v195", "v196", "v197", "v198", "v199", "v200", "v201", "v202", "v203", "v204", "v205", "v206", "v207", "v208", "v209", "v210", "v211", "v212", "v213", "v214", "v215", "v216", "v217", "v218", "v219", "v220", "v221", "v222", "v223", "v224", "v225", "v226", "v227", "v228", "v229", "v230", "v231", "v232", "v233", "v234", "v235", "v236", "v237", "v238", "v239", "v240", "v241", "v242", "v243", "v244", "v245", "v246", "v247", "v248", "v249", "v250", "v251", "v252", "v253", "v254", "v255", "s8", "s9", "s10", "s11", "s12", "s13", "s14", "s15", "s16", "s17", "s18", "s19", "s20", "s21", "s22", "s23", "s24", "s25", "s26", "s27", "s28", "s29", "s30", "s31", "s32", "s33", "s34", "s35", "s36", "s37", "s38", "s39", "s40", "s41", "s42", "s43", "s44", "s45", "s46", "s47", "s48", "s49", "s50", "s51", "s52", "s53", "s54", "s55", "s56", "s57", "s58", "s59", "s60", "s61", "s62", "s63", "s64", "s65", "s66", "s67", "s68", "s69", "s70", "s71", "s72", "s73", "s74", "s75", "s76", "s77", "s78", "s79", "s80", "s81", "s82", "s83", "s84", "s85", "s86", "s87", "s88", "s89", "s90", "s91", "s92", "s93", "s94", "s95", "s96", "s97", "s98", "s99", "s100", "s101", "vcc", "scc", "memory");
}

extern "C" void kernel_launch(void* const* d_in, const int* in_sizes, int n_in,
                              void* d_out, int out_size, void* d_ws, size_t ws_size,
                              hipStream_t stream) {
  fused_block_kernel<<<256, 512, 0, stream>>>(d_in[0], d_in[1], d_in[2], d_in[4], d_in[5], d_in[6], d_in[7], d_out, d_ws);
}
```
